```python
import torch

def _softmax(x, axis=-1):
    return torch.softmax(x, dim=axis)

def _gelu(x):
    return torch.nn.functional.gelu(x, approximate='tanh')

def _silu(x):
    return torch.nn.functional.silu(x)

def _sigmoid(x):
    return torch.sigmoid(x)

def _layernorm(x, axis=-1, eps=1e-05):
    m = x.mean(dim=axis, keepdim=True)
    v = x.var(dim=axis, keepdim=True, unbiased=False)
    return (x - m) / torch.sqrt(v + eps)

def _scatter_add(tgt, idx, src, axis=0):
    return tgt.clone().index_add_(axis, idx, src)

def _segment_sum(src, seg, n):
    out = torch.zeros((n,) + src.shape[1:], dtype=src.dtype)
    return out.index_add_(0, seg, src)

def _pad(x, pw):
    flat = tuple((v for pair in reversed(pw) for v in pair))
    return torch.nn.functional.pad(x, flat)
kernel_name = 'interleave_layers__spectral_fnet__mixer_gmlp__prompt'

def setup_inputs():

    _w = lambda a: (lambda t: t.to(torch.bfloat16) if t.is_floating_point() else t)(torch.as_tensor(a))

    def _A_setup():
        B, S, D = (1, 2048, 1024)
        ns = torch.arange(S)[:, None] * torch.arange(S)[None, :]
        nd = torch.arange(D)[:, None] * torch.arange(D)[None, :]
        Ws_re = torch.cos(-2.0 * torch.pi * ns / S).to(float)
        Ws_im = torch.sin(-2.0 * torch.pi * ns / S).to(float)
        Wd_re = torch.cos(-2.0 * torch.pi * nd / D).to(float)
        Wd_im = torch.sin(-2.0 * torch.pi * nd / D).to(float)
        return {'x': _w((lambda *s: torch.randn(*s, dtype=torch.bfloat16) * 0.05)(B, S, D)), 'dft_seq_re': _w(Ws_re), 'dft_seq_im': _w(Ws_im), 'dft_hid_re': _w(Wd_re), 'dft_hid_im': _w(Wd_im)}
    _A_in = _A_setup()

    def _B_setup():
        B, S, D, F = (1, 2048, 1024, 3584)
        Fh = F // 2
        return {'x': _w((lambda *s: torch.randn(*s, dtype=torch.bfloat16) * 0.05)(B, S, D)), 'w_in': _w((lambda *s: torch.randn(*s, dtype=torch.bfloat16) * 1.6)(D, 2 * Fh)), 'w_sp': _w(0.01 * (lambda *s: torch.randn(*s, dtype=torch.bfloat16) * 1.6)(S, S)), 'b_sp': _w(1.0 + 0.01 * (lambda *s: torch.randn(*s, dtype=torch.bfloat16) * 1.6)(S)), 'w_out': _w((lambda *s: torch.randn(*s, dtype=torch.bfloat16) * 1.6)(Fh, D))}
    _B_in = _B_setup()
    return {'A_x': _A_in['x'], 'A_dft_seq_re': _A_in['dft_seq_re'], 'A_dft_seq_im': _A_in['dft_seq_im'], 'A_dft_hid_re': _A_in['dft_hid_re'], 'A_dft_hid_im': _A_in['dft_hid_im'], 'B_x': _B_in['x'], 'B_w_in': _B_in['w_in'], 'B_w_sp': _B_in['w_sp'], 'B_b_sp': _B_in['b_sp'], 'B_w_out': _B_in['w_out']}

def reference(A_x, A_dft_seq_re, A_dft_seq_im, A_dft_hid_re, A_dft_hid_im, B_x, B_w_in, B_w_sp, B_b_sp, B_w_out):

    def _A_ref(x, dft_seq_re, dft_seq_im, dft_hid_re, dft_hid_im):
        a = torch.einsum('bsd,ed->bse', x, dft_hid_re)
        b = torch.einsum('bsd,ed->bse', x, dft_hid_im)
        y = torch.einsum('ts,bse->bte', dft_seq_re, a) - torch.einsum('ts,bse->bte', dft_seq_im, b)
        return y

    def _B_ref(x, w_in, w_sp, b_sp, w_out):
        B, S, D = x.shape
        F = w_in.shape[-1]
        Fh = F // 2
        z = _gelu(torch.einsum('bsd,df->bsf', x, w_in))
        z1 = z[..., :Fh].clone()
        z2 = _layernorm(z[..., Fh:], axis=-1)
        g = torch.einsum('ts,bsf->btf', w_sp, z2) + b_sp[None, :, None]
        return torch.einsum('bsf,fd->bsd', z1 * g, w_out)
    x = A_x
    x = x + _A_ref(_layernorm(x, axis=-1), A_dft_seq_re, A_dft_seq_im, A_dft_hid_re, A_dft_hid_im)
    x = x + _B_ref(_layernorm(x, axis=-1), B_w_in, B_w_sp, B_b_sp, B_w_out)
    x = x + _A_ref(_layernorm(x, axis=-1), A_dft_seq_re, A_dft_seq_im, A_dft_hid_re, A_dft_hid_im)
    x = x + _B_ref(_layernorm(x, axis=-1), B_w_in, B_w_sp, B_b_sp, B_w_out)
    return x
```

```cpp
#include <hip/hip_runtime.h>
#include <hip/hip_bf16.h>

typedef unsigned short u16;
typedef short bf16x8 __attribute__((ext_vector_type(8)));
typedef short s16x4 __attribute__((ext_vector_type(4)));
typedef float f32x2 __attribute__((ext_vector_type(2)));
typedef float f32x4 __attribute__((ext_vector_type(4)));
typedef unsigned u32x2 __attribute__((ext_vector_type(2)));
typedef unsigned u32x4 __attribute__((ext_vector_type(4)));
#define LAS __attribute__((address_space(3)))
#define DEV __device__ __forceinline__

constexpr int S_ = 2048, D_ = 1024, F_ = 3584, FH = 1792;
constexpr float LN_EPS = 1e-5f;
constexpr int STORE_AUX = 16;

constexpr int waitimm(int vm, int lgkm) { return (vm & 15) | (7 << 4) | ((lgkm & 15) << 8) | ((vm >> 4) << 14); }
#define WAITCNT(vm, lgkm) __builtin_amdgcn_s_waitcnt(waitimm(vm, lgkm))
#define BAR() do { asm volatile("" ::: "memory"); __builtin_amdgcn_s_barrier(); asm volatile("" ::: "memory"); } while (0)
#define SB0() __builtin_amdgcn_sched_barrier(0)

DEV unsigned pk_bf16(float lo, float hi) { unsigned r; asm("v_cvt_pk_bf16_f32 %0, %1, %2" : "=v"(r) : "v"(lo), "v"(hi)); return r; }
DEV float bf_lo(unsigned v) { return __uint_as_float(v << 16); }
DEV float bf_hi(unsigned v) { return __uint_as_float(v & 0xffff0000u); }
DEV __amdgpu_buffer_rsrc_t rsrc(const void* p, unsigned bytes) { return __builtin_amdgcn_make_buffer_rsrc((void*)p, 0, bytes, 0x00020000); }
DEV float gelu_tanh(float x) {
    const float c0 = -2.3022081986f, c1 = c0 * 0.044715f;
    const float e = __builtin_amdgcn_exp2f(x * __builtin_fmaf(x * x, c1, c0));
    return x * __builtin_amdgcn_rcpf(1.0f + e);
}

template <int BM, int BN, int NS, int NL, int LDP, int LDQ, int NT, int KSW>
DEV void loader_loop(LAS unsigned char* lds, const u16* P0, const u16* P1, const u16* Q, int lw, int lane) {
    constexpr int NPP = BM / 8, NPQ = BN / 8, LP = NPP / NL, LQ = (NPQ + NL - 1) / NL, LQMIN = NPQ / NL, LMIN = LP + LQMIN, STAGE = (BM + BN) * 128;
    static_assert(NPP % NL == 0 && NL % 2 == 0 && LMIN * (NS - 2) <= 63 && NS >= 3 && NS <= 5, "");
    const __amdgpu_buffer_rsrc_t rP0 = rsrc(P0, BM * LDP * 2), rP1 = rsrc(P1, BM * LDP * 2), rQ = rsrc(Q, BN * LDQ * 2);
    const int ch = ((lane & 7) ^ (((lw & 1) * 4 + (lane >> 4)) & 7)) * 16, row = lw * 8 + (lane >> 3);
    const int vP = row * LDP * 2 + ch, vQ = row * LDQ * 2 + ch;
    auto stage = [&](int slot, int kt) {
        LAS unsigned char* dst = lds + slot * STAGE + lw * 1024;
        const bool second = KSW && kt >= KSW;
        const __amdgpu_buffer_rsrc_t rP = second ? rP1 : rP0;
        const int kp = (second ? kt - KSW : kt) * 128;
#pragma unroll
        for (int i = 0; i < LP; ++i) __builtin_amdgcn_raw_ptr_buffer_load_lds(rP, (LAS void*)(dst + i * NL * 1024), 16, vP, i * NL * 8 * LDP * 2 + kp, 0, 0);
#pragma unroll
        for (int j = 0; j < LQ; ++j)
            if (j < LQMIN || j * NL + lw < NPQ) __builtin_amdgcn_raw_ptr_buffer_load_lds(rQ, (LAS void*)(dst + (NPP + j * NL) * 1024), 16, vQ, j * NL * 8 * LDQ * 2 + kt * 128, 0, 0);
    };
    stage(0, 0); stage(1, 1);
    WAITCNT(LMIN, 15);
    BAR();
#pragma unroll
    for (int s = 2; s < NS; ++s) stage(s, s);
    int slot = 0;
#pragma unroll 1
    for (int t = 0; t < NT - NS; ++t) {
        WAITCNT(LMIN * (NS - 2), 15);
        BAR();
        stage(slot, t + NS);
        slot = (slot + 1 == NS) ? 0 : slot + 1;
    }
    if constexpr (NS >= 5) { WAITCNT(LMIN * 3, 15); BAR(); }
    if constexpr (NS >= 4) { WAITCNT(LMIN * 2, 15); BAR(); }
    WAITCNT(LMIN, 15); BAR();
    WAITCNT(0, 15); BAR();
}

template <int BM, int BN, int NS, int NT, int MT, int NTL, bool SWAP>
DEV void compute_loop(LAS unsigned char* lds, int prow0, int qrow0, int lane, f32x4 (&acc)[MT][NTL]) {
    constexpr int STAGE = (BM + BN) * 128, NR = MT + NTL, NM = MT * NTL;
    static_assert(NR <= NM, "");
    const int fr = lane & 15, fq = lane >> 4;
    int aoff[2], boff[2];
#pragma unroll
    for (int k = 0; k < 2; ++k) {
        const int ch = ((k * 4 + fq) ^ ((fr >> 1) & 7)) * 16;
        aoff[k] = (prow0 + fr) * 128 + ch;
        boff[k] = (BM + qrow0 + fr) * 128 + ch;
    }
#pragma unroll
    for (int m = 0; m < MT; ++m)
#pragma unroll
        for (int n = 0; n < NTL; ++n) acc[m][n] = (f32x4){0.f, 0.f, 0.f, 0.f};
    bf16x8 fa[2][MT], fb[2][NTL];
    auto sub = [&](int set, int rslot, int k, bool rd) {
        const LAS unsigned char* b = lds + rslot * STAGE;
#pragma unroll
        for (int i = 0; i < NM; ++i) {
            const int m = i / NTL, n = i % NTL;
            acc[m][n] = SWAP ? __builtin_amdgcn_mfma_f32_16x16x32_bf16(fa[set][m], fb[set][n], acc[m][n], 0, 0, 0)
                             : __builtin_amdgcn_mfma_f32_16x16x32_bf16(fb[set][n], fa[set][m], acc[m][n], 0, 0, 0);
            SB0();
            if (rd && i < NR) {
                if (i == 0) fa[set ^ 1][0] = *(const LAS bf16x8*)(b + aoff[k]);
                else if (i <= NTL) fb[set ^ 1][i - 1] = *(const LAS bf16x8*)(b + boff[k] + (i - 1) * 2048);
                else fa[set ^ 1][i - NTL] = *(const LAS bf16x8*)(b + aoff[k] + (i - NTL) * 2048);
                SB0();
            }
        }
    };
    BAR();
#pragma unroll
    for (int n = 0; n < NTL; ++n) fb[0][n] = *(const LAS bf16x8*)(lds + boff[0] + n * 2048);
#pragma unroll
    for (int m = 0; m < MT; ++m) fa[0][m] = *(const LAS bf16x8*)(lds + aoff[0] + m * 2048);
    int slot = 0;
    for (int t = 0; t < NT - 1; ++t) {
        const int nslot = (slot + 1 == NS) ? 0 : slot + 1;
        SB0(); sub(0, slot, 1, true);
        WAITCNT(63, 0);
        BAR(); SB0();
        sub(1, nslot, 0, true);
        slot = nslot;
    }
    SB0(); sub(0, slot, 1, true); sub(1, 0, 0, false);
    WAITCNT(63, 0);
}

DEV void tile_of_block(int& tm, int& tn) {
    const int x = blockIdx.x & 7, idx = blockIdx.x >> 3;
    tm = (x >> 1) * 4 + (idx >> 3); tn = (x & 1) * 8 + (idx & 7);
}

DEV void row_tables(const float* sp, int row0, int tid, LAS float* R, LAS float* M, float sign) {
    f32x4 v[4];
#pragma unroll
    for (int i = 0; i < 4; ++i) v[i] = *(const f32x4*)(sp + (size_t)(row0 + i * 32 + (tid >> 3)) * 32 + (tid & 7) * 4);
#pragma unroll
    for (int i = 0; i < 4; ++i) {
        float s = v[i][0] + v[i][2], q = v[i][1] + v[i][3];
#pragma unroll
        for (int o = 1; o < 8; o <<= 1) { s += __shfl_xor(s, o); q += __shfl_xor(q, o); }
        const float mean = s * (1.0f / D_), var = fmaxf(q * (1.0f / D_) - mean * mean, 0.f), r = sign * __builtin_amdgcn_rsqf(var + LN_EPS);
        if ((tid & 7) == 0) { R[i * 32 + (tid >> 3)] = r; M[i * 32 + (tid >> 3)] = -mean * r; }
    }
}

constexpr int G1_NS = 4, G1_LDS = G1_NS * 256 * 128 + 1024;
__global__ __launch_bounds__(512, 2) void k_g1(const u16* __restrict__ wre, const u16* __restrict__ wim, const u16* __restrict__ x, const float* __restrict__ sp,
                                               const float* __restrict__ wsum, u16* __restrict__ T) {
    extern __shared__ __attribute__((aligned(1024))) unsigned char shm[];
    LAS unsigned char* lds = (LAS unsigned char*)shm;
    const int tid = threadIdx.x, lane = tid & 63, w = __builtin_amdgcn_readfirstlane(tid >> 6);
    int tm, tn; tile_of_block(tm, tn);
    constexpr int RS = 272;
    if (w >= 4) {
        const u16* P = (tm < 8 ? wre : wim) + (size_t)(tm & 7) * 128 * D_;
        loader_loop<128, 128, G1_NS, 4, D_, D_, D_ / 64, 0>(lds, P, P, x + (size_t)tn * 128 * D_, w - 4, lane);
        BAR();
    } else {
        LAS float* R = (LAS float*)(lds + G1_NS * 256 * 128); LAS float* M = R + 128;
        const int wm = w >> 1, wn = w & 1, fr = lane & 15, fq = lane >> 4;
        float ws[4];
#pragma unroll
        for (int m = 0; m < 4; ++m) ws[m] = wsum[tm * 128 + wm * 64 + m * 16 + fr];
        row_tables(sp, tn * 128, tid, R, M, tm < 8 ? 1.0f : -1.0f);
        WAITCNT(63, 0);
        f32x4 acc[4][4];
        compute_loop<128, 128, G1_NS, D_ / 64, 4, 4, false>(lds, wm * 64, wn * 64, lane, acc);
        BAR();
#pragma unroll
        for (int n = 0; n < 4; ++n) {
            const f32x4 r = *(const LAS f32x4*)(R + wn * 64 + n * 16 + fq * 4), mr = *(const LAS f32x4*)(M + wn * 64 + n * 16 + fq * 4);
#pragma unroll
            for (int m = 0; m < 4; ++m) {
                const f32x4 v = acc[m][n] * r + mr * ws[m];
                *(LAS u32x2*)(lds + (wm * 64 + m * 16 + fr) * RS + (wn * 64 + n * 16 + fq * 4) * 2) = (u32x2){pk_bf16(v[0], v[1]), pk_bf16(v[2], v[3])};
            }
        }
        WAITCNT(63, 0);
    }
    BAR();
    const __amdgpu_buffer_rsrc_t rC = rsrc(T + (size_t)(tm & 7) * 128 * 4096 + (tm < 8 ? 0 : 2048) + tn * 128, 128 * 4096 * 2 - (2048 + tn * 128) * 2);
#pragma unroll
    for (int i = 0; i < 4; ++i) {
        const int r = i * 32 + (tid >> 4), c = tid & 15;
        __builtin_amdgcn_raw_buffer_store_b128(*(const LAS u32x4*)(lds + r * RS + c * 16), rC, r * 4096 * 2 + c * 16, 0, STORE_AUX);
    }
}

constexpr int GR_NS = 4, GR_LDS = GR_NS * 192 * 128;
template <int LDP, int LDQ, int NT, int KSW, bool STATS>
__global__ __launch_bounds__(512, 2) void k_gres(const u16* __restrict__ p0, const u16* __restrict__ p1, const u16* __restrict__ q, const u16* __restrict__ res,
                                                 u16* __restrict__ out, float* __restrict__ sp) {
    extern __shared__ __attribute__((aligned(1024))) unsigned char shm[];
    LAS unsigned char* lds = (LAS unsigned char*)shm;
    const int tid = threadIdx.x, lane = tid & 63, w = __builtin_amdgcn_readfirstlane(tid >> 6);
    int tm, tn; tile_of_block(tm, tn);
    constexpr int RS = 144;
    u32x4 xr[2];
#pragma unroll
    for (int i = 0; i < 2; ++i) xr[i] = *(const u32x4*)(res + (size_t)(tm * 128 + i * 64 + (tid >> 3)) * D_ + tn * 64 + (tid & 7) * 8);
    if (w >= 4) {
        loader_loop<128, 64, GR_NS, 4, LDP, LDQ, NT, KSW>(lds, p0 + (size_t)tm * 128 * LDP, p1 + (size_t)tm * 128 * LDP, q + (size_t)tn * 64 * LDQ, w - 4, lane);
        BAR();
    } else {
        const int wm = w >> 1, wn = w & 1, fr = lane & 15, fq = lane >> 4;
        f32x4 acc[4][2];
        compute_loop<128, 64, GR_NS, NT, 4, 2, false>(lds, wm * 64, wn * 32, lane, acc);
        BAR();
#pragma unroll
        for (int m = 0; m < 4; ++m)
#pragma unroll
            for (int n = 0; n < 2; ++n)
                *(LAS u32x2*)(lds + (wm * 64 + m * 16 + fr) * RS + (wn * 32 + n * 16 + fq * 4) * 2) = (u32x2){pk_bf16(acc[m][n][0], acc[m][n][1]), pk_bf16(acc[m][n][2], acc[m][n][3])};
        WAITCNT(63, 0);
    }
    BAR();
    const __amdgpu_buffer_rsrc_t rC = rsrc(out + (size_t)tm * 128 * D_ + tn * 64, 128 * D_ * 2 - tn * 128);
#pragma unroll
    for (int i = 0; i < 2; ++i) {
        const int r = i * 64 + (tid >> 3), c = tid & 7;
        const u32x4 y = *(const LAS u32x4*)(lds + r * RS + c * 16);
        u32x4 o; float s = 0.f, qq = 0.f;
#pragma unroll
        for (int j = 0; j < 4; ++j) {
            o[j] = pk_bf16(bf_lo(xr[i][j]) + bf_lo(y[j]), bf_hi(xr[i][j]) + bf_hi(y[j]));
            const float a = bf_lo(o[j]), b = bf_hi(o[j]);
            s += a + b; qq += a * a + b * b;
        }
        __builtin_amdgcn_raw_buffer_store_b128(o, rC, r * D_ * 2 + c * 16, 0, STORE_AUX);
        if (STATS) {
#pragma unroll
            for (int of = 1; of < 8; of <<= 1) { s += __shfl_xor(s, of); qq += __shfl_xor(qq, of); }
            if (c == 0) *(f32x2*)(sp + (size_t)(tm * 128 + r) * 32 + tn * 2) = (f32x2){s, qq};
        }
    }
}

constexpr int G3_NS = 3, G3_RING = G3_NS * 352 * 128, G3_LDS = G3_RING + 1024 + 1024 + 1024;
__global__ __launch_bounds__(768, 3) void k_g3(const u16* __restrict__ x, const u16* __restrict__ wt, const float* __restrict__ sp, const float* __restrict__ wsp,
                                               u16* __restrict__ z1, u16* __restrict__ z2t, unsigned* xch, unsigned tag) {
    extern __shared__ __attribute__((aligned(1024))) unsigned char shm[];
    LAS unsigned char* lds = (LAS unsigned char*)shm;
    const int tid = threadIdx.x, lane = tid & 63, w = __builtin_amdgcn_readfirstlane(tid >> 6);
    int tm, tn; tile_of_block(tm, tn);
    constexpr int RS = 272;
    if (w >= 8) {
        loader_loop<128, 224, G3_NS, 4, D_, D_, D_ / 64, 0>(lds, x + (size_t)tm * 128 * D_, x, wt + (size_t)tn * 224 * D_, w - 8, lane);
        BAR();
    } else {
        LAS float* R = (LAS float*)(lds + G3_RING); LAS float* M = R + 128; LAS float* WS = R + 256; LAS f32x2* Z = (LAS f32x2*)(R + 512);
        const int wm = w & 3, fr = lane & 15, fq = lane >> 4;
        if (w < 4) row_tables(sp, tm * 128, tid, R, M, 1.0f);
        else {
            f32x4 v[4];
#pragma unroll
            for (int i = 0; i < 4; ++i) { const int c = i * 256 + tid - 256; v[i] = *(const f32x4*)(wsp + (size_t)tn * 224 * 16 + (c < 896 ? c : 0) * 4); }
#pragma unroll
            for (int i = 0; i < 4; ++i) {
                const int c = i * 256 + tid - 256;
                float s = (v[i][0] + v[i][1]) + (v[i][2] + v[i][3]);
                s += __shfl_xor(s, 1); s += __shfl_xor(s, 2);
                if ((c & 3) == 0 && c < 896) WS[c >> 2] = s;
            }
        }
        WAITCNT(63, 0);
        f32x4 acc[2][7];
        if (w < 4) {
            compute_loop<128, 224, G3_NS, D_ / 64, 2, 7, false>(lds, wm * 32, 0, lane, acc);
            BAR();
            float r[2], mr[2];
#pragma unroll
            for (int m = 0; m < 2; ++m) { r[m] = R[wm * 32 + m * 16 + fr]; mr[m] = M[wm * 32 + m * 16 + fr]; }
            const __amdgpu_buffer_rsrc_t rZ = rsrc(z1 + (size_t)((tm * 16 + tn) * 4 + wm) * 3584, 7168);
#pragma unroll
            for (int n = 0; n < 7; ++n) {
                const f32x4 ws = *(const LAS f32x4*)(WS + n * 16 + fq * 4);
                u32x4 o;
#pragma unroll
                for (int m = 0; m < 2; ++m) {
                    const f32x4 v = acc[m][n] * r[m] + ws * mr[m];
                    o[m * 2] = pk_bf16(gelu_tanh(v[0]), gelu_tanh(v[1])); o[m * 2 + 1] = pk_bf16(gelu_tanh(v[2]), gelu_tanh(v[3]));
                }
                __builtin_amdgcn_raw_buffer_store_b128(o, rZ, n * 1024 + lane * 16, 0, STORE_AUX);
            }
        } else {
            compute_loop<128, 224, G3_NS, D_ / 64, 2, 7, true>(lds, wm * 32, 112, lane, acc);
            BAR();
            __builtin_amdgcn_s_setprio(2);
            f32x4 s[2], q[2];
#pragma unroll
            for (int m = 0; m < 2; ++m) {
                const f32x4 r = *(const LAS f32x4*)(R + wm * 32 + m * 16 + fq * 4), mr = *(const LAS f32x4*)(M + wm * 32 + m * 16 + fq * 4);
                s[m] = (f32x4){0.f, 0.f, 0.f, 0.f}; q[m] = s[m];
#pragma unroll
                for (int n = 0; n < 7; ++n) {
                    const f32x4 v = acc[m][n] * r + mr * WS[112 + n * 16 + fr];
                    const unsigned a = pk_bf16(gelu_tanh(v[0]), gelu_tanh(v[1])), b = pk_bf16(gelu_tanh(v[2]), gelu_tanh(v[3]));
                    const f32x4 z = (f32x4){bf_lo(a), bf_hi(a), bf_lo(b), bf_hi(b)};
                    acc[m][n] = z; s[m] += z; q[m] += z * z;
                }
            }
            float ps = 0.f, pq = 0.f;
#pragma unroll
            for (int m = 0; m < 2; ++m)
#pragma unroll
                for (int j = 0; j < 4; ++j) {
                    float a = s[m][j], b = q[m][j];
#pragma unroll
                    for (int o = 1; o < 16; o <<= 1) { a += __shfl_xor(a, o); b += __shfl_xor(b, o); }
                    if (fr == m * 4 + j) { ps = a; pq = b; }
                }
            const __amdgpu_buffer_rsrc_t rX = rsrc(xch + (size_t)(tm * 128 + wm * 32) * 64, 32 * 256);
            if (fr < 8) __builtin_amdgcn_raw_buffer_store_b128((u32x4){__float_as_uint(ps), tag, __float_as_uint(pq), tag}, rX, ((fr >> 2) * 16 + fq * 4 + (fr & 3)) * 256 + tn * 16, 0, 16);
            __builtin_amdgcn_s_setprio(0);
            float ts, tq;
            for (int spin = 0;; ++spin) {
                u32x4 g[8]; bool ok = true; ts = 0.f; tq = 0.f;
#pragma unroll
                for (int i = 0; i < 8; ++i) g[i] = __builtin_amdgcn_raw_buffer_load_b128(rX, lane * 128 + i * 16, 0, 16);
#pragma unroll
                for (int i = 0; i < 8; ++i) { ok &= (g[i][1] == tag) & (g[i][3] == tag); ts += __uint_as_float(g[i][0]); tq += __uint_as_float(g[i][2]); }
                if (__all(ok) || spin > (1 << 20)) break;
                __builtin_amdgcn_s_sleep(1);
            }
            ts += __shfl_xor(ts, 1); tq += __shfl_xor(tq, 1);
            const float mean = ts * (1.0f / FH), var = fmaxf(tq * (1.0f / FH) - mean * mean, 0.f), rr = __builtin_amdgcn_rsqf(var + LN_EPS);
            if ((lane & 1) == 0) Z[wm * 32 + (lane >> 1)] = (f32x2){rr, -mean * rr};
            WAITCNT(63, 0);
#pragma unroll
            for (int m = 0; m < 2; ++m) {
                const f32x4 a = *(const LAS f32x4*)(Z + wm * 32 + m * 16 + fq * 4), b = *(const LAS f32x4*)(Z + wm * 32 + m * 16 + fq * 4 + 2);
                const f32x4 r = (f32x4){a[0], a[2], b[0], b[2]}, mr = (f32x4){a[1], a[3], b[1], b[3]};
#pragma unroll
                for (int n = 0; n < 7; ++n) {
                    const f32x4 v = acc[m][n] * r + mr;
                    *(LAS u32x2*)(lds + (n * 16 + fr) * RS + (wm * 32 + m * 16 + fq * 4) * 2) = (u32x2){pk_bf16(v[0], v[1]), pk_bf16(v[2], v[3])};
                }
            }
            WAITCNT(63, 0);
        }
    }
    BAR();
    const __amdgpu_buffer_rsrc_t rC = rsrc(z2t + (size_t)tn * 112 * S_ + tm * 128, 112 * S_ * 2 - tm * 256);
#pragma unroll
    for (int i = 0; i < 3; ++i) {
        const int r = i * 48 + (tid >> 4), c = tid & 15;
        if (r < 112) __builtin_amdgcn_raw_buffer_store_b128(*(const LAS u32x4*)(lds + r * RS + c * 16), rC, r * S_ * 2 + c * 16, 0, STORE_AUX);
    }
}

constexpr int G4_NS = 4, G4_LDS = G4_NS * 240 * 128;
__global__ __launch_bounds__(512, 2) void k_g4(const u16* __restrict__ wsp, const u16* __restrict__ z2t, const u16* __restrict__ z1, const u16* __restrict__ bsp, u16* __restrict__ h) {
    extern __shared__ __attribute__((aligned(1024))) unsigned char shm[];
    LAS unsigned char* lds = (LAS unsigned char*)shm;
    const int tid = threadIdx.x, lane = tid & 63, w = __builtin_amdgcn_readfirstlane(tid >> 6);
    int tm, tn; tile_of_block(tm, tn);
    constexpr int RS = 240;
    if (w >= 4) {
        loader_loop<128, 112, G4_NS, 4, S_, S_, S_ / 64, 0>(lds, wsp + (size_t)tm * 128 * S_, wsp, z2t + (size_t)tn * 112 * S_, w - 4, lane);
        BAR();
    } else {
        const int fr = lane & 15, fq = lane >> 4;
        u32x4 zz[7]; float b[2];
#pragma unroll
        for (int n = 0; n < 7; ++n) zz[n] = *(const u32x4*)(z1 + (size_t)((tm * 16 + tn) * 4 + w) * 3584 + n * 512 + lane * 8);
#pragma unroll
        for (int m = 0; m < 2; ++m) b[m] = bf_lo(bsp[tm * 128 + w * 32 + m * 16 + fr]);
        f32x4 acc[2][7];
        compute_loop<128, 112, G4_NS, S_ / 64, 2, 7, false>(lds, w * 32, 0, lane, acc);
        BAR();
#pragma unroll
        for (int m = 0; m < 2; ++m)
#pragma unroll
            for (int n = 0; n < 7; ++n) {
                const f32x4 z = (f32x4){bf_lo(zz[n][m * 2]), bf_hi(zz[n][m * 2]), bf_lo(zz[n][m * 2 + 1]), bf_hi(zz[n][m * 2 + 1])};
                const f32x4 v = z * (acc[m][n] + b[m]);
                *(LAS u32x2*)(lds + (w * 32 + m * 16 + fr) * RS + (n * 16 + fq * 4) * 2) = (u32x2){pk_bf16(v[0], v[1]), pk_bf16(v[2], v[3])};
            }
        WAITCNT(63, 0);
    }
    BAR();
    const __amdgpu_buffer_rsrc_t rC = rsrc(h + (size_t)tm * 128 * FH + tn * 112, 128 * FH * 2 - tn * 224);
#pragma unroll
    for (int i = 0; i < 4; ++i) {
        const int c = i * 512 + tid, r = c / 14, cc = c - r * 14;
        if (c < 1792) __builtin_amdgcn_raw_buffer_store_b128(*(const LAS u32x4*)(lds + r * RS + cc * 16), rC, r * FH * 2 + cc * 16, 0, STORE_AUX);
    }
}

constexpr int K0_BLOCKS = 1392;
__global__ __launch_bounds__(256) void k_prep(const u16* __restrict__ x, const u16* __restrict__ wre, const u16* __restrict__ wim, const u16* __restrict__ win, const u16* __restrict__ wout,
                                              u16* __restrict__ wint, u16* __restrict__ woutt, float* __restrict__ wsumd, float* __restrict__ wsp, float* __restrict__ sp, u32x4* __restrict__ xch) {
    __shared__ __attribute__((aligned(16))) unsigned char tile[4 * 64 * 144];
    const int tid = threadIdx.x, lane = tid & 63, w = tid >> 6, b = blockIdx.x;
    if (b < 336) {
        const bool isin = b < 224;
        const int bb = isin ? b : b - 224, nbn = isin ? 56 : 16, ld = isin ? F_ : D_, ldo = isin ? D_ : FH;
        const int k0 = (bb / nbn) * 256 + w * 64, n0 = (bb % nbn) * 64;
        const u16* src = (isin ? win : wout) + (size_t)k0 * ld + n0;
        LAS unsigned char* t = (LAS unsigned char*)tile + w * 64 * 144;
        u32x4 v[8];
#pragma unroll
        for (int i = 0; i < 8; ++i) v[i] = *(const u32x4*)(src + (size_t)(i * 8 + (lane >> 3)) * ld + (lane & 7) * 8);
#pragma unroll
        for (int i = 0; i < 8; ++i) *(LAS u32x4*)(t + (i * 8 + (lane >> 3)) * 144 + (lane & 7) * 16) = v[i];
        __syncthreads();
        const int g = lane >> 4, q = (lane & 15) >> 2, p = lane & 3;
#pragma unroll
        for (int nb = 0; nb < 4; ++nb) {
            const int n = n0 + nb * 16 + (lane & 15);
            int orow = n;
            if (isin) { const int half = n >= FH, nn = n - half * FH, tl = nn / 112; orow = tl * 224 + half * 112 + (nn - tl * 112); }
            float s = 0.f;
#pragma unroll
            for (int hh = 0; hh < 2; ++hh) {
                const s16x4 a = __builtin_amdgcn_ds_read_tr16_b64_v4i16((LAS s16x4*)(t + (hh * 32 + g * 8 + q) * 144 + nb * 32 + p * 8));
                const s16x4 c = __builtin_amdgcn_ds_read_tr16_b64_v4i16((LAS s16x4*)(t + (hh * 32 + g * 8 + 4 + q) * 144 + nb * 32 + p * 8));
                const u32x2 a2 = __builtin_bit_cast(u32x2, a), c2 = __builtin_bit_cast(u32x2, c);
                *(u32x4*)((isin ? wint : woutt) + (size_t)orow * ldo + k0 + hh * 32 + g * 8) = (u32x4){a2[0], a2[1], c2[0], c2[1]};
                s += (bf_lo(a2[0]) + bf_hi(a2[0])) + (bf_lo(a2[1]) + bf_hi(a2[1])) + (bf_lo(c2[0]) + bf_hi(c2[0])) + (bf_lo(c2[1]) + bf_hi(c2[1]));
            }
            s += __shfl_xor(s, 16); s += __shfl_xor(s, 32);
            if (isin && lane < 16) wsp[(size_t)orow * 16 + (k0 >> 6)] = s;
        }
    } else if (b < 1360) {
        const bool isw = b < 848;
        const int row = (isw ? b - 336 : b - 848) * 4 + w;
        const u16* src = isw ? (row < 1024 ? wre + (size_t)row * D_ : wim + (size_t)(row - 1024) * D_) : x + (size_t)row * D_;
        const u32x4 a = *(const u32x4*)(src + lane * 8), c = *(const u32x4*)(src + 512 + lane * 8);
        float s = 0.f, q = 0.f;
#pragma unroll
        for (int j = 0; j < 4; ++j) {
            const float a0 = bf_lo(a[j]), a1 = bf_hi(a[j]), c0 = bf_lo(c[j]), c1 = bf_hi(c[j]);
            s += (a0 + a1) + (c0 + c1); q += (a0 * a0 + a1 * a1) + (c0 * c0 + c1 * c1);
        }
        s += __shfl_xor(s, 1); s += __shfl_xor(s, 2); q += __shfl_xor(q, 1); q += __shfl_xor(q, 2);
        if (isw) {
#pragma unroll
            for (int o = 4; o < 64; o <<= 1) s += __shfl_xor(s, o);
            if (lane == 0) wsumd[row] = s;
        } else if ((lane & 3) == 0) *(f32x2*)(sp + (size_t)row * 32 + (lane >> 2) * 2) = (f32x2){s, q};
    } else {
#pragma unroll
        for (int i = 0; i < 4; ++i) xch[(size_t)(b - 1360) * 1024 + i * 256 + tid] = (u32x4){0u, 0u, 0u, 0u};
    }
}

extern "C" void kernel_launch(void* const* d_in, const int* in_sizes, int n_in, void* d_out, int out_size, void* d_ws, size_t ws_size, hipStream_t stream) {
    const u16* x0 = (const u16*)d_in[0];
    const u16* wsre = (const u16*)d_in[1];
    const u16* wsim = (const u16*)d_in[2];
    const u16* wdre = (const u16*)d_in[3];
    const u16* wdim = (const u16*)d_in[4];
    const u16* win = (const u16*)d_in[6];
    const u16* wsp = (const u16*)d_in[7];
    const u16* bsp = (const u16*)d_in[8];
    const u16* wout = (const u16*)d_in[9];
    char* ws = (char*)d_ws;
    constexpr size_t MB = 1 << 20;
    u16* T = (u16*)(ws); u16* xa = (u16*)(ws + 8 * MB); u16* xb = (u16*)(ws + 12 * MB);
    u16* wint = (u16*)(ws + 16 * MB); u16* woutt = (u16*)(ws + 24 * MB);
    u16* z1 = (u16*)(ws + 28 * MB); u16* z2t = (u16*)(ws + 36 * MB); u16* h = (u16*)(ws + 44 * MB);
    float* sp = (float*)(ws + 52 * MB); float* wsumd = (float*)(ws + 52 * MB + 256 * 1024); float* wsp_in = (float*)(ws + 52 * MB + 512 * 1024);
    unsigned* xch = (unsigned*)(ws + 53 * MB);

    auto g2 = k_gres<S_, 4096, 64, 32, true>;
    auto g5a = k_gres<FH, FH, FH / 64, 0, true>;
    auto g5b = k_gres<FH, FH, FH / 64, 0, false>;
    (void)hipFuncSetAttribute((const void*)k_g1, hipFuncAttributeMaxDynamicSharedMemorySize, G1_LDS);
    (void)hipFuncSetAttribute((const void*)g2, hipFuncAttributeMaxDynamicSharedMemorySize, GR_LDS);
    (void)hipFuncSetAttribute((const void*)g5a, hipFuncAttributeMaxDynamicSharedMemorySize, GR_LDS);
    (void)hipFuncSetAttribute((const void*)g5b, hipFuncAttributeMaxDynamicSharedMemorySize, GR_LDS);
    (void)hipFuncSetAttribute((const void*)k_g3, hipFuncAttributeMaxDynamicSharedMemorySize, G3_LDS);
    (void)hipFuncSetAttribute((const void*)k_g4, hipFuncAttributeMaxDynamicSharedMemorySize, G4_LDS);

    k_prep<<<K0_BLOCKS, 256, 0, stream>>>(x0, wdre, wdim, win, wout, wint, woutt, wsumd, wsp_in, sp, (u32x4*)xch);
    for (int l = 0; l < 2; ++l) {
        const u16* x = l == 0 ? x0 : xb;
        k_g1<<<256, 512, G1_LDS, stream>>>(wdre, wdim, x, sp, wsumd, T);
        g2<<<256, 512, GR_LDS, stream>>>(wsre, wsim, T, x, xa, sp);
        k_g3<<<256, 768, G3_LDS, stream>>>(xa, wint, sp, wsp_in, z1, z2t, xch, (unsigned)(l + 1));
        k_g4<<<256, 512, G4_LDS, stream>>>(wsp, z2t, z1, bsp, h);
        if (l == 0) g5a<<<256, 512, GR_LDS, stream>>>(h, h, woutt, xa, xb, sp);
        else g5b<<<256, 512, GR_LDS, stream>>>(h, h, woutt, xa, (u16*)d_out, sp);
    }
}
```

```cpp
#include <hip/hip_runtime.h>
#include <hip/hip_bf16.h>

typedef unsigned short u16;
typedef short bf16x8 __attribute__((ext_vector_type(8)));
typedef short s16x4 __attribute__((ext_vector_type(4)));
typedef float f32x2 __attribute__((ext_vector_type(2)));
typedef float f32x4 __attribute__((ext_vector_type(4)));
typedef unsigned u32x2 __attribute__((ext_vector_type(2)));
typedef unsigned u32x4 __attribute__((ext_vector_type(4)));
#define LAS __attribute__((address_space(3)))
#define DEV __device__ __forceinline__

constexpr int waitimm(int vm, int lgkm) { return (vm & 15) | (7 << 4) | ((lgkm & 15) << 8) | ((vm >> 4) << 14); }
#define WAITCNT(vm, lgkm) __builtin_amdgcn_s_waitcnt(waitimm(vm, lgkm))
#define BAR() do { asm volatile("" ::: "memory"); __builtin_amdgcn_s_barrier(); asm volatile("" ::: "memory"); } while (0)
#define LBAR() BAR()
#define SB0() __builtin_amdgcn_sched_barrier(0)

#define KID_ARG
#define KID(x)
#define ST_PARAM
#define ST_PASS
#define STAMP0()
#define STAMP(i)
#define STAMP_END(T)
#define G1_CFG true, 2, 3
#define GR_CFG true, 1, 1
#define G3_CFG false, 9, 13
#define G4_CFG true, 4, 5
constexpr int S_ = 2048, D_ = 1024, F_ = 3584, FH = 1792;
constexpr float LN_EPS = 1e-5f;
constexpr int STORE_AUX = 16;

DEV unsigned pk_bf16(float lo, float hi) { unsigned r; asm("v_cvt_pk_bf16_f32 %0, %1, %2" : "=v"(r) : "v"(lo), "v"(hi)); return r; }
DEV float bf_lo(unsigned v) { return __uint_as_float(v << 16); }
DEV float bf_hi(unsigned v) { return __uint_as_float(v & 0xffff0000u); }
DEV __amdgpu_buffer_rsrc_t rsrc(const void* p, unsigned bytes) { return __builtin_amdgcn_make_buffer_rsrc((void*)p, 0, bytes, 0x00020000); }
DEV f32x4 gelu_tanh(f32x4 x) {
    const float c0 = -2.3022081986f, c1 = c0 * 0.044715f;
    const f32x4 a = x * (x * x * c1 + c0);
    f32x4 d = (f32x4){__builtin_amdgcn_exp2f(a[0]), __builtin_amdgcn_exp2f(a[1]), __builtin_amdgcn_exp2f(a[2]), __builtin_amdgcn_exp2f(a[3])} + 1.0f;
    return x * (f32x4){__builtin_amdgcn_rcpf(d[0]), __builtin_amdgcn_rcpf(d[1]), __builtin_amdgcn_rcpf(d[2]), __builtin_amdgcn_rcpf(d[3])};
}
DEV u32x4 pk8(f32x4 a, f32x4 b) { return (u32x4){pk_bf16(a[0], a[1]), pk_bf16(a[2], a[3]), pk_bf16(b[0], b[1]), pk_bf16(b[2], b[3])}; }
DEV f32x4 unpk4(unsigned a, unsigned b) { return (f32x4){bf_lo(a), bf_hi(a), bf_lo(b), bf_hi(b)}; }
__host__ __device__ constexpr int perm32(int slot) { return 8 * ((slot & 15) >> 2) + 4 * ((slot >> 4) & 1) + (slot & 3); }
__host__ __device__ constexpr int inv32(int row) { return 16 * ((row >> 2) & 1) + 4 * ((row >> 3) & 3) + (row & 3); }
DEV float fq_sum(float a) {
    const auto p = __builtin_amdgcn_permlane16_swap(__float_as_uint(a), __float_as_uint(a), false, false);
    a = __uint_as_float(p[0]) + __uint_as_float(p[1]);
    const auto q = __builtin_amdgcn_permlane32_swap(__float_as_uint(a), __float_as_uint(a), false, false);
    return __uint_as_float(q[0]) + __uint_as_float(q[1]);
}
template <int CTRL> DEV float dpp_mov(float a) { return __builtin_bit_cast(float, __builtin_amdgcn_update_dpp(0, __builtin_bit_cast(int, a), CTRL, 0xf, 0xf, true)); }
template <int CTRL> DEV float dpp_add(float a) { return a + __builtin_bit_cast(float, __builtin_amdgcn_update_dpp(0, __builtin_bit_cast(int, a), CTRL, 0xf, 0xf, true)); }
DEV float row16_sum(float a) { a = dpp_add<0x128>(a); a = dpp_add<0x124>(a); a = dpp_add<0x122>(a); return dpp_add<0x121>(a); }
DEV float oct_sum(float a) { a = dpp_add<0xb1>(a); a = dpp_add<0x4e>(a); return dpp_add<0x141>(a); }
DEV float quad_sum(float a) { a = dpp_add<0xb1>(a); return dpp_add<0x4e>(a); }
constexpr unsigned XCH_EMPTY = 0xffffffffu;
DEV unsigned xch_word(float v) { const unsigned u = __float_as_uint(v); return u == XCH_EMPTY ? 0x7fc00000u : u; }

template <int BM, int BN, int NS, int NL, int LDP, int LDQ, int NT, int KSW, bool PERMP, bool PERMQ>
DEV void loader_loop(LAS unsigned char* lds, const u16* P0, const u16* P1, const u16* Q, int lw, int lane) {
    constexpr int NPP = BM / 8, NPQ = BN / 8, LP = NPP / NL, LQ = (NPQ + NL - 1) / NL, LQMIN = NPQ / NL, LMIN = LP + LQMIN, STAGE = (BM + BN) * 128;
    static_assert(NPP % NL == 0 && NL % 2 == 0 && LMIN * (NS - 2) <= 63 && NS >= 3 && NS <= 5, "");
    const __amdgpu_buffer_rsrc_t rP0 = rsrc(P0, BM * LDP * 2), rP1 = rsrc(P1, BM * LDP * 2), rQ = rsrc(Q, BN * LDQ * 2);
    const int ch = ((lane & 7) ^ (((lw & 1) * 4 + (lane >> 4)) & 7)) * 16, row = lw * 8 + (lane >> 3);
    static_assert(NL == 4 && !(PERMQ && BN % 32), "");
    const int vP = (PERMP ? perm32(row) : row) * LDP * 2 + ch, vQ = (PERMQ ? perm32(row) : row) * LDQ * 2 + ch;
    auto stage = [&](int slot, int kt) {
        LAS unsigned char* dst = lds + slot * STAGE + lw * 1024;
        const bool second = KSW && kt >= KSW;
        const __amdgpu_buffer_rsrc_t rP = second ? rP1 : rP0;
        const int kp = (second ? kt - KSW : kt) * 128;
#pragma unroll
        for (int i = 0; i < LP; ++i) __builtin_amdgcn_raw_ptr_buffer_load_lds(rP, (LAS void*)(dst + i * NL * 1024), 16, vP, i * NL * 8 * LDP * 2 + kp, 0, 0);
#pragma unroll
        for (int j = 0; j < LQ; ++j)
            if (j < LQMIN || j * NL + lw < NPQ) __builtin_amdgcn_raw_ptr_buffer_load_lds(rQ, (LAS void*)(dst + (NPP + j * NL) * 1024), 16, vQ, j * NL * 8 * LDQ * 2 + kt * 128, 0, 0);
    };
    stage(0, 0); stage(1, 1);
    WAITCNT(LMIN, 15);
    BAR();
#pragma unroll
    for (int s = 2; s < NS; ++s) stage(s, s);
    int slot = 0;
#pragma unroll 1
    for (int t = 0; t < NT - NS; ++t) {
        WAITCNT(LMIN * (NS - 2), 15);
        LBAR();
        stage(slot, t + NS);
        slot = (slot + 1 == NS) ? 0 : slot + 1;
    }
    if constexpr (NS >= 5) { WAITCNT(LMIN * 3, 15); LBAR(); }
    if constexpr (NS >= 4) { WAITCNT(LMIN * 2, 15); LBAR(); }
    WAITCNT(LMIN, 15); LBAR();
    WAITCNT(0, 15); LBAR();
}

template <int BM, int BN, int NS, int NT, int MT, int NTL, bool SWAP, bool DEEP, int RNUM, int RDEN>
DEV void compute_loop(LAS unsigned char* lds, int prow0, int qrow0, int lane, f32x4 (&acc)[MT][NTL] ST_PARAM) {
    constexpr int STAGE = (BM + BN) * 128, NR = MT + NTL, NM = MT * NTL, NSET = DEEP ? 4 : 2;
    const int fr = lane & 15, fq = lane >> 4;
    int aoff[2], boff[2];
#pragma unroll
    for (int k = 0; k < 2; ++k) {
        const int ch = ((k * 4 + fq) ^ ((fr >> 1) & 7)) * 16;
        aoff[k] = (prow0 + fr) * 128 + ch;
        boff[k] = (BM + qrow0 + fr) * 128 + ch;
    }
#pragma unroll
    for (int m = 0; m < MT; ++m)
#pragma unroll
        for (int n = 0; n < NTL; ++n) acc[m][n] = (f32x4){0.f, 0.f, 0.f, 0.f};
#pragma unroll
    for (int m = 0; m < MT; ++m)
#pragma unroll
        for (int n = 0; n < NTL; ++n) asm volatile("" : "+v"(acc[m][n]));
    bf16x8 fa[NSET][MT], fb[NSET][NTL];
    auto rd1 = [&](int set, const LAS unsigned char* b, int k, int i) {
        if (i == 0) fa[set][0] = *(const LAS bf16x8*)(b + aoff[k]);
        else if (i <= NTL) fb[set][i - 1] = *(const LAS bf16x8*)(b + boff[k] + (i - 1) * 2048);
        else fa[set][i - NTL] = *(const LAS bf16x8*)(b + aoff[k] + (i - NTL) * 2048);
    };
    auto mma1 = [&](int set, int i) {
        const int m = i / NTL, n = i % NTL;
        if (SWAP) asm volatile("v_mfma_f32_16x16x32_bf16 %0, %1, %2, %0" : "+v"(acc[m][n]) : "v"(fa[set][m]), "v"(fb[set][n]));
        else asm volatile("v_mfma_f32_16x16x32_bf16 %0, %1, %2, %0" : "+v"(acc[m][n]) : "v"(fb[set][n]), "v"(fa[set][m]));
    };
    BAR();
    STAMP(1);
    int slot = 0;
    if constexpr (!DEEP) {
        auto sub = [&](int set, int rslot, int k, bool rd) {
            const LAS unsigned char* b = lds + rslot * STAGE;
#pragma unroll
            for (int i = 0; i < NM; ++i) {
                mma1(set, i); SB0();
                if (rd) {
#pragma unroll
                    for (int r = (i * RNUM + RDEN - 1) / RDEN; r < ((i + 1) * RNUM + RDEN - 1) / RDEN; ++r) if (r < NR) rd1(set ^ 1, b, k, r);
                    SB0();
                }
            }
        };
        static_assert((NM * RNUM + RDEN - 1) / RDEN >= NR, "");
#pragma unroll
        for (int r = 0; r < NR; ++r) rd1(0, lds, 0, r);
        for (int t = 0; t < NT - 1; ++t) {
            const int nslot = (slot + 1 == NS) ? 0 : slot + 1;
            SB0(); sub(0, slot, 1, true);
            WAITCNT(63, 0);
            LBAR(); SB0();
            sub(1, nslot, 0, true);
            slot = nslot;
        }
        SB0(); sub(0, slot, 1, true); sub(1, 0, 0, false);
    } else {
        auto stg = [&](int set, int rslot, bool rd) {
            const LAS unsigned char* b = lds + rslot * STAGE;
#pragma unroll
            for (int i = 0; i < 2 * NM; ++i) {
                mma1(set + i / NM, i % NM); SB0();
                if (rd) {
#pragma unroll
                    for (int r = (i * RNUM + RDEN - 1) / RDEN; r < ((i + 1) * RNUM + RDEN - 1) / RDEN; ++r) if (r < 2 * NR) rd1((set ^ 2) + r / NR, b, r / NR, r % NR);
                    SB0();
                }
            }
        };
        static_assert((2 * NM * RNUM + RDEN - 1) / RDEN >= 2 * NR && NT % 2 == 0, "");
#pragma unroll
        for (int r = 0; r < 2 * NR; ++r) rd1(r / NR, lds, r / NR, r % NR);
        for (int t = 0; t < NT - 2; t += 2) {
            slot = (slot + 1 == NS) ? 0 : slot + 1;
            WAITCNT(63, 0); LBAR(); SB0();
            stg(0, slot, true);
            slot = (slot + 1 == NS) ? 0 : slot + 1;
            WAITCNT(63, 0); LBAR(); SB0();
            stg(2, slot, true);
        }
        slot = (slot + 1 == NS) ? 0 : slot + 1;
        stg(0, 0, false);
        LBAR(); SB0();
#pragma unroll
        for (int r = 0; r < 2 * NR; ++r) rd1(2 + r / NR, lds + slot * STAGE, r / NR, r % NR);
        SB0();
        stg(2, 0, false);
    }
    WAITCNT(63, 0);
    asm volatile("s_nop 15" ::: "memory");
#pragma unroll
    for (int m = 0; m < MT; ++m)
#pragma unroll
        for (int n = 0; n < NTL; ++n) asm volatile("" : "+v"(acc[m][n]));
}

DEV void tile_of_block(int& tm, int& tn) {
    const int x = blockIdx.x & 7, idx = blockIdx.x >> 3;
    tm = (x >> 1) * 4 + (idx >> 3); tn = (x & 1) * 8 + (idx & 7);
}

DEV void row_tables(const float* sp, int row0, int tid, LAS float* R, LAS float* M, float sign) {
    f32x4 v[4];
#pragma unroll
    for (int i = 0; i < 4; ++i) v[i] = *(const f32x4*)(sp + (size_t)(row0 + i * 32 + (tid >> 3)) * 32 + (tid & 7) * 4);
#pragma unroll
    for (int i = 0; i < 4; ++i) {
        float s = v[i][0] + v[i][2], q = v[i][1] + v[i][3];
        s = oct_sum(s); q = oct_sum(q);
        const float mean = s * (1.0f / D_), var = fmaxf(q * (1.0f / D_) - mean * mean, 0.f), r = sign * __builtin_amdgcn_rsqf(var + LN_EPS);
        if ((tid & 7) == 0) { R[i * 32 + (tid >> 3)] = r; M[i * 32 + (tid >> 3)] = -mean * r; }
    }
}

constexpr int G1_NS = 4, G1_LDS = G1_NS * 256 * 128 + 1024;
__global__ __launch_bounds__(512, 2) void k_g1(const u16* __restrict__ wre, const u16* __restrict__ wim, const u16* __restrict__ x, const float* __restrict__ sp,
                                               const float* __restrict__ wsum, u16* __restrict__ T KID_ARG) {
    STAMP0();
    extern __shared__ __attribute__((aligned(1024))) unsigned char shm[];
    LAS unsigned char* lds = (LAS unsigned char*)shm;
    const int tid = threadIdx.x, lane = tid & 63, w = __builtin_amdgcn_readfirstlane(tid >> 6);
    int tm, tn; tile_of_block(tm, tn);
    if (w >= 4) {
        const u16* P = (tm < 8 ? wre : wim) + (size_t)(tm & 7) * 128 * D_;
        loader_loop<128, 128, G1_NS, 4, D_, D_, D_ / 64, 0, false, true>(lds, P, P, x + (size_t)tn * 128 * D_, w - 4, lane);
    } else {
        LAS float* R = (LAS float*)(lds + G1_NS * 256 * 128); LAS float* M = R + 128;
        const int wm = w >> 1, wn = w & 1, fr = lane & 15, fq = lane >> 4;
        float ws[4];
#pragma unroll
        for (int m = 0; m < 4; ++m) ws[m] = wsum[tm * 128 + wm * 64 + m * 16 + fr];
        row_tables(sp, tn * 128, tid, R, M, tm < 8 ? 1.0f : -1.0f);
        WAITCNT(63, 0);
        f32x4 acc[4][4];
        compute_loop<128, 128, G1_NS, D_ / 64, 4, 4, false, G1_CFG>(lds, wm * 64, wn * 64, lane, acc ST_PASS);
        STAMP(2);
        const __amdgpu_buffer_rsrc_t rC = rsrc(T + (size_t)(tm & 7) * 128 * 4096 + (tm < 8 ? 0 : 2048) + tn * 128, 128 * 4096 * 2 - (2048 + tn * 128) * 2);
#pragma unroll
        for (int q = 0; q < 2; ++q) {
            const int c = wn * 64 + q * 32 + fq * 8;
            const f32x4 r0 = *(const LAS f32x4*)(R + c), r1 = *(const LAS f32x4*)(R + c + 4), m0 = *(const LAS f32x4*)(M + c), m1 = *(const LAS f32x4*)(M + c + 4);
#pragma unroll
            for (int m = 0; m < 4; ++m) {
                const int row = wm * 64 + (m >> 1) * 32 + inv32((m & 1) * 16 + fr);
                __builtin_amdgcn_raw_buffer_store_b128(pk8(acc[m][2 * q] * r0 + m0 * ws[m], acc[m][2 * q + 1] * r1 + m1 * ws[m]), rC, row * 8192 + c * 2, 0, STORE_AUX);
            }
        }
    }
    STAMP_END(0);
}

constexpr int GR_NS = 4, GR_LDS = GR_NS * 192 * 128;
template <int LDP, int LDQ, int NT, int KSW, bool STATS>
__global__ __launch_bounds__(512, 2) void k_gres(const u16* __restrict__ p0, const u16* __restrict__ p1, const u16* __restrict__ q, const u16* __restrict__ res,
                                                 u16* __restrict__ out, float* __restrict__ sp KID_ARG) {
    STAMP0();
    extern __shared__ __attribute__((aligned(1024))) unsigned char shm[];
    LAS unsigned char* lds = (LAS unsigned char*)shm;
    const int tid = threadIdx.x, lane = tid & 63, w = __builtin_amdgcn_readfirstlane(tid >> 6);
    int tm, tn; tile_of_block(tm, tn);
    if (w >= 4) {
        loader_loop<128, 64, GR_NS, 4, LDP, LDQ, NT, KSW, false, false>(lds, p0 + (size_t)tm * 128 * LDP, p1 + (size_t)tm * 128 * LDP, q + (size_t)tn * 64 * LDQ, w - 4, lane);
    } else {
        const int fr = lane & 15, fq = lane >> 4;
        u32x4 xr[2][2];
#pragma unroll
        for (int m = 0; m < 2; ++m)
#pragma unroll
            for (int h = 0; h < 2; ++h) xr[m][h] = *(const u32x4*)(res + (size_t)(tm * 128 + w * 32 + m * 16 + fr) * D_ + tn * 64 + h * 32 + fq * 8);
        f32x4 acc[2][4];
        compute_loop<128, 64, GR_NS, NT, 2, 4, false, GR_CFG>(lds, w * 32, 0, lane, acc ST_PASS);
        STAMP(2);
        const __amdgpu_buffer_rsrc_t rC = rsrc(out + (size_t)(tm * 128 + w * 32) * D_ + tn * 64, 32 * D_ * 2 - tn * 128);
#pragma unroll
        for (int m = 0; m < 2; ++m) {
            f32x4 s = (f32x4){0.f, 0.f, 0.f, 0.f}, qq = s;
#pragma unroll
            for (int h = 0; h < 2; ++h) {
                const u32x4 o = pk8(acc[m][2 * h] + unpk4(xr[m][h][0], xr[m][h][1]), acc[m][2 * h + 1] + unpk4(xr[m][h][2], xr[m][h][3]));
                __builtin_amdgcn_raw_buffer_store_b128(o, rC, (m * 16 + fr) * D_ * 2 + h * 64 + fq * 16, 0, STORE_AUX);
                const f32x4 a = unpk4(o[0], o[1]), b = unpk4(o[2], o[3]);
                s += a + b; qq += a * a + b * b;
            }
            if (STATS) {
                const float st = fq_sum((s[0] + s[1]) + (s[2] + s[3])), qt = fq_sum((qq[0] + qq[1]) + (qq[2] + qq[3]));
                if (fq == 0) *(f32x2*)(sp + (size_t)(tm * 128 + w * 32 + m * 16 + fr) * 32 + tn * 2) = (f32x2){st, qt};
            }
        }
    }
    STAMP_END(0);
}

constexpr int G3_NS = 3, G3_RING = G3_NS * 352 * 128, G3_LDS = G3_RING + 1024 + 1024 + 1024;
__global__ __launch_bounds__(768, 3) void k_g3(const u16* __restrict__ x, const u16* __restrict__ wt, const float* __restrict__ sp, const float* __restrict__ wsp,
                                               u16* __restrict__ z1, u16* __restrict__ z2t, unsigned* xch KID_ARG) {
    STAMP0();
    extern __shared__ __attribute__((aligned(1024))) unsigned char shm[];
    LAS unsigned char* lds = (LAS unsigned char*)shm;
    const int tid = threadIdx.x, lane = tid & 63, w = __builtin_amdgcn_readfirstlane(tid >> 6);
    int tm, tn; tile_of_block(tm, tn);
    if (w >= 8) {
        loader_loop<128, 224, G3_NS, 4, D_, D_, D_ / 64, 0, true, false>(lds, x + (size_t)tm * 128 * D_, x, wt + (size_t)tn * 224 * D_, w - 8, lane);
    } else {
        LAS float* R = (LAS float*)(lds + G3_RING); LAS float* M = R + 128; LAS float* WS = R + 256; LAS float* Z = R + 512;
        const int wm = w & 3, fr = lane & 15, fq = lane >> 4;
        if (w < 4) row_tables(sp, tm * 128, tid, R, M, 1.0f);
        else {
            f32x4 v[4];
#pragma unroll
            for (int i = 0; i < 4; ++i) { const int c = i * 256 + tid - 256; v[i] = *(const f32x4*)(wsp + (size_t)tn * 224 * 16 + (c < 896 ? c : 0) * 4); }
#pragma unroll
            for (int i = 0; i < 4; ++i) {
                const int c = i * 256 + tid - 256;
                const float s = quad_sum((v[i][0] + v[i][1]) + (v[i][2] + v[i][3]));
                if ((c & 3) == 0 && c < 896) WS[c >> 2] = s;
            }
        }
        WAITCNT(63, 0);
        f32x4 acc[2][7];
        if (w < 4) {
            compute_loop<128, 224, G3_NS, D_ / 64, 2, 7, false, G3_CFG>(lds, wm * 32, 0, lane, acc ST_PASS);
            STAMP(2);
            float r[2], mr[2];
#pragma unroll
            for (int m = 0; m < 2; ++m) { r[m] = R[wm * 32 + perm32(m * 16 + fr)]; mr[m] = M[wm * 32 + perm32(m * 16 + fr)]; }
            const __amdgpu_buffer_rsrc_t rZ = rsrc(z1 + (size_t)((tm * 16 + tn) * 4 + wm) * 3584, 7168);
#pragma unroll
            for (int n = 0; n < 7; ++n) {
                const f32x4 ws = *(const LAS f32x4*)(WS + n * 16 + fq * 4);
                __builtin_amdgcn_raw_buffer_store_b128(pk8(gelu_tanh(acc[0][n] * r[0] + ws * mr[0]), gelu_tanh(acc[1][n] * r[1] + ws * mr[1])), rZ, n * 1024 + lane * 16, 0, STORE_AUX);
            }
        } else {
            compute_loop<128, 224, G3_NS, D_ / 64, 2, 7, true, G3_CFG>(lds, wm * 32, 112, lane, acc ST_PASS);
            STAMP(2);
            __builtin_amdgcn_s_setprio(2);
            f32x4 s[2], q[2];
#pragma unroll
            for (int m = 0; m < 2; ++m) {
                const f32x4 r = *(const LAS f32x4*)(R + wm * 32 + fq * 8 + m * 4), mr = *(const LAS f32x4*)(M + wm * 32 + fq * 8 + m * 4);
                s[m] = (f32x4){0.f, 0.f, 0.f, 0.f}; q[m] = s[m];
#pragma unroll
                for (int n = 0; n < 7; ++n) {
                    const f32x4 z = gelu_tanh(acc[m][n] * r + mr * WS[112 + n * 16 + fr]);
                    acc[m][n] = z; s[m] += z; q[m] += z * z;
                }
            }
            STAMP(4);
            float v8[8], v4[4], v2[2];
#pragma unroll
            for (int i = 0; i < 8; ++i) {
                const float lo = (i & 1) ? q[0][i >> 1] : s[0][i >> 1], hi = (i & 1) ? q[1][i >> 1] : s[1][i >> 1];
                const float keep = (fr & 8) ? hi : lo, give = (fr & 8) ? lo : hi;
                v8[i] = keep + dpp_mov<0x128>(give);
            }
#pragma unroll
            for (int i = 0; i < 4; ++i) { const float keep = (fr & 4) ? v8[i + 4] : v8[i], give = (fr & 4) ? v8[i] : v8[i + 4]; v4[i] = keep + dpp_mov<0x141>(dpp_mov<0x1b>(give)); }
#pragma unroll
            for (int i = 0; i < 2; ++i) { const float keep = (fr & 2) ? v4[i + 2] : v4[i], give = (fr & 2) ? v4[i] : v4[i + 2]; v2[i] = keep + dpp_mov<0x4e>(give); }
            const float pv = ((fr & 1) ? v2[1] : v2[0]) + dpp_mov<0xb1>((fr & 1) ? v2[0] : v2[1]);
            const __amdgpu_buffer_rsrc_t rX = rsrc(xch + (size_t)(tm * 4 + wm) * 1024, 4096);
            __builtin_amdgcn_raw_buffer_store_b32(xch_word(pv), rX, tn * 256 + lane * 4, 0, 16);
            __builtin_amdgcn_s_setprio(0);
            STAMP(5);
            f32x4 t;
            for (int spin = 0;; ++spin) {
                u32x4 g[4]; bool ok = true; t = (f32x4){0.f, 0.f, 0.f, 0.f};
#pragma unroll
                for (int i = 0; i < 4; ++i) g[i] = __builtin_amdgcn_raw_buffer_load_b128(rX, lane * 16 + i * 1024, 0, 16);
#pragma unroll
                for (int i = 0; i < 4; ++i) {
                    ok &= (g[i][0] != XCH_EMPTY) & (g[i][1] != XCH_EMPTY) & (g[i][2] != XCH_EMPTY) & (g[i][3] != XCH_EMPTY);
                    t += __builtin_bit_cast(f32x4, g[i]);
                }
                if (__all(ok) || spin > (1 << 20)) break;
                __builtin_amdgcn_s_sleep(4);
            }
            STAMP(7);
            const float mean0 = fq_sum(t[0]) * (1.0f / FH), mean1 = fq_sum(t[2]) * (1.0f / FH);
            const float r0 = __builtin_amdgcn_rsqf(fmaxf(fq_sum(t[1]) * (1.0f / FH) - mean0 * mean0, 0.f) + LN_EPS);
            const float r1 = __builtin_amdgcn_rsqf(fmaxf(fq_sum(t[3]) * (1.0f / FH) - mean1 * mean1, 0.f) + LN_EPS);
            if (lane < 16) { *(LAS f32x2*)(Z + wm * 64 + lane * 2) = (f32x2){r0, r1}; *(LAS f32x2*)(Z + wm * 64 + 32 + lane * 2) = (f32x2){-mean0 * r0, -mean1 * r1}; }
            WAITCNT(63, 0);
            const f32x4 ra = *(const LAS f32x4*)(Z + wm * 64 + fq * 8), rb = *(const LAS f32x4*)(Z + wm * 64 + fq * 8 + 4);
            const f32x4 ma = *(const LAS f32x4*)(Z + wm * 64 + 32 + fq * 8), mb = *(const LAS f32x4*)(Z + wm * 64 + 32 + fq * 8 + 4);
            const __amdgpu_buffer_rsrc_t rC = rsrc(z2t + (size_t)tn * 112 * S_ + tm * 128 + wm * 32, 112 * S_ * 2 - tm * 256 - wm * 64);
#pragma unroll
            for (int n = 0; n < 7; ++n)
                __builtin_amdgcn_raw_buffer_store_b128(pk8(acc[0][n] * ra + ma, acc[1][n] * rb + mb), rC, (n * 16 + fr) * S_ * 2 + fq * 16, 0, STORE_AUX);
            STAMP(8);
        }
    }
    STAMP_END(256);
}

constexpr int G4_NS = 4, G4_LDS = G4_NS * 240 * 128;
__global__ __launch_bounds__(512, 2) void k_g4(const u16* __restrict__ wsp, const u16* __restrict__ z2t, const u16* __restrict__ z1, const u16* __restrict__ bsp, u16* __restrict__ h KID_ARG) {
    STAMP0();
    extern __shared__ __attribute__((aligned(1024))) unsigned char shm[];
    LAS unsigned char* lds = (LAS unsigned char*)shm;
    const int tid = threadIdx.x, lane = tid & 63, w = __builtin_amdgcn_readfirstlane(tid >> 6);
    int tm, tn; tile_of_block(tm, tn);
    if (w >= 4) {
        loader_loop<128, 112, G4_NS, 4, S_, S_, S_ / 64, 0, true, false>(lds, wsp + (size_t)tm * 128 * S_, wsp, z2t + (size_t)tn * 112 * S_, w - 4, lane);
    } else {
        const int fr = lane & 15, fq = lane >> 4;
        u32x4 zz[7]; float b[2];
#pragma unroll
        for (int n = 0; n < 7; ++n) zz[n] = *(const u32x4*)(z1 + (size_t)((tm * 16 + tn) * 4 + w) * 3584 + n * 512 + lane * 8);
#pragma unroll
        for (int m = 0; m < 2; ++m) b[m] = bf_lo(bsp[tm * 128 + w * 32 + perm32(m * 16 + fr)]);
        f32x4 acc[2][7];
        compute_loop<128, 112, G4_NS, S_ / 64, 2, 7, false, G4_CFG>(lds, w * 32, 0, lane, acc ST_PASS);
        STAMP(2);
        const __amdgpu_buffer_rsrc_t rC = rsrc(h + (size_t)(tm * 128 + w * 32) * FH + tn * 112, 32 * FH * 2 - tn * 224);
#pragma unroll
        for (int m = 0; m < 2; ++m) {
            const int ro = perm32(m * 16 + fr) * FH * 2;
#pragma unroll
            for (int q = 0; q < 3; ++q)
                __builtin_amdgcn_raw_buffer_store_b128(pk8(unpk4(zz[2 * q][m * 2], zz[2 * q][m * 2 + 1]) * (acc[m][2 * q] + b[m]), unpk4(zz[2 * q + 1][m * 2], zz[2 * q + 1][m * 2 + 1]) * (acc[m][2 * q + 1] + b[m])),
                                                       rC, ro + q * 64 + fq * 16, 0, STORE_AUX);
            const f32x4 v = unpk4(zz[6][m * 2], zz[6][m * 2 + 1]) * (acc[m][6] + b[m]);
            __builtin_amdgcn_raw_buffer_store_b64((u32x2){pk_bf16(v[0], v[1]), pk_bf16(v[2], v[3])}, rC, ro + 192 + fq * 8, 0, STORE_AUX);
        }
    }
    STAMP_END(0);
}

constexpr int K0_BLOCKS = 1392 + 768;
__global__ __launch_bounds__(256) void k_prep(const u16* __restrict__ x, const u16* __restrict__ wre, const u16* __restrict__ wim, const u16* __restrict__ win, const u16* __restrict__ wout,
                                              u16* __restrict__ wint, u16* __restrict__ woutt, float* __restrict__ wsumd, float* __restrict__ wsp, float* __restrict__ sp, u32x4* __restrict__ xch,
                                              const u32x4* __restrict__ pf0, const u32x4* __restrict__ pf1, const u32x4* __restrict__ pf2 KID_ARG) {
    STAMP0();
    __shared__ __attribute__((aligned(16))) unsigned char tile[4 * 64 * 144];
    const int tid = threadIdx.x, lane = tid & 63, w = tid >> 6, b = blockIdx.x;
    if (b < 336) {
        const bool isin = b < 224;
        const int bb = isin ? b : b - 224, nbn = isin ? 56 : 16, ld = isin ? F_ : D_, ldo = isin ? D_ : FH;
        const int k0 = (bb / nbn) * 256 + w * 64, n0 = (bb % nbn) * 64;
        const u16* src = (isin ? win : wout) + (size_t)k0 * ld + n0;
        LAS unsigned char* t = (LAS unsigned char*)tile + w * 64 * 144;
        u32x4 v[8];
#pragma unroll
        for (int i = 0; i < 8; ++i) v[i] = *(const u32x4*)(src + (size_t)(i * 8 + (lane >> 3)) * ld + (lane & 7) * 8);
#pragma unroll
        for (int i = 0; i < 8; ++i) *(LAS u32x4*)(t + (i * 8 + (lane >> 3)) * 144 + (lane & 7) * 16) = v[i];
        __syncthreads();
        const int g = lane >> 4, q = (lane & 15) >> 2, p = lane & 3;
        const __amdgpu_buffer_rsrc_t rO = rsrc(isin ? wint : woutt, isin ? F_ * D_ * 2 : D_ * FH * 2);
#pragma unroll
        for (int nb = 0; nb < 4; ++nb) {
            const int n = n0 + nb * 16 + (lane & 15);
            int orow = (n & ~31) + inv32(n & 31);
            if (isin) { const int half = n >= FH, nn = n - half * FH, tl = nn / 112, c = nn - tl * 112; orow = tl * 224 + half * 112 + (c < 96 ? (c & ~31) + inv32(c & 31) : c); }
            float s = 0.f;
#pragma unroll
            for (int hh = 0; hh < 2; ++hh) {
                const s16x4 a = __builtin_amdgcn_ds_read_tr16_b64_v4i16((LAS s16x4*)(t + (hh * 32 + g * 8 + q) * 144 + nb * 32 + p * 8));
                const s16x4 c = __builtin_amdgcn_ds_read_tr16_b64_v4i16((LAS s16x4*)(t + (hh * 32 + g * 8 + 4 + q) * 144 + nb * 32 + p * 8));
                const u32x2 a2 = __builtin_bit_cast(u32x2, a), c2 = __builtin_bit_cast(u32x2, c);
                __builtin_amdgcn_raw_buffer_store_b128((u32x4){a2[0], a2[1], c2[0], c2[1]}, rO, (orow * ldo + k0 + hh * 32 + g * 8) * 2, 0, STORE_AUX);
                s += (bf_lo(a2[0]) + bf_hi(a2[0])) + (bf_lo(a2[1]) + bf_hi(a2[1])) + (bf_lo(c2[0]) + bf_hi(c2[0])) + (bf_lo(c2[1]) + bf_hi(c2[1]));
            }
            s += __shfl_xor(s, 16); s += __shfl_xor(s, 32);
            if (isin && lane < 16) wsp[(size_t)orow * 16 + (k0 >> 6)] = s;
        }
    } else if (b < 1360) {
        const bool isw = b < 848;
        const int row = (isw ? b - 336 : b - 848) * 4 + w;
        const u16* src = isw ? (row < 1024 ? wre + (size_t)row * D_ : wim + (size_t)(row - 1024) * D_) : x + (size_t)row * D_;
        const u32x4 a = *(const u32x4*)(src + lane * 8), c = *(const u32x4*)(src + 512 + lane * 8);
        float s = 0.f, q = 0.f;
#pragma unroll
        for (int j = 0; j < 4; ++j) {
            const float a0 = bf_lo(a[j]), a1 = bf_hi(a[j]), c0 = bf_lo(c[j]), c1 = bf_hi(c[j]);
            s += (a0 + a1) + (c0 + c1); q += (a0 * a0 + a1 * a1) + (c0 * c0 + c1 * c1);
        }
        s += __shfl_xor(s, 1); s += __shfl_xor(s, 2); q += __shfl_xor(q, 1); q += __shfl_xor(q, 2);
        if (isw) {
#pragma unroll
            for (int o = 4; o < 64; o <<= 1) s += __shfl_xor(s, o);
            if (lane == 0) wsumd[row] = s;
        } else if ((lane & 3) == 0) *(f32x2*)(sp + (size_t)row * 32 + (lane >> 2) * 2) = (f32x2){s, q};
    } else if (b >= 1392) {
        const int bb = b - 1392;
        const u32x4* p = (bb < 256 ? pf0 : bb < 512 ? pf1 : pf2) + (size_t)(bb & 255) * 2048 + tid;
        u32x4 v[8];
#pragma unroll
        for (int i = 0; i < 8; ++i) v[i] = p[i * 256];
#pragma unroll
        for (int i = 0; i < 8; ++i) asm volatile("" :: "v"(v[i]));
    } else {
#pragma unroll
        for (int i = 0; i < 4; ++i) xch[(size_t)(b - 1360) * 1024 + i * 256 + tid] = (u32x4){XCH_EMPTY, XCH_EMPTY, XCH_EMPTY, XCH_EMPTY};
    }
    STAMP_END(0);
}

extern "C" void kernel_launch(void* const* d_in, const int* in_sizes, int n_in, void* d_out, int out_size, void* d_ws, size_t ws_size, hipStream_t stream) {
    const u16* x0 = (const u16*)d_in[0];
    const u16* wsre = (const u16*)d_in[1];
    const u16* wsim = (const u16*)d_in[2];
    const u16* wdre = (const u16*)d_in[3];
    const u16* wdim = (const u16*)d_in[4];
    const u16* win = (const u16*)d_in[6];
    const u16* wsp = (const u16*)d_in[7];
    const u16* bsp = (const u16*)d_in[8];
    const u16* wout = (const u16*)d_in[9];
    char* ws = (char*)d_ws;
    constexpr size_t MB = 1 << 20;
    u16* T = (u16*)(ws); u16* xa = (u16*)(ws + 8 * MB); u16* xb = (u16*)(ws + 12 * MB);
    u16* wint = (u16*)(ws + 16 * MB); u16* woutt = (u16*)(ws + 24 * MB);
    u16* z1 = (u16*)(ws + 28 * MB); u16* z2t = (u16*)(ws + 36 * MB); u16* h = (u16*)(ws + 44 * MB);
    float* sp = (float*)(ws + 52 * MB); float* wsumd = (float*)(ws + 52 * MB + 256 * 1024); float* wsp_in = (float*)(ws + 52 * MB + 512 * 1024);
    unsigned* xch = (unsigned*)(ws + 53 * MB);

    auto g2 = k_gres<S_, 4096, 64, 32, true>;
    auto g5a = k_gres<FH, FH, FH / 64, 0, true>;
    auto g5b = k_gres<FH, FH, FH / 64, 0, false>;
    (void)hipFuncSetAttribute((const void*)k_g1, hipFuncAttributeMaxDynamicSharedMemorySize, G1_LDS);
    (void)hipFuncSetAttribute((const void*)g2, hipFuncAttributeMaxDynamicSharedMemorySize, GR_LDS);
    (void)hipFuncSetAttribute((const void*)g5a, hipFuncAttributeMaxDynamicSharedMemorySize, GR_LDS);
    (void)hipFuncSetAttribute((const void*)g5b, hipFuncAttributeMaxDynamicSharedMemorySize, GR_LDS);
    (void)hipFuncSetAttribute((const void*)k_g3, hipFuncAttributeMaxDynamicSharedMemorySize, G3_LDS);
    (void)hipFuncSetAttribute((const void*)k_g4, hipFuncAttributeMaxDynamicSharedMemorySize, G4_LDS);

    k_prep<<<K0_BLOCKS, 256, 0, stream>>>(x0, wdre, wdim, win, wout, wint, woutt, wsumd, wsp_in, sp, (u32x4*)xch, (const u32x4*)wsre, (const u32x4*)wsim, (const u32x4*)wsp KID(0));
    for (int l = 0; l < 2; ++l) {
        const u16* x = l == 0 ? x0 : xb;
        k_g1<<<256, 512, G1_LDS, stream>>>(wdre, wdim, x, sp, wsumd, T KID(1 + l * 5));
        g2<<<256, 512, GR_LDS, stream>>>(wsre, wsim, T, x, xa, sp KID(2 + l * 5));
        k_g3<<<256, 768, G3_LDS, stream>>>(xa, wint, sp, wsp_in, z1, z2t, xch + l * 65536 KID(3 + l * 5));
        k_g4<<<256, 512, G4_LDS, stream>>>(wsp, z2t, z1, bsp, h KID(4 + l * 5));
        if (l == 0) g5a<<<256, 512, GR_LDS, stream>>>(h, h, woutt, xa, xb, sp KID(5));
        else g5b<<<256, 512, GR_LDS, stream>>>(h, h, woutt, xa, (u16*)d_out, sp KID(10));
    }
}
```

```cpp
#include <hip/hip_runtime.h>
#include <hip/hip_bf16.h>
#include <utility>

typedef unsigned short u16;
typedef short bf16x8 __attribute__((ext_vector_type(8)));
typedef short s16x4 __attribute__((ext_vector_type(4)));
typedef float f32x2 __attribute__((ext_vector_type(2)));
typedef float f32x4 __attribute__((ext_vector_type(4)));
typedef unsigned u32x2 __attribute__((ext_vector_type(2)));
typedef unsigned u32x4 __attribute__((ext_vector_type(4)));
#define LAS __attribute__((address_space(3)))
#define GAS __attribute__((address_space(1)))
#define DEV __device__ __forceinline__

constexpr int waitimm(int vm, int lgkm) { return (vm & 15) | (7 << 4) | ((lgkm & 15) << 8) | ((vm >> 4) << 14); }
#define WAITCNT(vm, lgkm) __builtin_amdgcn_s_waitcnt(waitimm(vm, lgkm))
#define BAR() do { asm volatile("" ::: "memory"); __builtin_amdgcn_s_barrier(); asm volatile("" ::: "memory"); } while (0)
#define SB0() __builtin_amdgcn_sched_barrier(0)
#define STAMP(on, ph, i)
#define ST_PARAM
#define ST_PASS(p)

constexpr int S_ = 2048, D_ = 1024, F_ = 3584, FH = 1792;
#define POLL_SLEEP 2
#define XT0 3
#define XT1 5
#define XT2 10
#define WGATE 2
constexpr float LN_EPS = 1e-5f;
constexpr int SPIN_MAX = 1 << 17;
constexpr int STORE_AUX = 16;

DEV unsigned pk_bf16(float lo, float hi) { unsigned r; asm("v_cvt_pk_bf16_f32 %0, %1, %2" : "=v"(r) : "v"(lo), "v"(hi)); return r; }
DEV float bf_lo(unsigned v) { return __uint_as_float(v << 16); }
DEV float bf_hi(unsigned v) { return __uint_as_float(v & 0xffff0000u); }
DEV __amdgpu_buffer_rsrc_t rsrc(const void* p, unsigned bytes) { return __builtin_amdgcn_make_buffer_rsrc((void*)p, 0, bytes, 0x00020000); }
DEV f32x4 gelu_tanh(f32x4 x) {
    const float c0 = -2.3022081986f, c1 = c0 * 0.044715f;
    const f32x4 a = x * (x * x * c1 + c0);
    f32x4 d = (f32x4){__builtin_amdgcn_exp2f(a[0]), __builtin_amdgcn_exp2f(a[1]), __builtin_amdgcn_exp2f(a[2]), __builtin_amdgcn_exp2f(a[3])} + 1.0f;
    return x * (f32x4){__builtin_amdgcn_rcpf(d[0]), __builtin_amdgcn_rcpf(d[1]), __builtin_amdgcn_rcpf(d[2]), __builtin_amdgcn_rcpf(d[3])};
}
DEV u32x4 pk8(f32x4 a, f32x4 b) { return (u32x4){pk_bf16(a[0], a[1]), pk_bf16(a[2], a[3]), pk_bf16(b[0], b[1]), pk_bf16(b[2], b[3])}; }
DEV f32x4 unpk4(unsigned a, unsigned b) { return (f32x4){bf_lo(a), bf_hi(a), bf_lo(b), bf_hi(b)}; }
__host__ __device__ constexpr int perm32(int slot) { return 8 * ((slot & 15) >> 2) + 4 * ((slot >> 4) & 1) + (slot & 3); }
__host__ __device__ constexpr int inv32(int row) { return 16 * ((row >> 2) & 1) + 4 * ((row >> 3) & 3) + (row & 3); }
DEV float fq_sum(float a) {
    const auto p = __builtin_amdgcn_permlane16_swap(__float_as_uint(a), __float_as_uint(a), false, false);
    a = __uint_as_float(p[0]) + __uint_as_float(p[1]);
    const auto q = __builtin_amdgcn_permlane32_swap(__float_as_uint(a), __float_as_uint(a), false, false);
    return __uint_as_float(q[0]) + __uint_as_float(q[1]);
}
template <int CTRL> DEV float dpp_mov(float a) { return __builtin_bit_cast(float, __builtin_amdgcn_update_dpp(0, __builtin_bit_cast(int, a), CTRL, 0xf, 0xf, true)); }
template <int CTRL> DEV float dpp_add(float a) { return a + __builtin_bit_cast(float, __builtin_amdgcn_update_dpp(0, __builtin_bit_cast(int, a), CTRL, 0xf, 0xf, true)); }
DEV float row16_sum(float a) { a = dpp_add<0x128>(a); a = dpp_add<0x124>(a); a = dpp_add<0x122>(a); return dpp_add<0x121>(a); }
DEV float oct_sum(float a) { a = dpp_add<0xb1>(a); a = dpp_add<0x4e>(a); return dpp_add<0x141>(a); }
DEV float quad_sum(float a) { a = dpp_add<0xb1>(a); return dpp_add<0x4e>(a); }
constexpr unsigned XCH_EMPTY = 0xffffffffu;
DEV unsigned xch_word(float v) { const unsigned u = __float_as_uint(v); return u == XCH_EMPTY ? 0x7fc00000u : u; }

DEV int opaque_v(int v) { asm volatile("" : "+v"(v)); return v; }
DEV int opaque_s(int v) { asm volatile("" : "+s"(v)); return v; }
template <class F, int... I> DEV void static_for_impl(F&& f, std::integer_sequence<int, I...>) { (f(std::integral_constant<int, I>{}), ...); }
template <int N, class F> DEV void static_for(F&& f) { static_for_impl(f, std::make_integer_sequence<int, N>{}); }

template <int BM_, int BN_, int NSP_, int NSQ_, int LDP_, int LDQ_, int NT_, int KSW_, bool PERMP_, bool PERMQ_, bool WISP_> struct Cfg {
    static constexpr int BM = BM_, BN = BN_, NSP = NSP_, NSQ = NSQ_, LDP = LDP_, LDQ = LDQ_, NT = NT_, KSW = KSW_;
    static constexpr bool PERMP = PERMP_, PERMQ = PERMQ_, WISP = WISP_;
    static constexpr int PB = BM * 128, QB = BN * 128, QBASE = NSP * PB, RING = QBASE + NSQ * QB;
    static constexpr int LP = BM / 32, LQ = BN / 32;
    static constexpr int NSW = WISP ? NSP : NSQ, NSA = WISP ? NSQ : NSP, LW = WISP ? LP : LQ, LA = WISP ? LQ : LP;
    static constexpr int it(int i) { return (i + NSW < NT ? LW : 0) + (i + NSA < NT ? LA : 0); }
    static constexpr int younger(int t) {
        int n = t + 1 <= NSA - 1 ? (NSA - 2 - t) * LA : 0;
        for (int i = (t + 2 - NSA > 0 ? t + 2 - NSA : 0); i < t; ++i) n += it(i);
        return n;
    }
};
constexpr int RING_MAX = 147456;
constexpr int L_R = RING_MAX, L_M = L_R + 512, L_WS = L_M + 512, L_Z = L_WS + 1024, L_SYNC = L_Z + 1024, LDS_TOTAL = L_SYNC + 64;

template <class C, bool EAGER, class Dep, class T1, class T2>
DEV void loader_phase(LAS unsigned char* lds, const u16* P0, const u16* P1, const u16* Q, int lw, int lane, unsigned drained, Dep&& dep, T1&& tail_load, T2&& tail_finish ST_PARAM) {
    static_assert(C::RING <= RING_MAX && C::NSW >= C::NSA && C::NSA >= 3 && C::NT - C::NSW >= C::NSA - 2 && C::BM % 32 == 0 && !(C::PERMQ && C::BN % 32), "");
    lane = opaque_v(lane); lw = opaque_s(lw);
    const __amdgpu_buffer_rsrc_t rP0 = rsrc(P0, C::BM * C::LDP * 2), rP1 = rsrc(P1, C::BM * C::LDP * 2), rQ = rsrc(Q, C::BN * C::LDQ * 2);
    const int ch = ((lane & 7) ^ (((lw & 1) * 4 + (lane >> 4)) & 7)) * 16, row = lw * 8 + (lane >> 3);
    const int vP = (C::PERMP ? perm32(row) : row) * C::LDP * 2 + ch, vQ = (C::PERMQ ? perm32(row) : row) * C::LDQ * 2 + ch;
    auto issueP = [&](int slot, int kt) {
        LAS unsigned char* dst = lds + slot * C::PB + lw * 1024;
        const bool second = C::KSW && kt >= C::KSW;
        const __amdgpu_buffer_rsrc_t rP = second ? rP1 : rP0;
        const int kp = (second ? kt - C::KSW : kt) * 128;
#pragma unroll
        for (int i = 0; i < C::LP; ++i) __builtin_amdgcn_raw_ptr_buffer_load_lds(rP, (LAS void*)(dst + i * 4096), 16, vP, i * 32 * C::LDP * 2 + kp, 0, C::WISP ? 0 : 16);
    };
    auto issueQ = [&](int slot, int kt) {
        LAS unsigned char* dst = lds + C::QBASE + slot * C::QB + lw * 1024;
#pragma unroll
        for (int j = 0; j < (C::BN + 31) / 32; ++j)
            if (j < C::LQ || j * 4 + lw < C::BN / 8) __builtin_amdgcn_raw_ptr_buffer_load_lds(rQ, (LAS void*)(dst + j * 4096), 16, vQ, j * 32 * C::LDQ * 2 + kt * 128, 0, C::WISP ? 16 : 0);
    };
    auto issueW = [&](int slot, int kt) { if (C::WISP) issueP(slot, kt); else issueQ(slot, kt); };
    auto issueA = [&](int slot, int kt) { if (C::WISP) issueQ(slot, kt); else issueP(slot, kt); };
    if (drained) { while ((unsigned)__builtin_amdgcn_readfirstlane(*(volatile LAS unsigned*)(lds + L_SYNC + 16)) < drained) __builtin_amdgcn_s_sleep(2); }
#pragma unroll 1
    for (int s = 0; s < C::NSW; ++s) issueW(s, s);
    if (drained && lw == 0) { while ((unsigned)__builtin_amdgcn_readfirstlane(*(volatile LAS unsigned*)(lds + L_SYNC + 16)) < drained) __builtin_amdgcn_s_sleep(2); }
    dep();
    STAMP(ston, ph, 4);
    issueA(0, 0); issueA(1, 1);
    if constexpr (!EAGER) { WAITCNT(C::LA, 15); BAR(); }
#pragma unroll
    for (int s = 2; s < C::NSA; ++s) issueA(s, s);
    if constexpr (EAGER) { WAITCNT((C::NSA - 1) * C::LA, 15); BAR(); }
    auto step = [&](auto tc) {
        constexpr int t = decltype(tc)::value;
        WAITCNT(C::younger(t), 15);
        BAR();
        if constexpr (t + C::NSW < C::NT) issueW(t % C::NSW, t + C::NSW);
        if constexpr (t + C::NSA < C::NT) issueA(t % C::NSA, t + C::NSA);
        if constexpr (t + C::NSA == C::NT - 1) tail_load();
    };
    static_for<C::NSA - 2>(step);
    int sw = (C::NSA - 2) % C::NSW, sa = C::NSA - 2;
#pragma unroll 1
    for (int t = C::NSA - 2; t < C::NT - C::NSW; ++t) {
        WAITCNT((C::NSA - 2) * (C::LA + C::LW), 15);
        BAR();
        issueW(sw, t + C::NSW); issueA(sa, t + C::NSA);
        sw = (sw + 1 == C::NSW) ? 0 : sw + 1; sa = (sa + 1 == C::NSA) ? 0 : sa + 1;
    }
    if constexpr (C::NSW == C::NSA) tail_load();
    static_for<C::NSW - 1>([&](auto ic) { step(std::integral_constant<int, C::NT - C::NSW + decltype(ic)::value>{}); });
    tail_finish();
    WAITCNT(63, 0);
    BAR();
}

template <class C, int MT, int NTL, int NSWAP, bool ROLL, int RNUM, int RDEN, class Hook>
DEV void compute_phase(LAS unsigned char* lds, int prow0, int qrow0, int lane, f32x4 (&acc)[MT][NTL], Hook&& hook ST_PARAM) {
    constexpr int NR = MT + NTL, NM = MT * NTL, NT = C::NT;
    const int fr = lane & 15, fq = lane >> 4;
    int aoff[2], boff[2];
#pragma unroll
    for (int k = 0; k < 2; ++k) {
        const int ch = ((k * 4 + fq) ^ ((fr >> 1) & 7)) * 16;
        aoff[k] = (prow0 + fr) * 128 + ch;
        boff[k] = C::QBASE + (qrow0 + fr) * 128 + ch;
    }
#pragma unroll
    for (int m = 0; m < MT; ++m)
#pragma unroll
        for (int n = 0; n < NTL; ++n) { acc[m][n] = (f32x4){0.f, 0.f, 0.f, 0.f}; asm volatile("" : "+v"(acc[m][n])); }
    auto mfma = [&](int m, int n, const bf16x8& a, const bf16x8& b) {
        if (n >= NSWAP) asm volatile("v_mfma_f32_16x16x32_bf16 %0, %1, %2, %0" : "+v"(acc[m][n]) : "v"(a), "v"(b));
        else asm volatile("v_mfma_f32_16x16x32_bf16 %0, %1, %2, %0" : "+v"(acc[m][n]) : "v"(b), "v"(a));
    };
    int sp = 0, sq = 0;
    auto next = [&]() { sp = (sp + C::PB == C::NSP * C::PB) ? 0 : sp + C::PB; sq = (sq + C::QB == C::NSQ * C::QB) ? 0 : sq + C::QB; };
    BAR();
    STAMP(ston, ph, 1);
    if constexpr (!ROLL) {
        bf16x8 fa[4][MT], fb[4][NTL];
        auto rd1 = [&](int set, int k, int i) {
            if (i == 0) fa[set][0] = *(const LAS bf16x8*)(lds + sp + aoff[k]);
            else if (i <= NTL) fb[set][i - 1] = *(const LAS bf16x8*)(lds + sq + boff[k] + (i - 1) * 2048);
            else fa[set][i - NTL] = *(const LAS bf16x8*)(lds + sp + aoff[k] + (i - NTL) * 2048);
        };
        auto stg = [&](int set, bool rd) {
#pragma unroll
            for (int i = 0; i < 2 * NM; ++i) {
                const int st = set + i / NM, m = (i % NM) / NTL, n = i % NTL;
                mfma(m, n, fa[st][m], fb[st][n]); SB0();
                if (rd) {
#pragma unroll
                    for (int r = (i * RNUM + RDEN - 1) / RDEN; r < ((i + 1) * RNUM + RDEN - 1) / RDEN; ++r) if (r < 2 * NR) rd1((set ^ 2) + r / NR, r / NR, r % NR);
                    SB0();
                }
            }
        };
        static_assert((2 * NM * RNUM + RDEN - 1) / RDEN >= 2 * NR && NT % 2 == 0, "");
#pragma unroll
        for (int r = 0; r < 2 * NR; ++r) rd1(r / NR, r / NR, r % NR);
#pragma unroll 1
        for (int t = 0; t < NT - 2; t += 2) {
            next(); WAITCNT(63, 0); BAR(); SB0();
            stg(0, true);
            next(); WAITCNT(63, 0); BAR(); SB0();
            stg(2, true);
        }
        next();
        stg(0, false);
        BAR(); SB0();
#pragma unroll
        for (int r = 0; r < 2 * NR; ++r) rd1(2 + r / NR, r / NR, r % NR);
        WAITCNT(63, 0); BAR(); SB0();
        stg(2, false);
    } else {
        static_assert(MT == 2, "");
        constexpr int G = RNUM;
        bf16x8 fa[2][2], fb[NTL];
        auto rda = [&](int set, int k, int m) { fa[set][m] = *(const LAS bf16x8*)(lds + sp + aoff[k] + m * 2048); };
        auto rdb = [&](int k, int n) { fb[n] = *(const LAS bf16x8*)(lds + sq + boff[k] + n * 2048); };
        auto half0 = [&]() {
#pragma unroll
            for (int j = 0; j < NM; ++j) {
                mfma(j & 1, j >> 1, fa[0][j & 1], fb[j >> 1]); SB0();
                if (j & 1) rdb(1, j >> 1); else if (j < 4) rda(1, 1, j >> 1);
                SB0();
            }
        };
        auto half1 = [&](bool rd) {
#pragma unroll
            for (int j = 0; j < NM; ++j) {
                if (j == G) { next(); WAITCNT(63, 0); BAR(); SB0(); }
                mfma(j & 1, j >> 1, fa[1][j & 1], fb[j >> 1]); SB0();
                if (rd && j >= G) {
                    if (j < G + 2) rda(0, 0, j - G);
#pragma unroll
                    for (int n = 0; n < NTL; ++n) if ((G + 2 + n > 2 * n + 1 ? G + 2 + n : 2 * n + 1) == j) rdb(0, n);
                    SB0();
                }
            }
        };
        rda(0, 0, 0); rda(0, 0, 1);
#pragma unroll
        for (int n = 0; n < NTL; ++n) rdb(0, n);
        SB0();
        static_for<4>([&](auto ic) {
            constexpr int i = decltype(ic)::value, ends[5] = {0, XT0, XT1, XT2, NT - 1};
#pragma unroll 1
            for (int t = ends[i]; t < ends[i + 1]; ++t) { half0(); half1(true); }
            hook(ic); SB0();
        });
        half0(); half1(false);
    }
    asm volatile("s_nop 15" ::: "memory");
#pragma unroll
    for (int m = 0; m < MT; ++m)
#pragma unroll
        for (int n = 0; n < NTL; ++n) asm volatile("" : "+v"(acc[m][n]));
    STAMP(ston, ph, 2);
}

DEV void dep_wait(const unsigned* box, int nprod, LAS unsigned char* lds, unsigned epoch, int lw, int lane) {
    volatile LAS unsigned* flag = (volatile LAS unsigned*)(lds + L_SYNC);
    if (lw == 0) {
#pragma unroll 1
        for (int spin = 0; spin < SPIN_MAX; ++spin) {
            const unsigned v = __hip_atomic_load((const GAS unsigned*)box + (lane & (nprod - 1)), __ATOMIC_RELAXED, __HIP_MEMORY_SCOPE_AGENT);
            if (__all(v != 0u)) break;
            __builtin_amdgcn_s_sleep(POLL_SLEEP);
        }
        *flag = epoch;
    } else {
        while ((unsigned)__builtin_amdgcn_readfirstlane(*flag) < epoch) __builtin_amdgcn_s_sleep(1);
    }
    asm volatile("" ::: "memory");
}
DEV void dep_signal(unsigned* boxes, int word, int ncons, LAS unsigned char* lds, int lane) {
    asm volatile("" ::: "memory");
    if (lane == 0) __hip_atomic_fetch_add((LAS unsigned*)(lds + L_SYNC + 32), 1u, __ATOMIC_RELAXED, __HIP_MEMORY_SCOPE_WORKGROUP);
    asm volatile("s_waitcnt vmcnt(0)" ::: "memory");
    unsigned old = 0;
    if (lane == 0) old = __hip_atomic_fetch_add((LAS unsigned*)(lds + L_SYNC + 16), 1u, __ATOMIC_RELAXED, __HIP_MEMORY_SCOPE_WORKGROUP);
    if ((__builtin_amdgcn_readfirstlane(old) & 3) == 3 && lane < ncons) __hip_atomic_store((GAS unsigned*)boxes + word, 1u, __ATOMIC_RELAXED, __HIP_MEMORY_SCOPE_AGENT);
}

DEV void row_tables_load(const float* sp, int tid, f32x4 (&v)[4]) {
    const __amdgpu_buffer_rsrc_t r = rsrc(sp, 128 * 128);
#pragma unroll
    for (int i = 0; i < 4; ++i) v[i] = __builtin_bit_cast(f32x4, __builtin_amdgcn_raw_buffer_load_b128(r, i * 4096 + tid * 16, 0, 16));
}
DEV void row_tables_finish(LAS unsigned char* lds, int tid, const f32x4 (&v)[4], float sign) {
#pragma unroll
    for (int i = 0; i < 4; ++i) {
        const float s = oct_sum(v[i][0] + v[i][2]), q = oct_sum(v[i][1] + v[i][3]);
        const float mean = s * (1.0f / D_), var = fmaxf(q * (1.0f / D_) - mean * mean, 0.f), r = sign * __builtin_amdgcn_rsqf(var + LN_EPS);
        if ((tid & 7) == 0) { ((LAS float*)(lds + L_R))[i * 32 + (tid >> 3)] = r; ((LAS float*)(lds + L_M))[i * 32 + (tid >> 3)] = -mean * r; }
    }
}

constexpr size_t MB = 1 << 20;
constexpr size_t W_T = 0, W_XA = 16 * MB, W_XB = 24 * MB, W_Z2T = 28 * MB, W_H = 44 * MB, W_WINT = 60 * MB, W_WOUTT = 68 * MB;
constexpr size_t W_SP = 72 * MB, W_WSUMD = 74 * MB, W_WSPIN = 74 * MB + 65536, W_XCH = 75 * MB, W_CTR = 75 * MB + 512 * 1024;

#define G1N 4, 4
#define G2N 4, 4
#define G4N 4, 4
#define G5N 4, 4
typedef Cfg<128, 128, G1N, D_, D_, D_ / 64, 0, false, true, true> CG1;
typedef Cfg<128, 64, G2N, S_, 4096, 64, 32, false, false, true> CG2;
typedef Cfg<128, 112, 4, 4, D_, D_, D_ / 64, 0, true, false, false> CG3;
typedef Cfg<128, 112, G4N, S_, S_, S_ / 64, 0, true, false, true> CG4;
typedef Cfg<128, 64, G5N, FH, FH, FH / 64, 0, false, false, false> CG5;

template <class C>
DEV void res_phase(LAS unsigned char* lds, const u16* res, u16* out, float* sp, int tm, int tn, int w, int lane ST_PARAM) {
    const int fr = lane & 15, fq = lane >> 4;
    const __amdgpu_buffer_rsrc_t rX = rsrc(res + (size_t)(tm * 128 + w * 32) * D_ + tn * 64, 32 * D_ * 2 - tn * 128);
    u32x4 xr[2][2];
#pragma unroll
    for (int m = 0; m < 2; ++m)
#pragma unroll
        for (int h = 0; h < 2; ++h) xr[m][h] = __builtin_amdgcn_raw_buffer_load_b128(rX, (m * 16 + fr) * D_ * 2 + h * 64 + fq * 16, 0, 16);
    f32x4 acc[2][4];
    compute_phase<C, 2, 4, 4, false, 1, 1>(lds, w * 32, 0, lane, acc, [](int) {} ST_PASS(ph));
    const __amdgpu_buffer_rsrc_t rC = rsrc(out + (size_t)(tm * 128 + w * 32) * D_ + tn * 64, 32 * D_ * 2 - tn * 128);
    const __amdgpu_buffer_rsrc_t rS = rsrc(sp + (size_t)(tm * 128 + w * 32) * 32, 32 * 128);
#pragma unroll
    for (int m = 0; m < 2; ++m) {
        f32x4 s = (f32x4){0.f, 0.f, 0.f, 0.f}, qq = s;
#pragma unroll
        for (int h = 0; h < 2; ++h) {
            const u32x4 o = pk8(acc[m][2 * h] + unpk4(xr[m][h][0], xr[m][h][1]), acc[m][2 * h + 1] + unpk4(xr[m][h][2], xr[m][h][3]));
            __builtin_amdgcn_raw_buffer_store_b128(o, rC, (m * 16 + fr) * D_ * 2 + h * 64 + fq * 16, 0, STORE_AUX);
            const f32x4 a = unpk4(o[0], o[1]), b = unpk4(o[2], o[3]);
            s += a + b; qq += a * a + b * b;
        }
        const float st = fq_sum((s[0] + s[1]) + (s[2] + s[3])), qt = fq_sum((qq[0] + qq[1]) + (qq[2] + qq[3]));
        if (fq == 0) __builtin_amdgcn_raw_buffer_store_b64((u32x2){__float_as_uint(st), __float_as_uint(qt)}, rS, (m * 16 + fr) * 128 + tn * 8, 0, 16);
    }
}

__global__ __launch_bounds__(512, 2) void k_main(const u16* __restrict__ x0, const u16* __restrict__ wsre, const u16* __restrict__ wsim, const u16* __restrict__ wdre,
                                                 const u16* __restrict__ wdim, const u16* __restrict__ wsp, const u16* __restrict__ bsp, char* ws, u16* out) {
    extern __shared__ __attribute__((aligned(1024))) unsigned char shm[];
    LAS unsigned char* lds = (LAS unsigned char*)shm;
    const int tid = threadIdx.x, lane0 = tid & 63, w0 = __builtin_amdgcn_readfirstlane(tid >> 6);
    const int tm0 = ((blockIdx.x & 7) >> 1) * 4 + (blockIdx.x >> 6), tn0 = (blockIdx.x & 1) * 8 + ((blockIdx.x >> 3) & 7);
#define PHASE_VARS() const int lane = opaque_v(lane0), w = opaque_s(w0), tm = opaque_s(tm0), tn = opaque_s(tn0), fr = lane & 15, fq = lane >> 4; (void)fr; (void)fq; (void)w
    unsigned* ctr = (unsigned*)(ws + W_CTR);
    if (w0 >= 4) {
        const int lw = w0 - 4;
        auto none = [&]() {};
#pragma unroll 1
        for (int l = 0; l < 2; ++l) {
            const u16* x = l == 0 ? x0 : (const u16*)(ws + W_XB);
            const u16* T = (const u16*)(ws + W_T + l * 8 * MB); const u16* xa = (const u16*)(ws + W_XA + l * 4 * MB);
            const u16* z2t = (const u16*)(ws + W_Z2T + l * 8 * MB); const u16* h = (const u16*)(ws + W_H + l * 8 * MB);
            const float* sp = (const float*)(ws + W_SP + l * 512 * 1024);
            const unsigned* c = ctr + l * 5 * 8192;
            f32x4 v[4], u[4];
            { PHASE_VARS(); const int lt = (w - 4) * 64 + lane;
            const u16* pw = (tm < 8 ? wdre : wdim) + (size_t)(tm & 7) * 128 * D_;
            loader_phase<CG1, false>(lds, pw, pw, x + (size_t)tn * 128 * D_, lw, lane, l * 20, [&]() { if (l) dep_wait(ctr + 4 * 8192 + (tm * 16 + tn) * 32, 16, lds, 5, lw, lane); },
                              [&]() { row_tables_load(sp + tn * 128 * 32, lt, v); }, [&]() { row_tables_finish(lds, lt, v, tm < 8 ? 1.0f : -1.0f); } ST_PASS(l * 5 + 1)); }
            { PHASE_VARS();
            loader_phase<CG2, false>(lds, wsre + (size_t)tm * 128 * S_, wsim + (size_t)tm * 128 * S_, T + (size_t)tn * 64 * 4096, lw, lane, l * 20 + 4,
                              [&]() { dep_wait(c + (tm * 16 + tn) * 32, 32, lds, l * 5 + 1, lw, lane); }, none, none ST_PASS(l * 5 + 2)); }
            { PHASE_VARS(); const int lt = (w - 4) * 64 + lane;
            loader_phase<CG3, false>(lds, xa + (size_t)tm * 128 * D_, xa, (const u16*)(ws + W_WINT) + (size_t)(tn * 224 + 112) * D_, lw, lane, l * 20 + 8,
                              [&]() { dep_wait(c + 8192 + (tm * 16 + tn) * 32, 16, lds, l * 5 + 2, lw, lane); },
                              [&]() { row_tables_load(sp + 65536 + tm * 128 * 32, lt, v);
                                      const __amdgpu_buffer_rsrc_t r = rsrc(ws + W_WSPIN + (size_t)tn * 224 * 64, 224 * 64);
#pragma unroll
                                      for (int i = 0; i < 4; ++i) u[i] = __builtin_bit_cast(f32x4, __builtin_amdgcn_raw_buffer_load_b128(r, i * 4096 + lt * 16, 0, 0)); },
                              [&]() { row_tables_finish(lds, lt, v, 1.0f);
#pragma unroll
                                      for (int i = 0; i < 4; ++i) { const float s = quad_sum((u[i][0] + u[i][1]) + (u[i][2] + u[i][3])); if ((lt & 3) == 0) ((LAS float*)(lds + L_WS))[i * 64 + (lt >> 2)] = s; } }
                              ST_PASS(l * 5 + 3)); }
            { PHASE_VARS();
            loader_phase<CG3, true>(lds, xa + (size_t)tm * 128 * D_, xa, (const u16*)(ws + W_WINT) + (size_t)tn * 224 * D_, lw, lane, 0, none, none, none ST_PASS(11 + l)); }
            { PHASE_VARS();
            loader_phase<CG4, true>(lds, wsp + (size_t)tm * 128 * S_, wsp, z2t + (size_t)tn * 112 * S_, lw, lane, l * 20 + 12, [&]() { dep_wait(c + 2 * 8192 + (tm * 16 + tn) * 32, 16, lds, l * 5 + 3, lw, lane); }, none, none ST_PASS(l * 5 + 4)); }
            { PHASE_VARS();
            loader_phase<CG5, false>(lds, h + (size_t)tm * 128 * FH, h, (const u16*)(ws + W_WOUTT) + (size_t)tn * 64 * FH, lw, lane, l * 20 + 16,
                              [&]() { dep_wait(c + 3 * 8192 + (tm * 16 + tn) * 32, 16, lds, l * 5 + 4, lw, lane); }, none, none ST_PASS(l * 5 + 5)); }
        }
        return;
    }
    if (tid < 16) *(LAS unsigned*)(lds + L_SYNC + tid * 4) = 0u;
    WAITCNT(63, 0);
    const LAS float* R = (const LAS float*)(lds + L_R); const LAS float* M = (const LAS float*)(lds + L_M); const LAS float* WS = (const LAS float*)(lds + L_WS);
#pragma unroll 1
    for (int l = 0; l < 2; ++l) {
        const u16* x = l == 0 ? x0 : (const u16*)(ws + W_XB);
        u16* xa = (u16*)(ws + W_XA + l * 4 * MB);
        float* sp = (float*)(ws + W_SP + l * 512 * 1024);
        unsigned* c = ctr + l * 5 * 8192;
        {
            PHASE_VARS();
            const int wm = w >> 1, wn = w & 1;
            float wsum[4];
#pragma unroll
            for (int m = 0; m < 4; ++m) wsum[m] = ((const float*)(ws + W_WSUMD))[tm * 128 + wm * 64 + m * 16 + fr];
            f32x4 acc[4][4];
            compute_phase<CG1, 4, 4, 4, false, 2, 3>(lds, wm * 64, wn * 64, lane, acc, [](int) {} ST_PASS(l * 5 + 1));
            const __amdgpu_buffer_rsrc_t rC = rsrc((u16*)(ws + W_T + l * 8 * MB) + (size_t)(tm & 7) * 128 * 4096 + (tm < 8 ? 0 : 2048) + tn * 128, 128 * 4096 * 2 - (2048 + tn * 128) * 2);
#pragma unroll
            for (int q = 0; q < 2; ++q) {
                const int cc = wn * 64 + q * 32 + fq * 8;
                const f32x4 r0 = *(const LAS f32x4*)(R + cc), r1 = *(const LAS f32x4*)(R + cc + 4), m0 = *(const LAS f32x4*)(M + cc), m1 = *(const LAS f32x4*)(M + cc + 4);
#pragma unroll
                for (int m = 0; m < 4; ++m) {
                    const int row = wm * 64 + (m >> 1) * 32 + inv32((m & 1) * 16 + fr);
                    __builtin_amdgcn_raw_buffer_store_b128(pk8(acc[m][2 * q] * r0 + m0 * wsum[m], acc[m][2 * q + 1] * r1 + m1 * wsum[m]), rC, row * 8192 + cc * 2, 0, STORE_AUX);
                }
            }
            dep_signal(c, ((lane >> 1) * 16 + (tm & 7) * 2 + (lane & 1)) * 32 + (tm >> 3) * 16 + tn, 32, lds, lane);
            STAMP(true, l * 5 + 1, 8 + w);
        }
        {
            PHASE_VARS();
            res_phase<CG2>(lds, x, xa, sp + 65536, tm, tn, w, lane ST_PASS(l * 5 + 2));
            dep_signal(c + 8192, (tm * 16 + lane) * 32 + tn, 16, lds, lane);
            STAMP(true, l * 5 + 2, 8 + w);
        }
        u32x4 zz[7];
        {
            PHASE_VARS();
            f32x4 z2[2][7];
            compute_phase<CG3, 2, 7, 0, false, 4, 5>(lds, w * 32, 0, lane, z2, [](int) {} ST_PASS(l * 5 + 3));
            f32x4 s[2], q[2];
#pragma unroll
            for (int m = 0; m < 2; ++m) {
                const f32x4 r = *(const LAS f32x4*)(R + w * 32 + fq * 8 + m * 4), mr = *(const LAS f32x4*)(M + w * 32 + fq * 8 + m * 4);
                s[m] = (f32x4){0.f, 0.f, 0.f, 0.f}; q[m] = s[m];
#pragma unroll
                for (int n = 0; n < 7; ++n) {
                    const f32x4 z = gelu_tanh(z2[m][n] * r + mr * WS[112 + n * 16 + fr]);
                    z2[m][n] = z; s[m] += z; q[m] += z * z;
                }
            }
            float v8[8], v4[4], v2[2];
#pragma unroll
            for (int i = 0; i < 8; ++i) {
                const float lo = (i & 1) ? q[0][i >> 1] : s[0][i >> 1], hi = (i & 1) ? q[1][i >> 1] : s[1][i >> 1];
                v8[i] = ((fr & 8) ? hi : lo) + dpp_mov<0x128>((fr & 8) ? lo : hi);
            }
#pragma unroll
            for (int i = 0; i < 4; ++i) v4[i] = ((fr & 4) ? v8[i + 4] : v8[i]) + dpp_mov<0x141>(dpp_mov<0x1b>((fr & 4) ? v8[i] : v8[i + 4]));
#pragma unroll
            for (int i = 0; i < 2; ++i) v2[i] = ((fr & 2) ? v4[i + 2] : v4[i]) + dpp_mov<0x4e>((fr & 2) ? v4[i] : v4[i + 2]);
            const float pv = ((fr & 1) ? v2[1] : v2[0]) + dpp_mov<0xb1>((fr & 1) ? v2[0] : v2[1]);
            const __amdgpu_buffer_rsrc_t rX = rsrc(ws + W_XCH + l * 262144 + (size_t)(tm * 4 + w) * 4096, 4096);
            __builtin_amdgcn_raw_buffer_store_b32(xch_word(pv), rX, tn * 256 + lane * 4, 0, 16);
            STAMP(ston, l * 5 + 3, 5);
            u32x4 g[4];
            auto hook = [&](auto ic) {
                constexpr int t = decltype(ic)::value;
                if constexpr (t == 0) {
#pragma unroll
                    for (int i = 0; i < 4; ++i) g[i] = __builtin_amdgcn_raw_buffer_load_b128(rX, lane * 16 + i * 1024, 0, 16);
                } else if constexpr (t == 1) {
                    f32x4 tt;
#pragma unroll 1
                    for (int spin = 0;; ++spin) {
                        bool ok = true; tt = (f32x4){0.f, 0.f, 0.f, 0.f};
#pragma unroll
                        for (int i = 0; i < 4; ++i) {
                            ok &= (g[i][0] != XCH_EMPTY) & (g[i][1] != XCH_EMPTY) & (g[i][2] != XCH_EMPTY) & (g[i][3] != XCH_EMPTY);
                            tt += __builtin_bit_cast(f32x4, g[i]);
                        }
                        if (__all(ok) || spin > SPIN_MAX) break;
#pragma unroll
                        for (int i = 0; i < 4; ++i) g[i] = __builtin_amdgcn_raw_buffer_load_b128(rX, lane * 16 + i * 1024, 0, 16);
                    }
                    const float mean0 = fq_sum(tt[0]) * (1.0f / FH), mean1 = fq_sum(tt[2]) * (1.0f / FH);
                    const float r0 = __builtin_amdgcn_rsqf(fmaxf(fq_sum(tt[1]) * (1.0f / FH) - mean0 * mean0, 0.f) + LN_EPS);
                    const float r1 = __builtin_amdgcn_rsqf(fmaxf(fq_sum(tt[3]) * (1.0f / FH) - mean1 * mean1, 0.f) + LN_EPS);
                    LAS float* Z = (LAS float*)(lds + L_Z) + w * 64;
                    if (lane < 16) { *(LAS f32x2*)(Z + lane * 2) = (f32x2){r0, r1}; *(LAS f32x2*)(Z + 32 + lane * 2) = (f32x2){-mean0 * r0, -mean1 * r1}; }
                    WAITCNT(63, 0);
                    const f32x4 ra = *(const LAS f32x4*)(Z + fq * 8), rb = *(const LAS f32x4*)(Z + fq * 8 + 4), ma = *(const LAS f32x4*)(Z + 32 + fq * 8), mb = *(const LAS f32x4*)(Z + 32 + fq * 8 + 4);
                    const __amdgpu_buffer_rsrc_t rC = rsrc((u16*)(ws + W_Z2T + l * 8 * MB) + (size_t)tn * 112 * S_ + tm * 128 + w * 32, 112 * S_ * 2 - tm * 256 - w * 64);
#pragma unroll
                    for (int n = 0; n < 7; ++n)
                        __builtin_amdgcn_raw_buffer_store_b128(pk8(z2[0][n] * ra + ma, z2[1][n] * rb + mb), rC, (n * 16 + fr) * S_ * 2 + fq * 16, 0, STORE_AUX);
                    STAMP(ston, l * 5 + 3, 7);
                } else if constexpr (t == 2) {
                    dep_signal(c + 2 * 8192, (lane * 16 + tn) * 32 + tm, 16, lds, lane);
                    STAMP(true, l * 5 + 3, 8 + w);
                }
            };
            f32x4 acc[2][7];
            compute_phase<CG3, 2, 7, 7, true, 4, 1>(lds, w * 32, 0, lane, acc, hook ST_PASS(11 + l));
            const float r0 = R[w * 32 + perm32(fr)], r1 = R[w * 32 + perm32(16 + fr)], m0 = M[w * 32 + perm32(fr)], m1 = M[w * 32 + perm32(16 + fr)];
#pragma unroll
            for (int n = 0; n < 7; ++n) {
                const f32x4 wv = *(const LAS f32x4*)(WS + n * 16 + fq * 4);
                zz[n] = pk8(gelu_tanh(acc[0][n] * r0 + wv * m0), gelu_tanh(acc[1][n] * r1 + wv * m1));
                asm volatile("" : "+v"(zz[n]));
            }
            STAMP(ston, l * 5 + 3, 6);
        }
        {
            PHASE_VARS();
            float b[2];
#pragma unroll
            for (int m = 0; m < 2; ++m) b[m] = bf_lo(bsp[tm * 128 + w * 32 + perm32(m * 16 + fr)]);
            f32x4 acc[2][7];
            compute_phase<CG4, 2, 7, 7, false, 4, 5>(lds, w * 32, 0, lane, acc, [](int) {} ST_PASS(l * 5 + 4));
            const __amdgpu_buffer_rsrc_t rC = rsrc((u16*)(ws + W_H + l * 8 * MB) + (size_t)(tm * 128 + w * 32) * FH, 32 * FH * 2);
#pragma unroll
            for (int m = 0; m < 2; ++m) {
                const int ro = perm32(m * 16 + fr) * FH * 2;
#pragma unroll
                for (int q = 0; q < 3; ++q)
                    __builtin_amdgcn_raw_buffer_store_b128(pk8(unpk4(zz[2 * q][m * 2], zz[2 * q][m * 2 + 1]) * (acc[m][2 * q] + b[m]), unpk4(zz[2 * q + 1][m * 2], zz[2 * q + 1][m * 2 + 1]) * (acc[m][2 * q + 1] + b[m])),
                                                           rC, ro + q * 64 + fq * 16, tn * 192, STORE_AUX);
                const f32x4 v = unpk4(zz[6][m * 2], zz[6][m * 2 + 1]) * (acc[m][6] + b[m]);
                __builtin_amdgcn_raw_buffer_store_b64((u32x2){pk_bf16(v[0], v[1]), pk_bf16(v[2], v[3])}, rC, ro + 3072 + fq * 8, tn * 32, STORE_AUX);
            }
            dep_signal(c + 3 * 8192, (tm * 16 + lane) * 32 + tn, 16, lds, lane);
            STAMP(true, l * 5 + 4, 8 + w);
        }
        {
            PHASE_VARS();
            res_phase<CG5>(lds, xa, l == 0 ? (u16*)(ws + W_XB) : out, sp + 131072, tm, tn, w, lane ST_PASS(l * 5 + 5));
            dep_signal(c + 4 * 8192, (lane * 16 + tm) * 32 + tn, 16, lds, lane);
            STAMP(true, l * 5 + 5, 8 + w);
        }
    }
}

constexpr int K0_BLOCKS = 1392 + 768;
__global__ __launch_bounds__(256) void k_prep(const u16* __restrict__ x, const u16* __restrict__ wre, const u16* __restrict__ wim, const u16* __restrict__ win, const u16* __restrict__ wout,
                                              u16* __restrict__ wint, u16* __restrict__ woutt, float* __restrict__ wsumd, float* __restrict__ wsp, float* __restrict__ sp, u32x4* __restrict__ xch,
                                              const u32x4* __restrict__ pf0, const u32x4* __restrict__ pf1, const u32x4* __restrict__ pf2) {
    __shared__ __attribute__((aligned(16))) unsigned char tile[4 * 64 * 144];
    const int tid = threadIdx.x, lane = tid & 63, w = tid >> 6, b = blockIdx.x;
    if (b < 336) {
        const bool isin = b < 224;
        const int bb = isin ? b : b - 224, nbn = isin ? 56 : 16, ld = isin ? F_ : D_, ldo = isin ? D_ : FH;
        const int k0 = (bb / nbn) * 256 + w * 64, n0 = (bb % nbn) * 64;
        const u16* src = (isin ? win : wout) + (size_t)k0 * ld + n0;
        LAS unsigned char* t = (LAS unsigned char*)tile + w * 64 * 144;
        u32x4 v[8];
#pragma unroll
        for (int i = 0; i < 8; ++i) v[i] = *(const u32x4*)(src + (size_t)(i * 8 + (lane >> 3)) * ld + (lane & 7) * 8);
#pragma unroll
        for (int i = 0; i < 8; ++i) *(LAS u32x4*)(t + (i * 8 + (lane >> 3)) * 144 + (lane & 7) * 16) = v[i];
        __syncthreads();
        const int g = lane >> 4, q = (lane & 15) >> 2, p = lane & 3;
        const __amdgpu_buffer_rsrc_t rO = rsrc(isin ? wint : woutt, isin ? F_ * D_ * 2 : D_ * FH * 2);
#pragma unroll
        for (int nb = 0; nb < 4; ++nb) {
            const int n = n0 + nb * 16 + (lane & 15);
            int orow = (n & ~31) + inv32(n & 31);
            if (isin) { const int half = n >= FH, nn = n - half * FH, tl = nn / 112, c = nn - tl * 112; orow = tl * 224 + half * 112 + (c < 96 ? (c & ~31) + inv32(c & 31) : c); }
            float s = 0.f;
#pragma unroll
            for (int hh = 0; hh < 2; ++hh) {
                const s16x4 a = __builtin_amdgcn_ds_read_tr16_b64_v4i16((LAS s16x4*)(t + (hh * 32 + g * 8 + q) * 144 + nb * 32 + p * 8));
                const s16x4 c = __builtin_amdgcn_ds_read_tr16_b64_v4i16((LAS s16x4*)(t + (hh * 32 + g * 8 + 4 + q) * 144 + nb * 32 + p * 8));
                const u32x2 a2 = __builtin_bit_cast(u32x2, a), c2 = __builtin_bit_cast(u32x2, c);
                int kk = k0 + hh * 32 + g * 8;
                if (!isin) { const int tl = kk / 112, cc = kk - tl * 112; kk = cc < 96 ? tl * 96 + cc : 1536 + tl * 16 + cc - 96; }
                __builtin_amdgcn_raw_buffer_store_b128((u32x4){a2[0], a2[1], c2[0], c2[1]}, rO, (orow * ldo + kk) * 2, 0, STORE_AUX);
                s += (bf_lo(a2[0]) + bf_hi(a2[0])) + (bf_lo(a2[1]) + bf_hi(a2[1])) + (bf_lo(c2[0]) + bf_hi(c2[0])) + (bf_lo(c2[1]) + bf_hi(c2[1]));
            }
            s += __shfl_xor(s, 16); s += __shfl_xor(s, 32);
            if (isin && lane < 16) wsp[(size_t)orow * 16 + (k0 >> 6)] = s;
        }
    } else if (b < 1360) {
        const bool isw = b < 848;
        const int row = (isw ? b - 336 : b - 848) * 4 + w;
        const u16* src = isw ? (row < 1024 ? wre + (size_t)row * D_ : wim + (size_t)(row - 1024) * D_) : x + (size_t)row * D_;
        const u32x4 a = *(const u32x4*)(src + lane * 8), c = *(const u32x4*)(src + 512 + lane * 8);
        float s = 0.f, q = 0.f;
#pragma unroll
        for (int j = 0; j < 4; ++j) {
            const float a0 = bf_lo(a[j]), a1 = bf_hi(a[j]), c0 = bf_lo(c[j]), c1 = bf_hi(c[j]);
            s += (a0 + a1) + (c0 + c1); q += (a0 * a0 + a1 * a1) + (c0 * c0 + c1 * c1);
        }
        s += __shfl_xor(s, 1); s += __shfl_xor(s, 2); q += __shfl_xor(q, 1); q += __shfl_xor(q, 2);
        if (isw) {
#pragma unroll
            for (int o = 4; o < 64; o <<= 1) s += __shfl_xor(s, o);
            if (lane == 0) wsumd[row] = s;
        } else if ((lane & 3) == 0) *(f32x2*)(sp + (size_t)row * 32 + (lane >> 2) * 2) = (f32x2){s, q};
    } else if (b >= 1392) {
        const int bb = b - 1392;
        const u32x4* p = (bb < 256 ? pf0 : bb < 512 ? pf1 : pf2) + (size_t)(bb & 255) * 2048 + tid;
        u32x4 v[8];
#pragma unroll
        for (int i = 0; i < 8; ++i) v[i] = p[i * 256];
#pragma unroll
        for (int i = 0; i < 8; ++i) asm volatile("" :: "v"(v[i]));
    } else {
#pragma unroll
        for (int i = 0; i < 4; ++i) xch[(size_t)(b - 1360) * 1024 + i * 256 + tid] = (u32x4){XCH_EMPTY, XCH_EMPTY, XCH_EMPTY, XCH_EMPTY};
        #pragma unroll
        for (int i = 0; i < 3; ++i) xch[32768 + (b - 1360) * 768 + i * 256 + tid] = (u32x4){0u, 0u, 0u, 0u};
    }
}

extern "C" void kernel_launch(void* const* d_in, const int* in_sizes, int n_in, void* d_out, int out_size, void* d_ws, size_t ws_size, hipStream_t stream) {
    const u16* x0 = (const u16*)d_in[0];
    const u16* wsre = (const u16*)d_in[1];
    const u16* wsim = (const u16*)d_in[2];
    const u16* wdre = (const u16*)d_in[3];
    const u16* wdim = (const u16*)d_in[4];
    const u16* win = (const u16*)d_in[6];
    const u16* wsp = (const u16*)d_in[7];
    const u16* bsp = (const u16*)d_in[8];
    const u16* wout = (const u16*)d_in[9];
    char* ws = (char*)d_ws;
    (void)hipFuncSetAttribute((const void*)k_main, hipFuncAttributeMaxDynamicSharedMemorySize, LDS_TOTAL);
    k_prep<<<K0_BLOCKS, 256, 0, stream>>>(x0, wdre, wdim, win, wout, (u16*)(ws + W_WINT), (u16*)(ws + W_WOUTT), (float*)(ws + W_WSUMD), (float*)(ws + W_WSPIN), (float*)(ws + W_SP),
                                          (u32x4*)(ws + W_XCH), (const u32x4*)wsre, (const u32x4*)wsim, (const u32x4*)wsp);
    k_main<<<256, 512, LDS_TOTAL, stream>>>(x0, wsre, wsim, wdre, wdim, wsp, bsp, ws, (u16*)d_out);
}
```

```cpp
#define TILE_AUX 2
#define PFSTART 2
#include <hip/hip_runtime.h>
#include <hip/hip_bf16.h>
#include <utility>

typedef unsigned short u16;
typedef unsigned long long u64;
typedef short bf16x8 __attribute__((ext_vector_type(8)));
typedef short s16x4 __attribute__((ext_vector_type(4)));
typedef float f32x2 __attribute__((ext_vector_type(2)));
typedef float f32x4 __attribute__((ext_vector_type(4)));
typedef unsigned u32x2 __attribute__((ext_vector_type(2)));
typedef unsigned u32x4 __attribute__((ext_vector_type(4)));
#define LAS __attribute__((address_space(3)))
#define GAS __attribute__((address_space(1)))
#define DEV __device__ __forceinline__

constexpr int waitimm(int vm, int lgkm) { return (vm & 15) | (7 << 4) | ((lgkm & 15) << 8) | ((vm >> 4) << 14); }
#define WAITCNT(vm, lgkm) __builtin_amdgcn_s_waitcnt(waitimm(vm, lgkm))
#define BAR() do { asm volatile("" ::: "memory"); __builtin_amdgcn_s_barrier(); asm volatile("" ::: "memory"); } while (0)
#define SB0() __builtin_amdgcn_sched_barrier(0)
#define STAMP(on, ph, i)
#define ST_PARAM
#define ST_PASS(p)

constexpr int S_ = 2048, D_ = 1024, F_ = 3584, FH = 1792;
#define T_IM 12
#define T_IN 0
#define T_STEP 2
#define T_OUT 20
#define T_SIG 40
#define XM2 ~0u
#define XM16 ~0u
#define POLL_SLEEP 2
constexpr float LN_EPS = 1e-5f;
constexpr int SPIN_MAX = 1 << 17;
constexpr int STORE_AUX = 16;

DEV unsigned pk_bf16(float lo, float hi) { unsigned r; asm("v_cvt_pk_bf16_f32 %0, %1, %2" : "=v"(r) : "v"(lo), "v"(hi)); return r; }
DEV float bf_lo(unsigned v) { return __uint_as_float(v << 16); }
DEV float bf_hi(unsigned v) { return __uint_as_float(v & 0xffff0000u); }
DEV __amdgpu_buffer_rsrc_t rsrc(const void* p, unsigned bytes) { return __builtin_amdgcn_make_buffer_rsrc((void*)p, 0, bytes, 0x00020000); }
DEV f32x4 gelu_tanh(f32x4 x) {
    const float c0 = -2.3022081986f, c1 = c0 * 0.044715f;
    const f32x4 a = x * (x * x * c1 + c0);
    f32x4 d = (f32x4){__builtin_amdgcn_exp2f(a[0]), __builtin_amdgcn_exp2f(a[1]), __builtin_amdgcn_exp2f(a[2]), __builtin_amdgcn_exp2f(a[3])} + 1.0f;
    return x * (f32x4){__builtin_amdgcn_rcpf(d[0]), __builtin_amdgcn_rcpf(d[1]), __builtin_amdgcn_rcpf(d[2]), __builtin_amdgcn_rcpf(d[3])};
}
DEV u32x4 pk8(f32x4 a, f32x4 b) { return (u32x4){pk_bf16(a[0], a[1]), pk_bf16(a[2], a[3]), pk_bf16(b[0], b[1]), pk_bf16(b[2], b[3])}; }
DEV f32x4 unpk4(unsigned a, unsigned b) { return (f32x4){bf_lo(a), bf_hi(a), bf_lo(b), bf_hi(b)}; }
__host__ __device__ constexpr int perm32(int slot) { return 8 * ((slot & 15) >> 2) + 4 * ((slot >> 4) & 1) + (slot & 3); }
__host__ __device__ constexpr int inv32(int row) { return 16 * ((row >> 2) & 1) + 4 * ((row >> 3) & 3) + (row & 3); }
DEV float fq_sum(float a) {
    const auto p = __builtin_amdgcn_permlane16_swap(__float_as_uint(a), __float_as_uint(a), false, false);
    a = __uint_as_float(p[0]) + __uint_as_float(p[1]);
    const auto q = __builtin_amdgcn_permlane32_swap(__float_as_uint(a), __float_as_uint(a), false, false);
    return __uint_as_float(q[0]) + __uint_as_float(q[1]);
}
template <int CTRL> DEV float dpp_mov(float a) { return __builtin_bit_cast(float, __builtin_amdgcn_update_dpp(0, __builtin_bit_cast(int, a), CTRL, 0xf, 0xf, true)); }
template <int CTRL> DEV float dpp_add(float a) { return a + __builtin_bit_cast(float, __builtin_amdgcn_update_dpp(0, __builtin_bit_cast(int, a), CTRL, 0xf, 0xf, true)); }
DEV float row16_sum(float a) { a = dpp_add<0x128>(a); a = dpp_add<0x124>(a); a = dpp_add<0x122>(a); return dpp_add<0x121>(a); }
DEV float oct_sum(float a) { a = dpp_add<0xb1>(a); a = dpp_add<0x4e>(a); return dpp_add<0x141>(a); }
DEV float quad_sum(float a) { a = dpp_add<0xb1>(a); return dpp_add<0x4e>(a); }
constexpr unsigned XCH_EMPTY = 0xffffffffu;
DEV unsigned xch_word(float v) { const unsigned u = __float_as_uint(v); return u == XCH_EMPTY ? 0x7fc00000u : u; }

DEV int opaque_v(int v) { asm volatile("" : "+v"(v)); return v; }
DEV int opaque_s(int v) { asm volatile("" : "+s"(v)); return v; }
template <class F, int... I> DEV void static_for_impl(F&& f, std::integer_sequence<int, I...>) { (f(std::integral_constant<int, I>{}), ...); }
template <int N, class F> DEV void static_for(F&& f) { static_for_impl(f, std::make_integer_sequence<int, N>{}); }

template <int BM_, int BN_, int NS_, int BASE_, int LDP_, int LDQ_, int NT_, int KSW_, bool PERMP_, bool PERMQ_, bool WISP_> struct Cfg {
    static constexpr int BM = BM_, BN = BN_, NS = NS_, LDP = LDP_, LDQ = LDQ_, NT = NT_, KSW = KSW_;
    static constexpr bool PERMP = PERMP_, PERMQ = PERMQ_, WISP = WISP_;
    static constexpr int PB = BM * 128, QB = BN * 128, PBASE = BASE_, QBASE = PBASE + NS * PB, RING = QBASE + NS * QB;
    static constexpr int LP = BM / 32, LQ = BN / 32;
    static constexpr int LW = WISP ? LP : LQ, LA = WISP ? LQ : LP;
};
constexpr int RING_MAX = 147456;
constexpr int L_R = RING_MAX, L_M = L_R + 512, L_WD = L_M + 512, L_WS = L_WD + 512, L_Z = L_WS + 1024, L_JUNK = L_Z + 1024, L_SYNC = L_JUNK + 256, L_TAG = L_SYNC + 48, LDS_TOTAL = L_SYNC + 64;
constexpr int L_TIN0 = 0, L_TIN1 = 131072, L_TOUT = 114688;

template <class C, int N0, int N1, class Dep, class Mid, class Tail>
DEV void loader_phase(LAS unsigned char* lds, const u16* P0, const u16* P1, const u16* Q, int lw, int lane, unsigned drained, Dep&& dep, Mid&& mid, Tail&& tail ST_PARAM) {
    constexpr int NS = C::NS, NT = C::NT, LA = C::LA, LW = C::LW;
    static_assert(C::RING <= RING_MAX && NS >= 3 && NT >= 2 * NS && C::BM % 32 == 0 && !(C::PERMQ && C::BN % 32), "");
    lane = opaque_v(lane); lw = opaque_s(lw);
    const __amdgpu_buffer_rsrc_t rP0 = rsrc(P0, C::BM * C::LDP * 2), rP1 = rsrc(P1, C::BM * C::LDP * 2), rQ = rsrc(Q, C::BN * C::LDQ * 2);
    const int ch = ((lane & 7) ^ (((lw & 1) * 4 + (lane >> 4)) & 7)) * 16, row = lw * 8 + (lane >> 3);
    const int vP = (C::PERMP ? perm32(row) : row) * C::LDP * 2 + ch, vQ = (C::PERMQ ? perm32(row) : row) * C::LDQ * 2 + ch;
    auto issueP = [&](int slot, int kt) {
        LAS unsigned char* dst = lds + C::PBASE + slot * C::PB + lw * 1024;
        const bool second = C::KSW && kt >= C::KSW;
        const __amdgpu_buffer_rsrc_t rP = second ? rP1 : rP0;
        const int kp = (second ? kt - C::KSW : kt) * 128;
#pragma unroll
        for (int i = 0; i < C::LP; ++i) __builtin_amdgcn_raw_ptr_buffer_load_lds(rP, (LAS void*)(dst + i * 4096), 16, vP, i * 32 * C::LDP * 2 + kp, 0, C::WISP ? 0 : 16);
    };
    auto issueQ = [&](int slot, int kt) {
        LAS unsigned char* dst = lds + C::QBASE + slot * C::QB + lw * 1024;
#pragma unroll
        for (int j = 0; j < (C::BN + 31) / 32; ++j)
            if (j < C::LQ || j * 4 + lw < C::BN / 8) __builtin_amdgcn_raw_ptr_buffer_load_lds(rQ, (LAS void*)(dst + j * 4096), 16, vQ, j * 32 * C::LDQ * 2 + kt * 128, 0, 16);
    };
    auto issueW = [&](int slot, int kt) { if (C::WISP) issueP(slot, kt); else issueQ(slot, kt); };
    auto issueA = [&](int slot, int kt) { if (C::WISP) issueQ(slot, kt); else issueP(slot, kt); };
    if (drained) {
        while ((unsigned)__builtin_amdgcn_readfirstlane(*(volatile LAS unsigned*)(lds + L_SYNC + 16)) < drained) __builtin_amdgcn_s_sleep(2);
#pragma unroll 1
        for (int s = 0; s < NS; ++s) issueW(s, s);
        dep();
        STAMP(ston, ph, 4);
        issueA(0, 0); issueA(1, 1);
        WAITCNT(LA, 15);
        BAR();
#pragma unroll
        for (int s = 2; s < NS; ++s) issueA(s, s);
    } else {
        issueW(0, 0); issueA(0, 0); issueW(1, 1); issueA(1, 1);
        STAMP(ston, ph, 4);
        WAITCNT(LA + LW, 15);
        BAR();
#pragma unroll 1
        for (int s = 2; s < NS; ++s) { issueW(s, s); issueA(s, s); }
    }
    static_for<NS - 2>([&](auto ic) {
        constexpr int t = decltype(ic)::value;
        WAITCNT((NS - 2 - t) * LA + t * (LA + LW), 15);
        BAR();
        issueW(t, t + NS); issueA(t, t + NS);
    });
    int sl = NS - 2;
#pragma unroll 1
    for (int t = NS - 2; t < NT - NS; ++t) {
        WAITCNT((NS - 2) * (LA + LW), 15);
        BAR();
        issueW(sl, t + NS); issueA(sl, t + NS);
        sl = (sl + 1 == NS) ? 0 : sl + 1;
        mid(t);
    }
    tail(std::integral_constant<int, 0>{});
    static_for<NS - 1>([&](auto ic) {
        constexpr int i = decltype(ic)::value;
        WAITCNT((NS - 2 - i) * (LA + LW) + N0 + (i ? N1 : 0), 15);
        BAR();
        if constexpr (i == 0) tail(std::integral_constant<int, 1>{});
    });
    tail(std::integral_constant<int, 2>{});
    WAITCNT(63, 0);
    BAR();
}

template <class C, int MT, int NTL, int NSWAP, bool ROLL, int RNUM, int RDEN, class Hook>
DEV void compute_phase(LAS unsigned char* lds, int prow0, int qrow0, int lane, f32x4 (&acc)[MT][NTL], Hook&& hook ST_PARAM) {
    constexpr int NR = MT + NTL, NM = MT * NTL, NT = C::NT;
    const int fr = lane & 15, fq = lane >> 4;
    int aoff[2], boff[2];
#pragma unroll
    for (int k = 0; k < 2; ++k) {
        const int ch = ((k * 4 + fq) ^ ((fr >> 1) & 7)) * 16;
        aoff[k] = C::PBASE + (prow0 + fr) * 128 + ch;
        boff[k] = C::QBASE + (qrow0 + fr) * 128 + ch;
    }
#pragma unroll
    for (int m = 0; m < MT; ++m)
#pragma unroll
        for (int n = 0; n < NTL; ++n) { acc[m][n] = (f32x4){0.f, 0.f, 0.f, 0.f}; asm volatile("" : "+v"(acc[m][n])); }
    auto mfma = [&](int m, int n, const bf16x8& a, const bf16x8& b) {
        if (n >= NSWAP) asm volatile("v_mfma_f32_16x16x32_bf16 %0, %1, %2, %0" : "+v"(acc[m][n]) : "v"(a), "v"(b));
        else asm volatile("v_mfma_f32_16x16x32_bf16 %0, %1, %2, %0" : "+v"(acc[m][n]) : "v"(b), "v"(a));
    };
    int sp = 0, sq = 0;
    auto next = [&]() { sp = (sp + C::PB == C::NS * C::PB) ? 0 : sp + C::PB; sq = (sq + C::QB == C::NS * C::QB) ? 0 : sq + C::QB; };
    BAR();
    STAMP(ston, ph, 1);
    if constexpr (!ROLL) {
        bf16x8 fa[4][MT], fb[4][NTL];
        auto rd1 = [&](int set, int k, int i) {
            if (i == 0) fa[set][0] = *(const LAS bf16x8*)(lds + sp + aoff[k]);
            else if (i <= NTL) fb[set][i - 1] = *(const LAS bf16x8*)(lds + sq + boff[k] + (i - 1) * 2048);
            else fa[set][i - NTL] = *(const LAS bf16x8*)(lds + sp + aoff[k] + (i - NTL) * 2048);
        };
        auto stg = [&](int set, bool rd) {
#pragma unroll
            for (int i = 0; i < 2 * NM; ++i) {
                const int st = set + i / NM, m = (i % NM) / NTL, n = i % NTL;
                mfma(m, n, fa[st][m], fb[st][n]); SB0();
                if (rd) {
#pragma unroll
                    for (int r = (i * RNUM + RDEN - 1) / RDEN; r < ((i + 1) * RNUM + RDEN - 1) / RDEN; ++r) if (r < 2 * NR) rd1((set ^ 2) + r / NR, r / NR, r % NR);
                    SB0();
                }
            }
        };
        static_assert((2 * NM * RNUM + RDEN - 1) / RDEN >= 2 * NR && NT % 2 == 0, "");
#pragma unroll
        for (int r = 0; r < 2 * NR; ++r) rd1(r / NR, r / NR, r % NR);
#pragma unroll 1
        for (int t = 0; t < NT - 2; t += 2) {
            next(); WAITCNT(63, 0); BAR(); SB0();
            stg(0, true);
            next(); WAITCNT(63, 0); BAR(); SB0();
            stg(2, true);
            hook(t); SB0();
        }
        next();
        stg(0, false);
        BAR(); SB0();
#pragma unroll
        for (int r = 0; r < 2 * NR; ++r) rd1(2 + r / NR, r / NR, r % NR);
        WAITCNT(63, 0); BAR(); SB0();
        stg(2, false);
    } else {
        static_assert(MT == 2, "");
        constexpr int G = 8;
        bf16x8 fa[2][2], fb[NTL];
        auto rda = [&](int set, int k, int m) { fa[set][m] = *(const LAS bf16x8*)(lds + sp + aoff[k] + m * 2048); };
        auto rdb = [&](int k, int n) { fb[n] = *(const LAS bf16x8*)(lds + sq + boff[k] + n * 2048); };
        auto half0 = [&]() {
#pragma unroll
            for (int j = 0; j < NM; ++j) {
                mfma(j & 1, j >> 1, fa[0][j & 1], fb[j >> 1]); SB0();
                if (j & 1) rdb(1, j >> 1); else if (j < 4) rda(1, 1, j >> 1);
                SB0();
            }
        };
        auto half1 = [&](bool rd) {
#pragma unroll
            for (int j = 0; j < NM; ++j) {
                if (j == G) { next(); WAITCNT(63, 0); BAR(); SB0(); }
                mfma(j & 1, j >> 1, fa[1][j & 1], fb[j >> 1]); SB0();
                if (rd && j >= G) {
                    if (j < G + 2) rda(0, 0, j - G);
#pragma unroll
                    for (int n = 0; n < NTL; ++n) if ((G + 2 + n > 2 * n + 1 ? G + 2 + n : 2 * n + 1) == j) rdb(0, n);
                    SB0();
                }
            }
        };
        rda(0, 0, 0); rda(0, 0, 1);
#pragma unroll
        for (int n = 0; n < NTL; ++n) rdb(0, n);
        SB0();
#pragma unroll 1
        for (int t = 0; t < NT - 1; ++t) { half0(); half1(true); }
        half0(); half1(false);
    }
    asm volatile("s_nop 15" ::: "memory");
#pragma unroll
    for (int m = 0; m < MT; ++m)
#pragma unroll
        for (int n = 0; n < NTL; ++n) asm volatile("" : "+v"(acc[m][n]));
    STAMP(ston, ph, 2);
}

DEV u64 launch_tag(LAS unsigned char* lds) { return *(volatile LAS u64*)(lds + L_TAG); }
DEV void box_poll(const u64* box, int nprod, u64 tag, int lane) {
#pragma unroll 1
    for (int spin = 0; spin < SPIN_MAX; ++spin) {
        const u64 v = __hip_atomic_load((const GAS u64*)box + (lane & (nprod - 1)), __ATOMIC_RELAXED, __HIP_MEMORY_SCOPE_AGENT);
        if (__all(v == tag)) break;
        __builtin_amdgcn_s_sleep(POLL_SLEEP);
    }
}
DEV void dep_wait(const u64* box, int nprod, LAS unsigned char* lds, unsigned epoch, int lw, int lane) {
    volatile LAS unsigned* flag = (volatile LAS unsigned*)(lds + L_SYNC);
    if (lw == 0) {
        box_poll(box, nprod, launch_tag(lds), lane);
        *flag = epoch;
    } else {
        while ((unsigned)__builtin_amdgcn_readfirstlane(*flag) < epoch) __builtin_amdgcn_s_sleep(1);
    }
    asm volatile("" ::: "memory");
}
template <int CNT = 16> DEV void dep_signal(u64* boxes, int word, int ncons, LAS unsigned char* lds, int lane) {
    const u64 tag = launch_tag(lds);
    asm volatile("s_waitcnt vmcnt(0)" ::: "memory");
    unsigned old = 0;
    if (lane == 0) old = __hip_atomic_fetch_add((LAS unsigned*)(lds + L_SYNC + CNT), 1u, __ATOMIC_RELAXED, __HIP_MEMORY_SCOPE_WORKGROUP);
    if ((__builtin_amdgcn_readfirstlane(old) & 3) == 3 && lane < ncons) __hip_atomic_store((GAS u64*)boxes + word, tag, __ATOMIC_RELAXED, __HIP_MEMORY_SCOPE_AGENT);
}
DEV void touch(LAS unsigned char* lds, const u16* p, unsigned bytes, int off) {
    __builtin_amdgcn_raw_ptr_buffer_load_lds(rsrc(p, bytes), (LAS void*)(lds + L_JUNK), 4, off, 0, 0, 0);
}

DEV void row_tables_load(const float* sp, unsigned bytes, int tid, f32x4 (&v)[4]) {
    const __amdgpu_buffer_rsrc_t r = rsrc(sp, bytes);
#pragma unroll
    for (int i = 0; i < 4; ++i) v[i] = __builtin_bit_cast(f32x4, __builtin_amdgcn_raw_buffer_load_b128(r, i * 4096 + tid * 16, 0, 16));
}
DEV void row_tables_finish(LAS unsigned char* lds, int tid, const f32x4 (&v)[4], float sign) {
#pragma unroll
    for (int i = 0; i < 4; ++i) {
        const float s = oct_sum(v[i][0] + v[i][2]), q = oct_sum(v[i][1] + v[i][3]);
        const float mean = s * (1.0f / D_), var = fmaxf(q * (1.0f / D_) - mean * mean, 0.f), r = sign * __builtin_amdgcn_rsqf(var + LN_EPS);
        if ((tid & 7) == 0) { ((LAS float*)(lds + L_R))[i * 32 + (tid >> 3)] = r; ((LAS float*)(lds + L_M))[i * 32 + (tid >> 3)] = -mean * r; }
    }
}

constexpr size_t MB = 1 << 20;
constexpr size_t W_T = 0, W_XA = 16 * MB, W_XB = 24 * MB, W_Z2T = 28 * MB, W_H = 44 * MB, W_WINT = 60 * MB, W_WOUTT = 68 * MB;
constexpr size_t W_SP = 72 * MB, W_GRAN = 74 * MB, W_WSPIN = 74 * MB + 65536, W_XCH = 74 * MB + 512 * 1024, W_CTR = 75 * MB, W_NONCE = 75 * MB + 768 * 1024;

typedef Cfg<128, 128, 4, 0, D_, D_, D_ / 64, 0, false, true, true> CG1;
typedef Cfg<128, 64, 4, 16384, S_, 4096, 64, 32, false, false, true> CG2;
typedef Cfg<128, 224, 3, 0, D_, D_, D_ / 64, 0, true, false, false> CG3;
typedef Cfg<128, 112, 4, 0, S_, S_, S_ / 64, 0, true, false, true> CG4;
typedef Cfg<128, 64, 4, 0, FH, FH, FH / 64, 0, false, false, false> CG5;

template <class C, class Hook>
DEV void res_phase(LAS unsigned char* lds, const u16* res, u16* out, float* sp, int tm, int tn, int w, int lane, Hook&& hook ST_PARAM) {
    const int fr = lane & 15, fq = lane >> 4;
    const __amdgpu_buffer_rsrc_t rX = rsrc(res + (size_t)(tm * 128 + w * 32) * D_ + tn * 64, 32 * D_ * 2 - tn * 128);
    u32x4 xr[2][2];
#pragma unroll
    for (int m = 0; m < 2; ++m)
#pragma unroll
        for (int h = 0; h < 2; ++h) xr[m][h] = __builtin_amdgcn_raw_buffer_load_b128(rX, (m * 16 + fr) * D_ * 2 + h * 64 + fq * 16, 0, 16);
    f32x4 acc[2][4];
    compute_phase<C, 2, 4, 4, false, 1, 1>(lds, w * 32, 0, lane, acc, hook ST_PASS(ph));
    const __amdgpu_buffer_rsrc_t rC = rsrc(out + (size_t)(tm * 128 + w * 32) * D_ + tn * 64, 32 * D_ * 2 - tn * 128);
    const __amdgpu_buffer_rsrc_t rS = rsrc(sp + (size_t)(tm * 128 + w * 32) * 32, 32 * 128);
#pragma unroll
    for (int m = 0; m < 2; ++m) {
        f32x4 s = (f32x4){0.f, 0.f, 0.f, 0.f}, qq = s;
#pragma unroll
        for (int h = 0; h < 2; ++h) {
            const u32x4 o = pk8(acc[m][2 * h] + unpk4(xr[m][h][0], xr[m][h][1]), acc[m][2 * h + 1] + unpk4(xr[m][h][2], xr[m][h][3]));
            __builtin_amdgcn_raw_buffer_store_b128(o, rC, (m * 16 + fr) * D_ * 2 + h * 64 + fq * 16, 0, STORE_AUX);
            const f32x4 a = unpk4(o[0], o[1]), b = unpk4(o[2], o[3]);
            s += a + b; qq += a * a + b * b;
        }
        const float st = fq_sum((s[0] + s[1]) + (s[2] + s[3])), qt = fq_sum((qq[0] + qq[1]) + (qq[2] + qq[3]));
        if (fq == 0) __builtin_amdgcn_raw_buffer_store_b64((u32x2){__float_as_uint(st), __float_as_uint(qt)}, rS, (m * 16 + fr) * 128 + tn * 8, 0, 16);
    }
}

__global__ __launch_bounds__(512, 2) void k_main(const u16* __restrict__ x0, const u16* __restrict__ wsre, const u16* __restrict__ wsim, const u16* __restrict__ wdre,
                                                 const u16* __restrict__ wdim, const u16* __restrict__ win, const u16* __restrict__ wsp, const u16* __restrict__ bsp,
                                                 const u16* __restrict__ wout, char* ws, u16* out) {
    extern __shared__ __attribute__((aligned(1024))) unsigned char shm[];
    LAS unsigned char* lds = (LAS unsigned char*)shm;
    const int tid = threadIdx.x, lane0 = tid & 63, w0 = __builtin_amdgcn_readfirstlane(tid >> 6);
    const int tm0 = ((blockIdx.x & 7) >> 1) * 4 + (blockIdx.x >> 6), tn0 = (blockIdx.x & 1) * 8 + ((blockIdx.x >> 3) & 7);
#define PHASE_VARS() const int lane = opaque_v(lane0), w = opaque_s(w0), tm = opaque_s(tm0), tn = opaque_s(tn0), fr = lane & 15, fq = lane >> 4; (void)fr; (void)fq; (void)w
    u64* ctr = (u64*)(ws + W_CTR);
    if (w0 >= 4) {
        const int lw = w0 - 4;
        auto none = [&](auto) {};
#pragma unroll 1
        for (int l = 0; l < 2; ++l) {
            const u16* x = l == 0 ? x0 : (const u16*)(ws + W_XB);
            const u16* T = (const u16*)(ws + W_T + l * 8 * MB); const u16* xa = (const u16*)(ws + W_XA + l * 4 * MB);
            const u16* z2t = (const u16*)(ws + W_Z2T + l * 8 * MB); const u16* h = (const u16*)(ws + W_H + l * 8 * MB);
            const float* sp = (const float*)(ws + W_SP + l * 512 * 1024);
            const u64* c = ctr + l * 5 * 8192;
            const unsigned first = l == 0 ? ~0u : 0u;
            f32x4 v[4], u[4];
            { PHASE_VARS(); const int lt = (w - 4) * 64 + lane;
            const u16* pw = (tm < 8 ? wdre : wdim) + (size_t)(tm & 7) * 128 * D_;
            const __amdgpu_buffer_rsrc_t rg = rsrc(ws + W_GRAN, 65536 & first);
            const int goff = (lt < 128 ? tn * 128 + lt : 1920 + tm * 128 + lt) * 16;
            u32x4 g;
            loader_phase<CG1, 6, 7>(lds, pw, pw, x + (size_t)tn * 128 * D_, lw, lane, l * 20, [&]() { dep_wait(ctr + 4 * 8192 + (tm * 16 + tn) * 32, 16, lds, 5, lw, lane); }, none, [&](auto ic) {
                constexpr int i = decltype(ic)::value;
                if constexpr (i == 0) {
                    row_tables_load(sp + tn * 128 * 32, 16384 & ~first, lt, v);
                    g = __builtin_amdgcn_raw_buffer_load_b128(rg, goff, 0, 16);
                    touch(lds, wsre + (size_t)(tm * 128 + tn * 8 + lw * 2) * S_, (PFSTART & 1) ? 0 : 8192 & first, lane * 128);
                } else if constexpr (i == 1) {
                    const __amdgpu_buffer_rsrc_t r = rsrc(win + (size_t)(tm * 64 + (lw >> 1) * 32) * F_, (32 * F_ * 2) & first & XM2);
#pragma unroll
                    for (int j = 0; j < 7; ++j) {
                        const int q = (lw & 1) * 7 + j, cb = q * 64 + lane, row = cb / 28, ch = cb - row * 28;
                        __builtin_amdgcn_raw_ptr_buffer_load_lds(r, (LAS void*)(lds + (lw < 2 ? L_TIN0 : L_TIN1) + q * 1024), 16, row * F_ * 2 + (ch < 14 ? ch * 16 : FH * 2 - 224 + ch * 16), tn * 224, 0, TILE_AUX);
                    }
                } else {
                    const float sign = tm < 8 ? 1.0f : -1.0f;
                    if (l) { row_tables_finish(lds, lt, v, sign); return; }
                    const u64 tag = launch_tag(lds);
                    if (!__all(g[1] == (unsigned)tag && g[3] == (unsigned)(tag >> 32))) {
                        asm volatile("" ::: "memory");
#pragma unroll 1
                        for (int spin = 0; spin < SPIN_MAX; ++spin) {
                            g = __builtin_amdgcn_raw_buffer_load_b128(rg, goff, 0, 16);
                            if (__all(g[1] == (unsigned)tag && g[3] == (unsigned)(tag >> 32))) break;
                            __builtin_amdgcn_s_sleep(POLL_SLEEP);
                        }
                    }
                    const float mean = __uint_as_float(g[0]) * (1.0f / D_), var = fmaxf(__uint_as_float(g[2]) * (1.0f / D_) - mean * mean, 0.f), rs = sign * __builtin_amdgcn_rsqf(var + LN_EPS);
                    if (lt < 128) { ((LAS float*)(lds + L_R))[lt] = rs; ((LAS float*)(lds + L_M))[lt] = -mean * rs; }
                    else ((LAS float*)(lds + L_WD))[lt - 128] = __uint_as_float(g[0]);
                }
            } ST_PASS(l * 5 + 1)); }
            { PHASE_VARS();
            u64 tw;
            const u64* tbox = ctr + 8192 + (tm * 16 + tn) * 32 + 16;
            loader_phase<CG2, 1, 0>(lds, wsre + (size_t)tm * 128 * S_, wsim + (size_t)tm * 128 * S_, T + (size_t)tn * 64 * 4096, lw, lane, l * 20 + 4,
                              [&]() { dep_wait(c + (tm * 16 + tn) * 32, 32, lds, l * 5 + 1, lw, lane); },
                              [&](int t) {
                                  if (t != T_IM || l) return;
                                  if (!(PFSTART & 2)) touch(lds, wsim + (size_t)(tm * 128 + tn * 8 + lw * 2) * S_, 8192, lane * 128);
                                  const __amdgpu_buffer_rsrc_t r = rsrc(wout + (size_t)tm * 112 * D_, (112 * D_ * 2) & XM2);
#pragma unroll
                                  for (int j = 0; j < 4; ++j)
                                      if (j < 3 || lw < 2) __builtin_amdgcn_raw_ptr_buffer_load_lds(r, (LAS void*)(lds + L_TOUT + (j * 4 + lw) * 1024), 16, ((j * 4 + lw) * 8 + (lane >> 3)) * D_ * 2 + (lane & 7) * 16, tn * 128, 0, TILE_AUX);
                              },
                              [&](auto ic) {
                                  constexpr int i = decltype(ic)::value;
                                  if constexpr (i == 0) tw = __hip_atomic_load((const GAS u64*)tbox + (lane & 15), __ATOMIC_RELAXED, __HIP_MEMORY_SCOPE_AGENT);
                                  if constexpr (i == 2) { const u64 tag = launch_tag(lds); if (!__all(tw == tag)) box_poll(tbox, 16, tag, lane); }
                              } ST_PASS(l * 5 + 2)); }
            { PHASE_VARS(); const int lt = (w - 4) * 64 + lane;
            loader_phase<CG3, 9, 0>(lds, xa + (size_t)tm * 128 * D_, xa, (const u16*)(ws + W_WINT) + (size_t)tn * 224 * D_, lw, lane, l * 20 + 8,
                              [&]() { dep_wait(c + 8192 + (tm * 16 + tn) * 32, 16, lds, l * 5 + 2, lw, lane);
                                      if (blockIdx.x == 0 && l == 0 && lt == 0) __hip_atomic_store((GAS u64*)(ws + W_NONCE), launch_tag(lds), __ATOMIC_RELAXED, __HIP_MEMORY_SCOPE_AGENT); },
                              none, [&](auto ic) {
                                  constexpr int i = decltype(ic)::value;
                                  if constexpr (i == 0) {
                                      row_tables_load(sp + 65536 + tm * 128 * 32, 16384, lt, v);
                                      const __amdgpu_buffer_rsrc_t r = rsrc(ws + W_WSPIN + (size_t)tn * 224 * 64, 224 * 64);
#pragma unroll
                                      for (int j = 0; j < 4; ++j) u[j] = __builtin_bit_cast(f32x4, __builtin_amdgcn_raw_buffer_load_b128(r, j * 4096 + lt * 16, 0, 16));
                                      touch(lds, wsp + (size_t)(tm * 128 + tn * 8 + lw * 2) * S_, 8192 & first, lane * 128);
                                  } else if constexpr (i == 2) {
                                      row_tables_finish(lds, lt, v, 1.0f);
#pragma unroll
                                      for (int j = 0; j < 4; ++j) { const float s = quad_sum((u[j][0] + u[j][1]) + (u[j][2] + u[j][3])); if ((lt & 3) == 0) ((LAS float*)(lds + L_WS))[j * 64 + (lt >> 2)] = s; }
                                  }
                              } ST_PASS(l * 5 + 3)); }
            { PHASE_VARS();
            loader_phase<CG4, 0, 0>(lds, wsp + (size_t)tm * 128 * S_, wsp, z2t + (size_t)tn * 112 * S_, lw, lane, l * 20 + 12, [&]() { dep_wait(c + 2 * 8192 + (tm * 16 + tn) * 32, 16, lds, l * 5 + 3, lw, lane); }, none, none ST_PASS(l * 5 + 4)); }
            { PHASE_VARS();
            loader_phase<CG5, 0, 0>(lds, h + (size_t)tm * 128 * FH, h, (const u16*)(ws + W_WOUTT) + (size_t)tn * 64 * FH, lw, lane, l * 20 + 16,
                              [&]() { dep_wait(c + 3 * 8192 + (tm * 16 + tn) * 32, 16, lds, l * 5 + 4, lw, lane); }, none, none ST_PASS(l * 5 + 5)); }
        }
        asm volatile(".Lk_main_e0:");
        return;
    }
    {
        PHASE_VARS();
        u64 z = __hip_atomic_load((const GAS u64*)(ws + W_NONCE), __ATOMIC_RELAXED, __HIP_MEMORY_SCOPE_AGENT);
        const int xr = tn * 128 + tm * 8 + w * 2, wr = (tm & 7) * 128 + tn * 8 + w * 2;
        const __amdgpu_buffer_rsrc_t rx = rsrc(x0 + (size_t)xr * D_, 4096), rw = rsrc((tm < 8 ? wdre : wdim) + (size_t)wr * D_, 4096);
        u32x4 a[4], b[4];
#pragma unroll
        for (int i = 0; i < 4; ++i) { a[i] = __builtin_amdgcn_raw_buffer_load_b128(rx, i * 1024 + lane * 16, 0, 0); b[i] = __builtin_amdgcn_raw_buffer_load_b128(rw, i * 1024 + lane * 16, 0, 0); }
        u64 pc; unsigned len;
        asm volatile("s_getpc_b64 %0\n.Lk_main_pc:\n\ts_max_i32 %1, .Lk_main_e0-.Lk_main_pc, 0\n\ts_max_i32 %1, .Lk_main_e1-.Lk_main_pc, %1" : "=s"(pc), "=s"(len) :: "scc");
        touch(lds, (const u16*)pc, len, (w * 64 + lane) * 128); touch(lds, (const u16*)pc, len, (256 + w * 64 + lane) * 128);
        if (PFSTART & 1) touch(lds, wsre + (size_t)(tm * 128 + tn * 8 + w * 2) * S_, 8192, lane * 128);
        if (PFSTART & 2) touch(lds, wsim + (size_t)(tm * 128 + tn * 8 + w * 2) * S_, 8192, lane * 128);
        STAMP(ston, 1, 5);
        if (tid < 128) __builtin_amdgcn_raw_buffer_store_b128((u32x4){XCH_EMPTY, XCH_EMPTY, XCH_EMPTY, XCH_EMPTY}, rsrc(ws + W_XCH + blockIdx.x * 2048, 2048), tid * 16, 0, 16);
        if (tid < 12) *(LAS unsigned*)(lds + L_SYNC + tid * 4) = 0u;
        z += 0x9E3779B97F4A7C15ull; z = (z ^ (z >> 30)) * 0xBF58476D1CE4E5B9ull; z = (z ^ (z >> 27)) * 0x94D049BB133111EBull; z ^= z >> 31;
        if (tid == 0) *(LAS u64*)(lds + L_TAG) = z;
        STAMP(ston, 1, 6);
        float g0 = 0.f, g1 = 0.f;
#pragma unroll
        for (int r = 0; r < 2; ++r) {
            f32x4 sx = (f32x4){0.f, 0.f, 0.f, 0.f}, qx = sx, sw = sx;
#pragma unroll
            for (int i = 0; i < 2; ++i) {
                const f32x4 a0 = unpk4(a[2 * r + i][0], a[2 * r + i][1]), a1 = unpk4(a[2 * r + i][2], a[2 * r + i][3]);
                sx += a0 + a1; qx += a0 * a0 + a1 * a1;
                sw += unpk4(b[2 * r + i][0], b[2 * r + i][1]) + unpk4(b[2 * r + i][2], b[2 * r + i][3]);
            }
            const float s1 = fq_sum(row16_sum((sx[0] + sx[1]) + (sx[2] + sx[3]))), q1 = fq_sum(row16_sum((qx[0] + qx[1]) + (qx[2] + qx[3]))), s2 = fq_sum(row16_sum((sw[0] + sw[1]) + (sw[2] + sw[3])));
            if (lane == r) { g0 = s1; g1 = q1; }
            if (lane == 2 + r) g0 = s2;
        }
        if (lane < 4) __builtin_amdgcn_raw_buffer_store_b128((u32x4){__float_as_uint(g0), (unsigned)z, __float_as_uint(g1), (unsigned)(z >> 32)}, rsrc(ws + W_GRAN, 65536),
                                                             (lane < 2 ? xr + lane : 2046 + (tm >> 3) * 1024 + wr + lane) * 16, 0, 16);
        STAMP(ston, 1, 7);
    }
    WAITCNT(63, 0);
    const LAS float* R = (const LAS float*)(lds + L_R); const LAS float* M = (const LAS float*)(lds + L_M); const LAS float* WS = (const LAS float*)(lds + L_WS);
#pragma unroll 1
    for (int l = 0; l < 2; ++l) {
        const u16* x = l == 0 ? x0 : (const u16*)(ws + W_XB);
        u16* xa = (u16*)(ws + W_XA + l * 4 * MB);
        float* sp = (float*)(ws + W_SP + l * 512 * 1024);
        u64* c = ctr + l * 5 * 8192;
        {
            PHASE_VARS();
            const int wm = w >> 1, wn = w & 1;
            f32x4 acc[4][4];
            compute_phase<CG1, 4, 4, 4, false, 2, 3>(lds, wm * 64, wn * 64, lane, acc, [](int) {} ST_PASS(l * 5 + 1));
            float wsum[4];
#pragma unroll
            for (int m = 0; m < 4; ++m) wsum[m] = ((const LAS float*)(lds + L_WD))[wm * 64 + m * 16 + fr];
            const __amdgpu_buffer_rsrc_t rC = rsrc((u16*)(ws + W_T + l * 8 * MB) + (size_t)(tm & 7) * 128 * 4096 + (tm < 8 ? 0 : 2048) + tn * 128, 128 * 4096 * 2 - (2048 + tn * 128) * 2);
#pragma unroll
            for (int q = 0; q < 2; ++q) {
                const int cc = wn * 64 + q * 32 + fq * 8;
                const f32x4 r0 = *(const LAS f32x4*)(R + cc), r1 = *(const LAS f32x4*)(R + cc + 4), m0 = *(const LAS f32x4*)(M + cc), m1 = *(const LAS f32x4*)(M + cc + 4);
#pragma unroll
                for (int m = 0; m < 4; ++m) {
                    const int row = wm * 64 + (m >> 1) * 32 + inv32((m & 1) * 16 + fr);
                    __builtin_amdgcn_raw_buffer_store_b128(pk8(acc[m][2 * q] * r0 + m0 * wsum[m], acc[m][2 * q + 1] * r1 + m1 * wsum[m]), rC, row * 8192 + cc * 2, 0, STORE_AUX);
                }
            }
            dep_signal(c, ((lane >> 1) * 16 + (tm & 7) * 2 + (lane & 1)) * 32 + (tm >> 3) * 16 + tn, 32, lds, lane);
            STAMP(true, l * 5 + 1, 8 + w);
        }
        {
            PHASE_VARS();
            res_phase<CG2>(lds, x, xa, sp + 65536, tm, tn, w, lane, [&](int t) {
                if (l) return;
                auto rdt = [&](int at, int stride) {
                    LAS unsigned char* p = lds + at + (fr >> 2) * stride + (fr & 3) * 8;
                    const u32x2 a = __builtin_bit_cast(u32x2, __builtin_amdgcn_ds_read_tr16_b64_v4i16((LAS s16x4*)p)), b = __builtin_bit_cast(u32x2, __builtin_amdgcn_ds_read_tr16_b64_v4i16((LAS s16x4*)(p + 4 * stride)));
                    return (u32x4){a[0], a[1], b[0], b[1]};
                };
                const int g = (t - T_IN) / T_STEP * 4 + w, j = (t - T_OUT) / T_STEP;
                if (t >= T_IN && g < 14 && (t - T_IN) % T_STEP == 0) {
                    const __amdgpu_buffer_rsrc_t ri = rsrc((u16*)(ws + W_WINT) + (size_t)tn * 224 * D_, (224 * D_ * 2) & XM16), rs = rsrc(ws + W_WSPIN + (size_t)tn * 224 * 64, (224 * 64) & XM16);
                    const int half = g >= 7, cc = (g - half * 7) * 16 + fr, orow = half * 112 + (cc < 96 ? (cc & ~31) + inv32(cc & 31) : cc);
                    const u32x4 v0 = rdt(L_TIN0 + fq * 8 * 448 + g * 32, 448), v1 = rdt(L_TIN1 + fq * 8 * 448 + g * 32, 448);
                    __builtin_amdgcn_raw_buffer_store_b128(v0, ri, orow * D_ * 2 + fq * 16, tm * 128, STORE_AUX);
                    __builtin_amdgcn_raw_buffer_store_b128(v1, ri, orow * D_ * 2 + 64 + fq * 16, tm * 128, STORE_AUX);
                    const f32x4 sm = (unpk4(v0[0], v0[1]) + unpk4(v0[2], v0[3])) + (unpk4(v1[0], v1[1]) + unpk4(v1[2], v1[3]));
                    const float st = fq_sum((sm[0] + sm[1]) + (sm[2] + sm[3]));
                    if (fq == 0) __builtin_amdgcn_raw_buffer_store_b32(__float_as_uint(st), rs, orow * 64 + tm * 4, 0, STORE_AUX);
                } else if (t >= T_OUT && j < 2 && (t - T_OUT) % T_STEP == 0) {
                    const __amdgpu_buffer_rsrc_t ro = rsrc((u16*)(ws + W_WOUTT) + (size_t)tn * 64 * FH, (64 * FH * 2) & XM16);
                    const int orow = ((w >> 1) * 32 + inv32((w & 1) * 16 + fr)) * FH * 2, k0 = j * 64 + fq * 8, k1 = j ? 96 + (fq & 1) * 8 : 32 + fq * 8;
                    const u32x4 v0 = rdt(L_TOUT + k0 * 128 + w * 32, 128), v1 = rdt(L_TOUT + k1 * 128 + w * 32, 128);
                    __builtin_amdgcn_raw_buffer_store_b128(v0, ro, orow + (tm * 96 + k0) * 2, 0, STORE_AUX);
                    if (j == 0 || fq < 2) __builtin_amdgcn_raw_buffer_store_b128(v1, ro, orow + (j ? 1440 + tm * 16 + k1 : tm * 96 + k1) * 2, 0, STORE_AUX);
                } else if (t == T_SIG) {
                    dep_signal<32>(ctr + 8192, (lane * 16 + tn) * 32 + 16 + tm, 16, lds, lane);
                }
            } ST_PASS(l * 5 + 2));
            dep_signal(c + 8192, (tm * 16 + lane) * 32 + tn, 16, lds, lane);
            STAMP(true, l * 5 + 2, 8 + w);
        }
        u32x4 zz[7];
        {
            PHASE_VARS();
            f32x4 acc[2][14];
            compute_phase<CG3, 2, 14, 7, true, 1, 1>(lds, w * 32, 0, lane, acc, [](int) {} ST_PASS(l * 5 + 3));
            f32x4 s[2], q[2];
#pragma unroll
            for (int m = 0; m < 2; ++m) {
                const f32x4 r = *(const LAS f32x4*)(R + w * 32 + fq * 8 + m * 4), mr = *(const LAS f32x4*)(M + w * 32 + fq * 8 + m * 4);
                s[m] = (f32x4){0.f, 0.f, 0.f, 0.f}; q[m] = s[m];
#pragma unroll
                for (int n = 7; n < 14; ++n) {
                    const f32x4 z = gelu_tanh(acc[m][n] * r + mr * WS[n * 16 + fr]);
                    acc[m][n] = z; s[m] += z; q[m] += z * z;
                }
            }
            float v8[8], v4[4], v2[2];
#pragma unroll
            for (int i = 0; i < 8; ++i) {
                const float lo = (i & 1) ? q[0][i >> 1] : s[0][i >> 1], hi = (i & 1) ? q[1][i >> 1] : s[1][i >> 1];
                v8[i] = ((fr & 8) ? hi : lo) + dpp_mov<0x128>((fr & 8) ? lo : hi);
            }
#pragma unroll
            for (int i = 0; i < 4; ++i) v4[i] = ((fr & 4) ? v8[i + 4] : v8[i]) + dpp_mov<0x141>(dpp_mov<0x1b>((fr & 4) ? v8[i] : v8[i + 4]));
#pragma unroll
            for (int i = 0; i < 2; ++i) v2[i] = ((fr & 2) ? v4[i + 2] : v4[i]) + dpp_mov<0x4e>((fr & 2) ? v4[i] : v4[i + 2]);
            const float pv = ((fr & 1) ? v2[1] : v2[0]) + dpp_mov<0xb1>((fr & 1) ? v2[0] : v2[1]);
            const __amdgpu_buffer_rsrc_t rX = rsrc(ws + W_XCH + l * 262144 + (size_t)(tm * 4 + w) * 4096, 4096);
            __builtin_amdgcn_raw_buffer_store_b32(xch_word(pv), rX, tn * 256 + lane * 4, 0, 16);
            SB0();
            STAMP(ston, l * 5 + 3, 5);
            {
                const float r0 = R[w * 32 + perm32(fr)], r1 = R[w * 32 + perm32(16 + fr)], m0 = M[w * 32 + perm32(fr)], m1 = M[w * 32 + perm32(16 + fr)];
#pragma unroll
                for (int n = 0; n < 7; ++n) {
                    const f32x4 wv = *(const LAS f32x4*)(WS + n * 16 + fq * 4);
                    zz[n] = pk8(gelu_tanh(acc[0][n] * r0 + wv * m0), gelu_tanh(acc[1][n] * r1 + wv * m1));
                    asm volatile("" : "+v"(zz[n]));
                }
            }
            SB0();
            STAMP(ston, l * 5 + 3, 6);
            f32x4 t;
            for (int spin = 0;; ++spin) {
                u32x4 g[4]; bool ok = true; t = (f32x4){0.f, 0.f, 0.f, 0.f};
#pragma unroll
                for (int i = 0; i < 4; ++i) g[i] = __builtin_amdgcn_raw_buffer_load_b128(rX, lane * 16 + i * 1024, 0, 16);
#pragma unroll
                for (int i = 0; i < 4; ++i) {
                    ok &= (g[i][0] != XCH_EMPTY) & (g[i][1] != XCH_EMPTY) & (g[i][2] != XCH_EMPTY) & (g[i][3] != XCH_EMPTY);
                    t += __builtin_bit_cast(f32x4, g[i]);
                }
                if (__all(ok) || spin > SPIN_MAX) break;
                __builtin_amdgcn_s_sleep(4);
            }
            STAMP(ston, l * 5 + 3, 7);
            const float mean0 = fq_sum(t[0]) * (1.0f / FH), mean1 = fq_sum(t[2]) * (1.0f / FH);
            const float r0 = __builtin_amdgcn_rsqf(fmaxf(fq_sum(t[1]) * (1.0f / FH) - mean0 * mean0, 0.f) + LN_EPS);
            const float r1 = __builtin_amdgcn_rsqf(fmaxf(fq_sum(t[3]) * (1.0f / FH) - mean1 * mean1, 0.f) + LN_EPS);
            LAS float* Z = (LAS float*)(lds + L_Z) + w * 64;
            if (lane < 16) { *(LAS f32x2*)(Z + lane * 2) = (f32x2){r0, r1}; *(LAS f32x2*)(Z + 32 + lane * 2) = (f32x2){-mean0 * r0, -mean1 * r1}; }
            WAITCNT(63, 0);
            const f32x4 ra = *(const LAS f32x4*)(Z + fq * 8), rb = *(const LAS f32x4*)(Z + fq * 8 + 4), ma = *(const LAS f32x4*)(Z + 32 + fq * 8), mb = *(const LAS f32x4*)(Z + 32 + fq * 8 + 4);
            const __amdgpu_buffer_rsrc_t rC = rsrc((u16*)(ws + W_Z2T + l * 8 * MB) + (size_t)tn * 112 * S_ + tm * 128 + w * 32, 112 * S_ * 2 - tm * 256 - w * 64);
#pragma unroll
            for (int n = 0; n < 7; ++n)
                __builtin_amdgcn_raw_buffer_store_b128(pk8(acc[0][7 + n] * ra + ma, acc[1][7 + n] * rb + mb), rC, (n * 16 + fr) * S_ * 2 + fq * 16, 0, STORE_AUX);
            dep_signal(c + 2 * 8192, (lane * 16 + tn) * 32 + tm, 16, lds, lane);
            STAMP(true, l * 5 + 3, 8 + w);
        }
        {
            PHASE_VARS();
            float b[2];
#pragma unroll
            for (int m = 0; m < 2; ++m) b[m] = bf_lo(bsp[tm * 128 + w * 32 + perm32(m * 16 + fr)]);
            f32x4 acc[2][7];
            compute_phase<CG4, 2, 7, 7, false, 4, 5>(lds, w * 32, 0, lane, acc, [](int) {} ST_PASS(l * 5 + 4));
            const __amdgpu_buffer_rsrc_t rC = rsrc((u16*)(ws + W_H + l * 8 * MB) + (size_t)(tm * 128 + w * 32) * FH, 32 * FH * 2);
#pragma unroll
            for (int m = 0; m < 2; ++m) {
                const int ro = perm32(m * 16 + fr) * FH * 2;
#pragma unroll
                for (int q = 0; q < 3; ++q)
                    __builtin_amdgcn_raw_buffer_store_b128(pk8(unpk4(zz[2 * q][m * 2], zz[2 * q][m * 2 + 1]) * (acc[m][2 * q] + b[m]), unpk4(zz[2 * q + 1][m * 2], zz[2 * q + 1][m * 2 + 1]) * (acc[m][2 * q + 1] + b[m])),
                                                           rC, ro + q * 64 + fq * 16, tn * 192, STORE_AUX);
                const f32x4 v = unpk4(zz[6][m * 2], zz[6][m * 2 + 1]) * (acc[m][6] + b[m]);
                __builtin_amdgcn_raw_buffer_store_b64((u32x2){pk_bf16(v[0], v[1]), pk_bf16(v[2], v[3])}, rC, ro + 3072 + fq * 8, tn * 32, STORE_AUX);
            }
            dep_signal(c + 3 * 8192, (tm * 16 + lane) * 32 + tn, 16, lds, lane);
            STAMP(true, l * 5 + 4, 8 + w);
        }
        {
            PHASE_VARS();
            res_phase<CG5>(lds, xa, l == 0 ? (u16*)(ws + W_XB) : out, sp + 131072, tm, tn, w, lane, [](int) {} ST_PASS(l * 5 + 5));
            dep_signal(c + 4 * 8192, (lane * 16 + tm) * 32 + tn, 16, lds, lane);
            STAMP(true, l * 5 + 5, 8 + w);
        }
    }
    asm volatile(".Lk_main_e1:");
}

extern "C" void kernel_launch(void* const* d_in, const int* in_sizes, int n_in, void* d_out, int out_size, void* d_ws, size_t ws_size, hipStream_t stream) {
    const u16* x0 = (const u16*)d_in[0];
    const u16* wsre = (const u16*)d_in[1];
    const u16* wsim = (const u16*)d_in[2];
    const u16* wdre = (const u16*)d_in[3];
    const u16* wdim = (const u16*)d_in[4];
    const u16* win = (const u16*)d_in[6];
    const u16* wsp = (const u16*)d_in[7];
    const u16* bsp = (const u16*)d_in[8];
    const u16* wout = (const u16*)d_in[9];
    char* ws = (char*)d_ws;
    (void)hipFuncSetAttribute((const void*)k_main, hipFuncAttributeMaxDynamicSharedMemorySize, LDS_TOTAL);
    k_main<<<256, 512, LDS_TOTAL, stream>>>(x0, wsre, wsim, wdre, wdim, win, wsp, bsp, wout, ws, (u16*)d_out);
}
```

```cpp
#define NREP 1
#define TILE_AUX 2
#define PFSTART 2
#include <hip/hip_runtime.h>
#include <hip/hip_bf16.h>
#include <utility>

typedef unsigned short u16;
typedef unsigned long long u64;
typedef short bf16x8 __attribute__((ext_vector_type(8)));
typedef short s16x4 __attribute__((ext_vector_type(4)));
typedef float f32x2 __attribute__((ext_vector_type(2)));
typedef float f32x4 __attribute__((ext_vector_type(4)));
typedef unsigned u32x2 __attribute__((ext_vector_type(2)));
typedef unsigned u32x4 __attribute__((ext_vector_type(4)));
#define LAS __attribute__((address_space(3)))
#define GAS __attribute__((address_space(1)))
#define DEV __device__ __forceinline__

constexpr int waitimm(int vm, int lgkm) { return (vm & 15) | (7 << 4) | ((lgkm & 15) << 8) | ((vm >> 4) << 14); }
#define WAITCNT(vm, lgkm) __builtin_amdgcn_s_waitcnt(waitimm(vm, lgkm))
#define BAR() do { asm volatile("" ::: "memory"); __builtin_amdgcn_s_barrier(); asm volatile("" ::: "memory"); } while (0)
#define SB0() __builtin_amdgcn_sched_barrier(0)
#define STAMP(on, ph, i)
#define ST_PARAM
#define ST_PASS(p)

constexpr int S_ = 2048, D_ = 1024, F_ = 3584, FH = 1792;
#define T_IM 12
#define T_IN 0
#define T_STEP 2
#define T_OUT 20
#define T_SIG 40
#define XM2 ~0u
#define XM16 ~0u
#define POLL_SLEEP 2
constexpr float LN_EPS = 1e-5f;
constexpr int SPIN_MAX = 1 << 17;
constexpr int STORE_AUX = 16;

DEV unsigned pk_bf16(float lo, float hi) { unsigned r; asm("v_cvt_pk_bf16_f32 %0, %1, %2" : "=v"(r) : "v"(lo), "v"(hi)); return r; }
DEV float bf_lo(unsigned v) { return __uint_as_float(v << 16); }
DEV float bf_hi(unsigned v) { return __uint_as_float(v & 0xffff0000u); }
DEV __amdgpu_buffer_rsrc_t rsrc(const void* p, unsigned bytes) { return __builtin_amdgcn_make_buffer_rsrc((void*)p, 0, bytes, 0x00020000); }
DEV f32x4 gelu_tanh(f32x4 x) {
    const float c0 = -2.3022081986f, c1 = c0 * 0.044715f;
    const f32x4 a = x * (x * x * c1 + c0);
    f32x4 d = (f32x4){__builtin_amdgcn_exp2f(a[0]), __builtin_amdgcn_exp2f(a[1]), __builtin_amdgcn_exp2f(a[2]), __builtin_amdgcn_exp2f(a[3])} + 1.0f;
    return x * (f32x4){__builtin_amdgcn_rcpf(d[0]), __builtin_amdgcn_rcpf(d[1]), __builtin_amdgcn_rcpf(d[2]), __builtin_amdgcn_rcpf(d[3])};
}
DEV u32x4 pk8(f32x4 a, f32x4 b) { return (u32x4){pk_bf16(a[0], a[1]), pk_bf16(a[2], a[3]), pk_bf16(b[0], b[1]), pk_bf16(b[2], b[3])}; }
DEV f32x4 unpk4(unsigned a, unsigned b) { return (f32x4){bf_lo(a), bf_hi(a), bf_lo(b), bf_hi(b)}; }
__host__ __device__ constexpr int perm32(int slot) { return 8 * ((slot & 15) >> 2) + 4 * ((slot >> 4) & 1) + (slot & 3); }
__host__ __device__ constexpr int inv32(int row) { return 16 * ((row >> 2) & 1) + 4 * ((row >> 3) & 3) + (row & 3); }
DEV float fq_sum(float a) {
    const auto p = __builtin_amdgcn_permlane16_swap(__float_as_uint(a), __float_as_uint(a), false, false);
    a = __uint_as_float(p[0]) + __uint_as_float(p[1]);
    const auto q = __builtin_amdgcn_permlane32_swap(__float_as_uint(a), __float_as_uint(a), false, false);
    return __uint_as_float(q[0]) + __uint_as_float(q[1]);
}
template <int CTRL> DEV float dpp_mov(float a) { return __builtin_bit_cast(float, __builtin_amdgcn_update_dpp(0, __builtin_bit_cast(int, a), CTRL, 0xf, 0xf, true)); }
template <int CTRL> DEV float dpp_add(float a) { return a + __builtin_bit_cast(float, __builtin_amdgcn_update_dpp(0, __builtin_bit_cast(int, a), CTRL, 0xf, 0xf, true)); }
DEV float row16_sum(float a) { a = dpp_add<0x128>(a); a = dpp_add<0x124>(a); a = dpp_add<0x122>(a); return dpp_add<0x121>(a); }
DEV float oct_sum(float a) { a = dpp_add<0xb1>(a); a = dpp_add<0x4e>(a); return dpp_add<0x141>(a); }
DEV float quad_sum(float a) { a = dpp_add<0xb1>(a); return dpp_add<0x4e>(a); }
constexpr unsigned XCH_EMPTY = 0xffffffffu;
DEV unsigned xch_word(float v) { const unsigned u = __float_as_uint(v); return u == XCH_EMPTY ? 0x7fc00000u : u; }

DEV int opaque_v(int v) { asm volatile("" : "+v"(v)); return v; }
DEV int opaque_s(int v) { asm volatile("" : "+s"(v)); return v; }
template <class F, int... I> DEV void static_for_impl(F&& f, std::integer_sequence<int, I...>) { (f(std::integral_constant<int, I>{}), ...); }
template <int N, class F> DEV void static_for(F&& f) { static_for_impl(f, std::make_integer_sequence<int, N>{}); }

template <int BM_, int BN_, int NS_, int BASE_, int LDP_, int LDQ_, int NT_, int KSW_, bool PERMP_, bool PERMQ_, bool WISP_> struct Cfg {
    static constexpr int BM = BM_, BN = BN_, NS = NS_, LDP = LDP_, LDQ = LDQ_, NT = NT_, KSW = KSW_;
    static constexpr bool PERMP = PERMP_, PERMQ = PERMQ_, WISP = WISP_;
    static constexpr int PB = BM * 128, QB = BN * 128, PBASE = BASE_, QBASE = PBASE + NS * PB, RING = QBASE + NS * QB;
    static constexpr int LP = BM / 32, LQ = BN / 32;
    static constexpr int LW = WISP ? LP : LQ, LA = WISP ? LQ : LP;
};
constexpr int RING_MAX = 147456;
constexpr int L_R = RING_MAX, L_M = L_R + 512, L_WD = L_M + 512, L_WS = L_WD + 512, L_Z = L_WS + 1024, L_JUNK = L_Z + 1024, L_SYNC = L_JUNK + 256, L_TAG = L_SYNC + 48, LDS_TOTAL = L_SYNC + 64;
constexpr int L_TIN0 = 0, L_TIN1 = 131072, L_TOUT = 114688;

template <class C, int N0, int N1, class Dep, class Mid, class Tail>
DEV void loader_phase(LAS unsigned char* lds, const u16* P0, const u16* P1, const u16* Q, int lw, int lane, unsigned drained, Dep&& dep, Mid&& mid, Tail&& tail ST_PARAM) {
    constexpr int NS = C::NS, NT = C::NT, LA = C::LA, LW = C::LW;
    static_assert(C::RING <= RING_MAX && NS >= 3 && NT >= 2 * NS && C::BM % 32 == 0 && !(C::PERMQ && C::BN % 32), "");
    lane = opaque_v(lane); lw = opaque_s(lw);
    const __amdgpu_buffer_rsrc_t rP0 = rsrc(P0, C::BM * C::LDP * 2), rP1 = rsrc(P1, C::BM * C::LDP * 2), rQ = rsrc(Q, C::BN * C::LDQ * 2);
    const int ch = ((lane & 7) ^ (((lw & 1) * 4 + (lane >> 4)) & 7)) * 16, row = lw * 8 + (lane >> 3);
    const int vP = (C::PERMP ? perm32(row) : row) * C::LDP * 2 + ch, vQ = (C::PERMQ ? perm32(row) : row) * C::LDQ * 2 + ch;
    auto issueP = [&](int slot, int kt) {
        LAS unsigned char* dst = lds + C::PBASE + slot * C::PB + lw * 1024;
        const bool second = C::KSW && kt >= C::KSW;
        const __amdgpu_buffer_rsrc_t rP = second ? rP1 : rP0;
        const int kp = (second ? kt - C::KSW : kt) * 128;
#pragma unroll
        for (int i = 0; i < C::LP; ++i) __builtin_amdgcn_raw_ptr_buffer_load_lds(rP, (LAS void*)(dst + i * 4096), 16, vP, i * 32 * C::LDP * 2 + kp, 0, C::WISP ? 0 : 16);
    };
    auto issueQ = [&](int slot, int kt) {
        LAS unsigned char* dst = lds + C::QBASE + slot * C::QB + lw * 1024;
#pragma unroll
        for (int j = 0; j < (C::BN + 31) / 32; ++j)
            if (j < C::LQ || j * 4 + lw < C::BN / 8) __builtin_amdgcn_raw_ptr_buffer_load_lds(rQ, (LAS void*)(dst + j * 4096), 16, vQ, j * 32 * C::LDQ * 2 + kt * 128, 0, 16);
    };
    auto issueW = [&](int slot, int kt) { if (C::WISP) issueP(slot, kt); else issueQ(slot, kt); };
    auto issueA = [&](int slot, int kt) { if (C::WISP) issueQ(slot, kt); else issueP(slot, kt); };
    if (drained) {
        while ((unsigned)__builtin_amdgcn_readfirstlane(*(volatile LAS unsigned*)(lds + L_SYNC + 16)) < drained) __builtin_amdgcn_s_sleep(2);
#pragma unroll 1
        for (int s = 0; s < NS; ++s) issueW(s, s);
        dep();
        STAMP(ston, ph, 4);
        issueA(0, 0); issueA(1, 1);
        WAITCNT(LA, 15);
        BAR();
#pragma unroll
        for (int s = 2; s < NS; ++s) issueA(s, s);
    } else {
        issueW(0, 0); issueA(0, 0); issueW(1, 1); issueA(1, 1);
        STAMP(ston, ph, 4);
        WAITCNT(LA + LW, 15);
        BAR();
#pragma unroll 1
        for (int s = 2; s < NS; ++s) { issueW(s, s); issueA(s, s); }
    }
    static_for<NS - 2>([&](auto ic) {
        constexpr int t = decltype(ic)::value;
        WAITCNT((NS - 2 - t) * LA + t * (LA + LW), 15);
        BAR();
        issueW(t, t + NS); issueA(t, t + NS);
    });
    int sl = NS - 2;
#pragma unroll 1
    for (int t = NS - 2; t < NT - NS; ++t) {
        WAITCNT((NS - 2) * (LA + LW), 15);
        BAR();
        issueW(sl, t + NS); issueA(sl, t + NS);
        sl = (sl + 1 == NS) ? 0 : sl + 1;
        mid(t);
    }
    tail(std::integral_constant<int, 0>{});
    static_for<NS - 1>([&](auto ic) {
        constexpr int i = decltype(ic)::value;
        WAITCNT((NS - 2 - i) * (LA + LW) + N0 + (i ? N1 : 0), 15);
        BAR();
        if constexpr (i == 0) tail(std::integral_constant<int, 1>{});
    });
    tail(std::integral_constant<int, 2>{});
    WAITCNT(63, 0);
    BAR();
}

template <class C, int MT, int NTL, int NSWAP, bool ROLL, int RNUM, int RDEN, class Hook>
DEV void compute_phase(LAS unsigned char* lds, int prow0, int qrow0, int lane, f32x4 (&acc)[MT][NTL], Hook&& hook ST_PARAM) {
    constexpr int NR = MT + NTL, NM = MT * NTL, NT = C::NT;
    const int fr = lane & 15, fq = lane >> 4;
    int aoff[2], boff[2];
#pragma unroll
    for (int k = 0; k < 2; ++k) {
        const int ch = ((k * 4 + fq) ^ ((fr >> 1) & 7)) * 16;
        aoff[k] = C::PBASE + (prow0 + fr) * 128 + ch;
        boff[k] = C::QBASE + (qrow0 + fr) * 128 + ch;
    }
#pragma unroll
    for (int m = 0; m < MT; ++m)
#pragma unroll
        for (int n = 0; n < NTL; ++n) { acc[m][n] = (f32x4){0.f, 0.f, 0.f, 0.f}; asm volatile("" : "+v"(acc[m][n])); }
    auto mfma = [&](int m, int n, const bf16x8& a, const bf16x8& b) {
        if (n >= NSWAP) asm volatile("v_mfma_f32_16x16x32_bf16 %0, %1, %2, %0" : "+v"(acc[m][n]) : "v"(a), "v"(b));
        else asm volatile("v_mfma_f32_16x16x32_bf16 %0, %1, %2, %0" : "+v"(acc[m][n]) : "v"(b), "v"(a));
    };
    int sp = 0, sq = 0;
    auto next = [&]() { sp = (sp + C::PB == C::NS * C::PB) ? 0 : sp + C::PB; sq = (sq + C::QB == C::NS * C::QB) ? 0 : sq + C::QB; };
    BAR();
    STAMP(ston, ph, 1);
    if constexpr (!ROLL) {
        bf16x8 fa[4][MT], fb[4][NTL];
        auto rd1 = [&](int set, int k, int i) {
            if (i == 0) fa[set][0] = *(const LAS bf16x8*)(lds + sp + aoff[k]);
            else if (i <= NTL) fb[set][i - 1] = *(const LAS bf16x8*)(lds + sq + boff[k] + (i - 1) * 2048);
            else fa[set][i - NTL] = *(const LAS bf16x8*)(lds + sp + aoff[k] + (i - NTL) * 2048);
        };
        auto stg = [&](int set, bool rd) {
#pragma unroll
            for (int i = 0; i < 2 * NM; ++i) {
                const int st = set + i / NM, m = (i % NM) / NTL, n = i % NTL;
                mfma(m, n, fa[st][m], fb[st][n]); SB0();
                if (rd) {
#pragma unroll
                    for (int r = (i * RNUM + RDEN - 1) / RDEN; r < ((i + 1) * RNUM + RDEN - 1) / RDEN; ++r) if (r < 2 * NR) rd1((set ^ 2) + r / NR, r / NR, r % NR);
                    SB0();
                }
            }
        };
        static_assert((2 * NM * RNUM + RDEN - 1) / RDEN >= 2 * NR && NT % 2 == 0, "");
#pragma unroll
        for (int r = 0; r < 2 * NR; ++r) rd1(r / NR, r / NR, r % NR);
#pragma unroll 1
        for (int t = 0; t < NT - 2; t += 2) {
            next(); WAITCNT(63, 0); BAR(); SB0();
            stg(0, true);
            next(); WAITCNT(63, 0); BAR(); SB0();
            stg(2, true);
            hook(t); SB0();
        }
        next();
        stg(0, false);
        BAR(); SB0();
#pragma unroll
        for (int r = 0; r < 2 * NR; ++r) rd1(2 + r / NR, r / NR, r % NR);
        WAITCNT(63, 0); BAR(); SB0();
        stg(2, false);
    } else {
        static_assert(MT == 2, "");
        constexpr int G = 8;
        bf16x8 fa[2][2], fb[NTL];
        auto rda = [&](int set, int k, int m) { fa[set][m] = *(const LAS bf16x8*)(lds + sp + aoff[k] + m * 2048); };
        auto rdb = [&](int k, int n) { fb[n] = *(const LAS bf16x8*)(lds + sq + boff[k] + n * 2048); };
        auto half0 = [&]() {
#pragma unroll
            for (int j = 0; j < NM; ++j) {
                mfma(j & 1, j >> 1, fa[0][j & 1], fb[j >> 1]); SB0();
                if (j & 1) rdb(1, j >> 1); else if (j < 4) rda(1, 1, j >> 1);
                SB0();
            }
        };
        auto half1 = [&](bool rd) {
#pragma unroll
            for (int j = 0; j < NM; ++j) {
                if (j == G) { next(); WAITCNT(63, 0); BAR(); SB0(); }
                mfma(j & 1, j >> 1, fa[1][j & 1], fb[j >> 1]); SB0();
                if (rd && j >= G) {
                    if (j < G + 2) rda(0, 0, j - G);
#pragma unroll
                    for (int n = 0; n < NTL; ++n) if ((G + 2 + n > 2 * n + 1 ? G + 2 + n : 2 * n + 1) == j) rdb(0, n);
                    SB0();
                }
            }
        };
        rda(0, 0, 0); rda(0, 0, 1);
#pragma unroll
        for (int n = 0; n < NTL; ++n) rdb(0, n);
        SB0();
#pragma unroll 1
        for (int t = 0; t < NT - 1; ++t) { half0(); half1(true); }
        half0(); half1(false);
    }
    asm volatile("s_nop 15" ::: "memory");
#pragma unroll
    for (int m = 0; m < MT; ++m)
#pragma unroll
        for (int n = 0; n < NTL; ++n) asm volatile("" : "+v"(acc[m][n]));
    STAMP(ston, ph, 2);
}

DEV u64 launch_tag(LAS unsigned char* lds) { return *(volatile LAS u64*)(lds + L_TAG); }
DEV void box_poll(const u64* box, int nprod, u64 tag, int lane) {
#pragma unroll 1
    for (int spin = 0; spin < SPIN_MAX; ++spin) {
        const u64 v = __hip_atomic_load((const GAS u64*)box + (lane & (nprod - 1)), __ATOMIC_RELAXED, __HIP_MEMORY_SCOPE_AGENT);
        if (__all(v == tag)) break;
        __builtin_amdgcn_s_sleep(POLL_SLEEP);
    }
}
DEV void dep_wait_(const u64* box, int nprod, LAS unsigned char* lds, unsigned epoch, int lw, int lane, int rp) {
    volatile LAS unsigned* flag = (volatile LAS unsigned*)(lds + L_SYNC);
    if (lw == 0) {
        box_poll(box, nprod, launch_tag(lds) + rp, lane);
        *flag = epoch;
    } else {
        while ((unsigned)__builtin_amdgcn_readfirstlane(*flag) < epoch) __builtin_amdgcn_s_sleep(1);
    }
    asm volatile("" ::: "memory");
}
template <int CNT = 16> DEV void dep_signal_(u64* boxes, int word, int ncons, LAS unsigned char* lds, int lane, int rp) {
    const u64 tag = launch_tag(lds) + rp;
    asm volatile("s_waitcnt vmcnt(0)" ::: "memory");
    unsigned old = 0;
    if (lane == 0) old = __hip_atomic_fetch_add((LAS unsigned*)(lds + L_SYNC + CNT), 1u, __ATOMIC_RELAXED, __HIP_MEMORY_SCOPE_WORKGROUP);
    if ((__builtin_amdgcn_readfirstlane(old) & 3) == 3 && lane < ncons) __hip_atomic_store((GAS u64*)boxes + word, tag, __ATOMIC_RELAXED, __HIP_MEMORY_SCOPE_AGENT);
}
DEV void touch(LAS unsigned char* lds, const u16* p, unsigned bytes, int off) {
    __builtin_amdgcn_raw_ptr_buffer_load_lds(rsrc(p, bytes), (LAS void*)(lds + L_JUNK), 4, off, 0, 0, 0);
}

#define dep_wait(b, n, ld, e, lw, ln) dep_wait_(b, n, ld, (e) + rp * 10, lw, ln, rp)
#define dep_signal(b, w, n, ld, ln) dep_signal_(b, w, n, ld, ln, rp)
DEV void row_tables_load(const float* sp, unsigned bytes, int tid, f32x4 (&v)[4]) {
    const __amdgpu_buffer_rsrc_t r = rsrc(sp, bytes);
#pragma unroll
    for (int i = 0; i < 4; ++i) v[i] = __builtin_bit_cast(f32x4, __builtin_amdgcn_raw_buffer_load_b128(r, i * 4096 + tid * 16, 0, 16));
}
DEV void row_tables_finish(LAS unsigned char* lds, int tid, const f32x4 (&v)[4], float sign) {
#pragma unroll
    for (int i = 0; i < 4; ++i) {
        const float s = oct_sum(v[i][0] + v[i][2]), q = oct_sum(v[i][1] + v[i][3]);
        const float mean = s * (1.0f / D_), var = fmaxf(q * (1.0f / D_) - mean * mean, 0.f), r = sign * __builtin_amdgcn_rsqf(var + LN_EPS);
        if ((tid & 7) == 0) { ((LAS float*)(lds + L_R))[i * 32 + (tid >> 3)] = r; ((LAS float*)(lds + L_M))[i * 32 + (tid >> 3)] = -mean * r; }
    }
}

constexpr size_t MB = 1 << 20;
constexpr size_t W_T = 0, W_XA = 16 * MB, W_XB = 24 * MB, W_Z2T = 28 * MB, W_H = 44 * MB, W_WINT = 60 * MB, W_WOUTT = 68 * MB;
constexpr size_t W_SP = 72 * MB, W_GRAN = 74 * MB, W_WSPIN = 74 * MB + 65536, W_XCH = 74 * MB + 512 * 1024, W_CTR = 75 * MB, W_NONCE = 75 * MB + 768 * 1024;

typedef Cfg<128, 128, 4, 0, D_, D_, D_ / 64, 0, false, true, true> CG1;
typedef Cfg<128, 64, 4, 16384, S_, 4096, 64, 32, false, false, true> CG2;
typedef Cfg<128, 224, 3, 0, D_, D_, D_ / 64, 0, true, false, false> CG3;
typedef Cfg<128, 112, 4, 0, S_, S_, S_ / 64, 0, true, false, true> CG4;
typedef Cfg<128, 64, 4, 0, FH, FH, FH / 64, 0, false, false, false> CG5;

template <class C, class Hook>
DEV void res_phase(LAS unsigned char* lds, const u16* res, u16* out, float* sp, int tm, int tn, int w, int lane, Hook&& hook ST_PARAM) {
    const int fr = lane & 15, fq = lane >> 4;
    const __amdgpu_buffer_rsrc_t rX = rsrc(res + (size_t)(tm * 128 + w * 32) * D_ + tn * 64, 32 * D_ * 2 - tn * 128);
    u32x4 xr[2][2];
#pragma unroll
    for (int m = 0; m < 2; ++m)
#pragma unroll
        for (int h = 0; h < 2; ++h) xr[m][h] = __builtin_amdgcn_raw_buffer_load_b128(rX, (m * 16 + fr) * D_ * 2 + h * 64 + fq * 16, 0, 16);
    f32x4 acc[2][4];
    compute_phase<C, 2, 4, 4, false, 1, 1>(lds, w * 32, 0, lane, acc, hook ST_PASS(ph));
    const __amdgpu_buffer_rsrc_t rC = rsrc(out + (size_t)(tm * 128 + w * 32) * D_ + tn * 64, 32 * D_ * 2 - tn * 128);
    const __amdgpu_buffer_rsrc_t rS = rsrc(sp + (size_t)(tm * 128 + w * 32) * 32, 32 * 128);
#pragma unroll
    for (int m = 0; m < 2; ++m) {
        f32x4 s = (f32x4){0.f, 0.f, 0.f, 0.f}, qq = s;
#pragma unroll
        for (int h = 0; h < 2; ++h) {
            const u32x4 o = pk8(acc[m][2 * h] + unpk4(xr[m][h][0], xr[m][h][1]), acc[m][2 * h + 1] + unpk4(xr[m][h][2], xr[m][h][3]));
            __builtin_amdgcn_raw_buffer_store_b128(o, rC, (m * 16 + fr) * D_ * 2 + h * 64 + fq * 16, 0, STORE_AUX);
            const f32x4 a = unpk4(o[0], o[1]), b = unpk4(o[2], o[3]);
            s += a + b; qq += a * a + b * b;
        }
        const float st = fq_sum((s[0] + s[1]) + (s[2] + s[3])), qt = fq_sum((qq[0] + qq[1]) + (qq[2] + qq[3]));
        if (fq == 0) __builtin_amdgcn_raw_buffer_store_b64((u32x2){__float_as_uint(st), __float_as_uint(qt)}, rS, (m * 16 + fr) * 128 + tn * 8, 0, 16);
    }
}

__global__ __launch_bounds__(512, 2) void k_main(const u16* __restrict__ x0, const u16* __restrict__ wsre, const u16* __restrict__ wsim, const u16* __restrict__ wdre,
                                                 const u16* __restrict__ wdim, const u16* __restrict__ win, const u16* __restrict__ wsp, const u16* __restrict__ bsp,
                                                 const u16* __restrict__ wout, char* ws, u16* out) {
    extern __shared__ __attribute__((aligned(1024))) unsigned char shm[];
    LAS unsigned char* lds = (LAS unsigned char*)shm;
    const int tid = threadIdx.x, lane0 = tid & 63, w0 = __builtin_amdgcn_readfirstlane(tid >> 6);
    const int tm0 = ((blockIdx.x & 7) >> 1) * 4 + (blockIdx.x >> 6), tn0 = (blockIdx.x & 1) * 8 + ((blockIdx.x >> 3) & 7);
#define PHASE_VARS() const int lane = opaque_v(lane0), w = opaque_s(w0), tm = opaque_s(tm0), tn = opaque_s(tn0), fr = lane & 15, fq = lane >> 4; (void)fr; (void)fq; (void)w
    u64* ctr = (u64*)(ws + W_CTR);
    if (w0 >= 4) {
        const int lw = w0 - 4;
        auto none = [&](auto) {};
#pragma unroll 1
        for (int ll = 0; ll < 2 * NREP; ++ll) { const int l = ll & 1, rp = ll >> 1;
            const u16* x = l == 0 ? x0 : (const u16*)(ws + W_XB);
            const u16* T = (const u16*)(ws + W_T + l * 8 * MB); const u16* xa = (const u16*)(ws + W_XA + l * 4 * MB);
            const u16* z2t = (const u16*)(ws + W_Z2T + l * 8 * MB); const u16* h = (const u16*)(ws + W_H + l * 8 * MB);
            const float* sp = (const float*)(ws + W_SP + l * 512 * 1024);
            const u64* c = ctr + l * 5 * 8192;
            const unsigned first0 = l == 0 ? ~0u : 0u, first = ll == 0 ? ~0u : 0u;
            f32x4 v[4], u[4];
            { PHASE_VARS(); const int lt = (w - 4) * 64 + lane;
            const u16* pw = (tm < 8 ? wdre : wdim) + (size_t)(tm & 7) * 128 * D_;
            const __amdgpu_buffer_rsrc_t rg = rsrc(ws + W_GRAN, 65536 & first0);
            const int goff = (lt < 128 ? tn * 128 + lt : 1920 + tm * 128 + lt) * 16;
            u32x4 g;
            loader_phase<CG1, 6, 7>(lds, pw, pw, x + (size_t)tn * 128 * D_, lw, lane, ll * 20, [&]() { dep_wait_(ctr + (l ? 4 : 9) * 8192 + (tm * 16 + tn) * 32, 16, lds, ll * 5, lw, lane, l ? rp : rp - 1); }, none, [&](auto ic) {
                constexpr int i = decltype(ic)::value;
                if constexpr (i == 0) {
                    row_tables_load(sp + tn * 128 * 32, 16384 & ~first0, lt, v);
                    g = __builtin_amdgcn_raw_buffer_load_b128(rg, goff, 0, 16);
                    touch(lds, wsre + (size_t)(tm * 128 + tn * 8 + lw * 2) * S_, (PFSTART & 1) ? 0 : 8192 & first, lane * 128);
                } else if constexpr (i == 1) {
                    const __amdgpu_buffer_rsrc_t r = rsrc(win + (size_t)(tm * 64 + (lw >> 1) * 32) * F_, (32 * F_ * 2) & first & XM2);
#pragma unroll
                    for (int j = 0; j < 7; ++j) {
                        const int q = (lw & 1) * 7 + j, cb = q * 64 + lane, row = cb / 28, ch = cb - row * 28;
                        __builtin_amdgcn_raw_ptr_buffer_load_lds(r, (LAS void*)(lds + (lw < 2 ? L_TIN0 : L_TIN1) + q * 1024), 16, row * F_ * 2 + (ch < 14 ? ch * 16 : FH * 2 - 224 + ch * 16), tn * 224, 0, TILE_AUX);
                    }
                } else {
                    const float sign = tm < 8 ? 1.0f : -1.0f;
                    if (l) { row_tables_finish(lds, lt, v, sign); return; }
                    const u64 tag = launch_tag(lds);
                    if (!__all(g[1] == (unsigned)tag && g[3] == (unsigned)(tag >> 32))) {
                        asm volatile("" ::: "memory");
#pragma unroll 1
                        for (int spin = 0; spin < SPIN_MAX; ++spin) {
                            g = __builtin_amdgcn_raw_buffer_load_b128(rg, goff, 0, 16);
                            if (__all(g[1] == (unsigned)tag && g[3] == (unsigned)(tag >> 32))) break;
                            __builtin_amdgcn_s_sleep(POLL_SLEEP);
                        }
                    }
                    const float mean = __uint_as_float(g[0]) * (1.0f / D_), var = fmaxf(__uint_as_float(g[2]) * (1.0f / D_) - mean * mean, 0.f), rs = sign * __builtin_amdgcn_rsqf(var + LN_EPS);
                    if (lt < 128) { ((LAS float*)(lds + L_R))[lt] = rs; ((LAS float*)(lds + L_M))[lt] = -mean * rs; }
                    else ((LAS float*)(lds + L_WD))[lt - 128] = __uint_as_float(g[0]);
                }
            } ST_PASS(l * 5 + 1)); }
            { PHASE_VARS();
            u64 tw;
            const u64* tbox = ctr + 8192 + (tm * 16 + tn) * 32 + 16;
            loader_phase<CG2, 1, 0>(lds, wsre + (size_t)tm * 128 * S_, wsim + (size_t)tm * 128 * S_, T + (size_t)tn * 64 * 4096, lw, lane, ll * 20 + 4,
                              [&]() { dep_wait(c + (tm * 16 + tn) * 32, 32, lds, l * 5 + 1, lw, lane); },
                              [&](int t) {
                                  if (t != T_IM || ll) return;
                                  if (!(PFSTART & 2)) touch(lds, wsim + (size_t)(tm * 128 + tn * 8 + lw * 2) * S_, 8192, lane * 128);
                                  const __amdgpu_buffer_rsrc_t r = rsrc(wout + (size_t)tm * 112 * D_, (112 * D_ * 2) & XM2);
#pragma unroll
                                  for (int j = 0; j < 4; ++j)
                                      if (j < 3 || lw < 2) __builtin_amdgcn_raw_ptr_buffer_load_lds(r, (LAS void*)(lds + L_TOUT + (j * 4 + lw) * 1024), 16, ((j * 4 + lw) * 8 + (lane >> 3)) * D_ * 2 + (lane & 7) * 16, tn * 128, 0, TILE_AUX);
                              },
                              [&](auto ic) {
                                  constexpr int i = decltype(ic)::value;
                                  if constexpr (i == 0) tw = __hip_atomic_load((const GAS u64*)tbox + (lane & 15), __ATOMIC_RELAXED, __HIP_MEMORY_SCOPE_AGENT);
                                  if constexpr (i == 2) { const u64 tag = launch_tag(lds); if (!__all(tw == tag)) box_poll(tbox, 16, tag, lane); }
                              } ST_PASS(l * 5 + 2)); }
            { PHASE_VARS(); const int lt = (w - 4) * 64 + lane;
            loader_phase<CG3, 9, 0>(lds, xa + (size_t)tm * 128 * D_, xa, (const u16*)(ws + W_WINT) + (size_t)tn * 224 * D_, lw, lane, ll * 20 + 8,
                              [&]() { dep_wait(c + 8192 + (tm * 16 + tn) * 32, 16, lds, l * 5 + 2, lw, lane);
                                      if (blockIdx.x == 0 && ll == 0 && lt == 0) __hip_atomic_store((GAS u64*)(ws + W_NONCE), launch_tag(lds), __ATOMIC_RELAXED, __HIP_MEMORY_SCOPE_AGENT); },
                              none, [&](auto ic) {
                                  constexpr int i = decltype(ic)::value;
                                  if constexpr (i == 0) {
                                      row_tables_load(sp + 65536 + tm * 128 * 32, 16384, lt, v);
                                      const __amdgpu_buffer_rsrc_t r = rsrc(ws + W_WSPIN + (size_t)tn * 224 * 64, 224 * 64);
#pragma unroll
                                      for (int j = 0; j < 4; ++j) u[j] = __builtin_bit_cast(f32x4, __builtin_amdgcn_raw_buffer_load_b128(r, j * 4096 + lt * 16, 0, 16));
                                      touch(lds, wsp + (size_t)(tm * 128 + tn * 8 + lw * 2) * S_, 8192 & first, lane * 128);
                                  } else if constexpr (i == 2) {
                                      row_tables_finish(lds, lt, v, 1.0f);
#pragma unroll
                                      for (int j = 0; j < 4; ++j) { const float s = quad_sum((u[j][0] + u[j][1]) + (u[j][2] + u[j][3])); if ((lt & 3) == 0) ((LAS float*)(lds + L_WS))[j * 64 + (lt >> 2)] = s; }
                                  }
                              } ST_PASS(l * 5 + 3)); }
            { PHASE_VARS();
            loader_phase<CG4, 0, 0>(lds, wsp + (size_t)tm * 128 * S_, wsp, z2t + (size_t)tn * 112 * S_, lw, lane, ll * 20 + 12, [&]() { dep_wait(c + 2 * 8192 + (tm * 16 + tn) * 32, 16, lds, l * 5 + 3, lw, lane); }, none, none ST_PASS(l * 5 + 4)); }
            { PHASE_VARS();
            loader_phase<CG5, 0, 0>(lds, h + (size_t)tm * 128 * FH, h, (const u16*)(ws + W_WOUTT) + (size_t)tn * 64 * FH, lw, lane, ll * 20 + 16,
                              [&]() { dep_wait(c + 3 * 8192 + (tm * 16 + tn) * 32, 16, lds, l * 5 + 4, lw, lane); }, none, none ST_PASS(l * 5 + 5)); }
        }
        asm volatile(".Lk_main_e0:");
        return;
    }
    {
        PHASE_VARS();
        u64 z = __hip_atomic_load((const GAS u64*)(ws + W_NONCE), __ATOMIC_RELAXED, __HIP_MEMORY_SCOPE_AGENT);
        const int xr = tn * 128 + tm * 8 + w * 2, wr = (tm & 7) * 128 + tn * 8 + w * 2;
        const __amdgpu_buffer_rsrc_t rx = rsrc(x0 + (size_t)xr * D_, 4096), rw = rsrc((tm < 8 ? wdre : wdim) + (size_t)wr * D_, 4096);
        u32x4 a[4], b[4];
#pragma unroll
        for (int i = 0; i < 4; ++i) { a[i] = __builtin_amdgcn_raw_buffer_load_b128(rx, i * 1024 + lane * 16, 0, 0); b[i] = __builtin_amdgcn_raw_buffer_load_b128(rw, i * 1024 + lane * 16, 0, 0); }
        u64 pc; unsigned len;
        asm volatile("s_getpc_b64 %0\n.Lk_main_pc:\n\ts_max_i32 %1, .Lk_main_e0-.Lk_main_pc, 0\n\ts_max_i32 %1, .Lk_main_e1-.Lk_main_pc, %1" : "=s"(pc), "=s"(len) :: "scc");
        touch(lds, (const u16*)pc, len, (w * 64 + lane) * 128); touch(lds, (const u16*)pc, len, (256 + w * 64 + lane) * 128);
        if (PFSTART & 1) touch(lds, wsre + (size_t)(tm * 128 + tn * 8 + w * 2) * S_, 8192, lane * 128);
        if (PFSTART & 2) touch(lds, wsim + (size_t)(tm * 128 + tn * 8 + w * 2) * S_, 8192, lane * 128);
        STAMP(ston, 1, 5);
        if (tid < 128) __builtin_amdgcn_raw_buffer_store_b128((u32x4){XCH_EMPTY, XCH_EMPTY, XCH_EMPTY, XCH_EMPTY}, rsrc(ws + W_XCH + blockIdx.x * 2048, 2048), tid * 16, 0, 16);
        if (tid < 12) *(LAS unsigned*)(lds + L_SYNC + tid * 4) = 0u;
        z += 0x9E3779B97F4A7C15ull; z = (z ^ (z >> 30)) * 0xBF58476D1CE4E5B9ull; z = (z ^ (z >> 27)) * 0x94D049BB133111EBull; z ^= z >> 31;
        if (tid == 0) *(LAS u64*)(lds + L_TAG) = z;
        STAMP(ston, 1, 6);
        float g0 = 0.f, g1 = 0.f;
#pragma unroll
        for (int r = 0; r < 2; ++r) {
            f32x4 sx = (f32x4){0.f, 0.f, 0.f, 0.f}, qx = sx, sw = sx;
#pragma unroll
            for (int i = 0; i < 2; ++i) {
                const f32x4 a0 = unpk4(a[2 * r + i][0], a[2 * r + i][1]), a1 = unpk4(a[2 * r + i][2], a[2 * r + i][3]);
                sx += a0 + a1; qx += a0 * a0 + a1 * a1;
                sw += unpk4(b[2 * r + i][0], b[2 * r + i][1]) + unpk4(b[2 * r + i][2], b[2 * r + i][3]);
            }
            const float s1 = fq_sum(row16_sum((sx[0] + sx[1]) + (sx[2] + sx[3]))), q1 = fq_sum(row16_sum((qx[0] + qx[1]) + (qx[2] + qx[3]))), s2 = fq_sum(row16_sum((sw[0] + sw[1]) + (sw[2] + sw[3])));
            if (lane == r) { g0 = s1; g1 = q1; }
            if (lane == 2 + r) g0 = s2;
        }
        if (lane < 4) __builtin_amdgcn_raw_buffer_store_b128((u32x4){__float_as_uint(g0), (unsigned)z, __float_as_uint(g1), (unsigned)(z >> 32)}, rsrc(ws + W_GRAN, 65536),
                                                             (lane < 2 ? xr + lane : 2046 + (tm >> 3) * 1024 + wr + lane) * 16, 0, 16);
        STAMP(ston, 1, 7);
    }
    WAITCNT(63, 0);
    const LAS float* R = (const LAS float*)(lds + L_R); const LAS float* M = (const LAS float*)(lds + L_M); const LAS float* WS = (const LAS float*)(lds + L_WS);
#pragma unroll 1
    for (int ll = 0; ll < 2 * NREP; ++ll) { const int l = ll & 1, rp = ll >> 1;
        const u16* x = l == 0 ? x0 : (const u16*)(ws + W_XB);
        u16* xa = (u16*)(ws + W_XA + l * 4 * MB);
        float* sp = (float*)(ws + W_SP + l * 512 * 1024);
        u64* c = ctr + l * 5 * 8192;
        {
            PHASE_VARS();
            const int wm = w >> 1, wn = w & 1;
            f32x4 acc[4][4];
            compute_phase<CG1, 4, 4, 4, false, 2, 3>(lds, wm * 64, wn * 64, lane, acc, [](int) {} ST_PASS(l * 5 + 1));
            float wsum[4];
#pragma unroll
            for (int m = 0; m < 4; ++m) wsum[m] = ((const LAS float*)(lds + L_WD))[wm * 64 + m * 16 + fr];
            const __amdgpu_buffer_rsrc_t rC = rsrc((u16*)(ws + W_T + l * 8 * MB) + (size_t)(tm & 7) * 128 * 4096 + (tm < 8 ? 0 : 2048) + tn * 128, 128 * 4096 * 2 - (2048 + tn * 128) * 2);
#pragma unroll
            for (int q = 0; q < 2; ++q) {
                const int cc = wn * 64 + q * 32 + fq * 8;
                const f32x4 r0 = *(const LAS f32x4*)(R + cc), r1 = *(const LAS f32x4*)(R + cc + 4), m0 = *(const LAS f32x4*)(M + cc), m1 = *(const LAS f32x4*)(M + cc + 4);
#pragma unroll
                for (int m = 0; m < 4; ++m) {
                    const int row = wm * 64 + (m >> 1) * 32 + inv32((m & 1) * 16 + fr);
                    __builtin_amdgcn_raw_buffer_store_b128(pk8(acc[m][2 * q] * r0 + m0 * wsum[m], acc[m][2 * q + 1] * r1 + m1 * wsum[m]), rC, row * 8192 + cc * 2, 0, STORE_AUX);
                }
            }
            dep_signal(c, ((lane >> 1) * 16 + (tm & 7) * 2 + (lane & 1)) * 32 + (tm >> 3) * 16 + tn, 32, lds, lane);
            STAMP(true, l * 5 + 1, 8 + w);
        }
        {
            PHASE_VARS();
            res_phase<CG2>(lds, x, xa, sp + 65536, tm, tn, w, lane, [&](int t) {
                if (ll) return;
                auto rdt = [&](int at, int stride) {
                    LAS unsigned char* p = lds + at + (fr >> 2) * stride + (fr & 3) * 8;
                    const u32x2 a = __builtin_bit_cast(u32x2, __builtin_amdgcn_ds_read_tr16_b64_v4i16((LAS s16x4*)p)), b = __builtin_bit_cast(u32x2, __builtin_amdgcn_ds_read_tr16_b64_v4i16((LAS s16x4*)(p + 4 * stride)));
                    return (u32x4){a[0], a[1], b[0], b[1]};
                };
                const int g = (t - T_IN) / T_STEP * 4 + w, j = (t - T_OUT) / T_STEP;
                if (t >= T_IN && g < 14 && (t - T_IN) % T_STEP == 0) {
                    const __amdgpu_buffer_rsrc_t ri = rsrc((u16*)(ws + W_WINT) + (size_t)tn * 224 * D_, (224 * D_ * 2) & XM16), rs = rsrc(ws + W_WSPIN + (size_t)tn * 224 * 64, (224 * 64) & XM16);
                    const int half = g >= 7, cc = (g - half * 7) * 16 + fr, orow = half * 112 + (cc < 96 ? (cc & ~31) + inv32(cc & 31) : cc);
                    const u32x4 v0 = rdt(L_TIN0 + fq * 8 * 448 + g * 32, 448), v1 = rdt(L_TIN1 + fq * 8 * 448 + g * 32, 448);
                    __builtin_amdgcn_raw_buffer_store_b128(v0, ri, orow * D_ * 2 + fq * 16, tm * 128, STORE_AUX);
                    __builtin_amdgcn_raw_buffer_store_b128(v1, ri, orow * D_ * 2 + 64 + fq * 16, tm * 128, STORE_AUX);
                    const f32x4 sm = (unpk4(v0[0], v0[1]) + unpk4(v0[2], v0[3])) + (unpk4(v1[0], v1[1]) + unpk4(v1[2], v1[3]));
                    const float st = fq_sum((sm[0] + sm[1]) + (sm[2] + sm[3]));
                    if (fq == 0) __builtin_amdgcn_raw_buffer_store_b32(__float_as_uint(st), rs, orow * 64 + tm * 4, 0, STORE_AUX);
                } else if (t >= T_OUT && j < 2 && (t - T_OUT) % T_STEP == 0) {
                    const __amdgpu_buffer_rsrc_t ro = rsrc((u16*)(ws + W_WOUTT) + (size_t)tn * 64 * FH, (64 * FH * 2) & XM16);
                    const int orow = ((w >> 1) * 32 + inv32((w & 1) * 16 + fr)) * FH * 2, k0 = j * 64 + fq * 8, k1 = j ? 96 + (fq & 1) * 8 : 32 + fq * 8;
                    const u32x4 v0 = rdt(L_TOUT + k0 * 128 + w * 32, 128), v1 = rdt(L_TOUT + k1 * 128 + w * 32, 128);
                    __builtin_amdgcn_raw_buffer_store_b128(v0, ro, orow + (tm * 96 + k0) * 2, 0, STORE_AUX);
                    if (j == 0 || fq < 2) __builtin_amdgcn_raw_buffer_store_b128(v1, ro, orow + (j ? 1440 + tm * 16 + k1 : tm * 96 + k1) * 2, 0, STORE_AUX);
                } else if (t == T_SIG) {
                    dep_signal_<32>(ctr + 8192, (lane * 16 + tn) * 32 + 16 + tm, 16, lds, lane, 0);
                }
            } ST_PASS(l * 5 + 2));
            dep_signal(c + 8192, (tm * 16 + lane) * 32 + tn, 16, lds, lane);
            STAMP(true, l * 5 + 2, 8 + w);
        }
        u32x4 zz[7];
        {
            PHASE_VARS();
            f32x4 acc[2][14];
            compute_phase<CG3, 2, 14, 7, true, 1, 1>(lds, w * 32, 0, lane, acc, [](int) {} ST_PASS(l * 5 + 3));
            f32x4 s[2], q[2];
#pragma unroll
            for (int m = 0; m < 2; ++m) {
                const f32x4 r = *(const LAS f32x4*)(R + w * 32 + fq * 8 + m * 4), mr = *(const LAS f32x4*)(M + w * 32 + fq * 8 + m * 4);
                s[m] = (f32x4){0.f, 0.f, 0.f, 0.f}; q[m] = s[m];
#pragma unroll
                for (int n = 7; n < 14; ++n) {
                    const f32x4 z = gelu_tanh(acc[m][n] * r + mr * WS[n * 16 + fr]);
                    acc[m][n] = z; s[m] += z; q[m] += z * z;
                }
            }
            float v8[8], v4[4], v2[2];
#pragma unroll
            for (int i = 0; i < 8; ++i) {
                const float lo = (i & 1) ? q[0][i >> 1] : s[0][i >> 1], hi = (i & 1) ? q[1][i >> 1] : s[1][i >> 1];
                v8[i] = ((fr & 8) ? hi : lo) + dpp_mov<0x128>((fr & 8) ? lo : hi);
            }
#pragma unroll
            for (int i = 0; i < 4; ++i) v4[i] = ((fr & 4) ? v8[i + 4] : v8[i]) + dpp_mov<0x141>(dpp_mov<0x1b>((fr & 4) ? v8[i] : v8[i + 4]));
#pragma unroll
            for (int i = 0; i < 2; ++i) v2[i] = ((fr & 2) ? v4[i + 2] : v4[i]) + dpp_mov<0x4e>((fr & 2) ? v4[i] : v4[i + 2]);
            const float pv = ((fr & 1) ? v2[1] : v2[0]) + dpp_mov<0xb1>((fr & 1) ? v2[0] : v2[1]);
            const __amdgpu_buffer_rsrc_t rX = rsrc(ws + W_XCH + l * 262144 + (size_t)(tm * 4 + w) * 4096, 4096);
            __builtin_amdgcn_raw_buffer_store_b32(xch_word(pv), rX, tn * 256 + lane * 4, 0, 16);
            SB0();
            STAMP(ston, l * 5 + 3, 5);
            {
                const float r0 = R[w * 32 + perm32(fr)], r1 = R[w * 32 + perm32(16 + fr)], m0 = M[w * 32 + perm32(fr)], m1 = M[w * 32 + perm32(16 + fr)];
#pragma unroll
                for (int n = 0; n < 7; ++n) {
                    const f32x4 wv = *(const LAS f32x4*)(WS + n * 16 + fq * 4);
                    zz[n] = pk8(gelu_tanh(acc[0][n] * r0 + wv * m0), gelu_tanh(acc[1][n] * r1 + wv * m1));
                    asm volatile("" : "+v"(zz[n]));
                }
            }
            SB0();
            STAMP(ston, l * 5 + 3, 6);
            f32x4 t;
            for (int spin = 0;; ++spin) {
                u32x4 g[4]; bool ok = true; t = (f32x4){0.f, 0.f, 0.f, 0.f};
#pragma unroll
                for (int i = 0; i < 4; ++i) g[i] = __builtin_amdgcn_raw_buffer_load_b128(rX, lane * 16 + i * 1024, 0, 16);
#pragma unroll
                for (int i = 0; i < 4; ++i) {
                    ok &= (g[i][0] != XCH_EMPTY) & (g[i][1] != XCH_EMPTY) & (g[i][2] != XCH_EMPTY) & (g[i][3] != XCH_EMPTY);
                    t += __builtin_bit_cast(f32x4, g[i]);
                }
                if (__all(ok) || spin > SPIN_MAX) break;
                __builtin_amdgcn_s_sleep(4);
            }
            STAMP(ston, l * 5 + 3, 7);
            const float mean0 = fq_sum(t[0]) * (1.0f / FH), mean1 = fq_sum(t[2]) * (1.0f / FH);
            const float r0 = __builtin_amdgcn_rsqf(fmaxf(fq_sum(t[1]) * (1.0f / FH) - mean0 * mean0, 0.f) + LN_EPS);
            const float r1 = __builtin_amdgcn_rsqf(fmaxf(fq_sum(t[3]) * (1.0f / FH) - mean1 * mean1, 0.f) + LN_EPS);
            LAS float* Z = (LAS float*)(lds + L_Z) + w * 64;
            if (lane < 16) { *(LAS f32x2*)(Z + lane * 2) = (f32x2){r0, r1}; *(LAS f32x2*)(Z + 32 + lane * 2) = (f32x2){-mean0 * r0, -mean1 * r1}; }
            WAITCNT(63, 0);
            const f32x4 ra = *(const LAS f32x4*)(Z + fq * 8), rb = *(const LAS f32x4*)(Z + fq * 8 + 4), ma = *(const LAS f32x4*)(Z + 32 + fq * 8), mb = *(const LAS f32x4*)(Z + 32 + fq * 8 + 4);
            const __amdgpu_buffer_rsrc_t rC = rsrc((u16*)(ws + W_Z2T + l * 8 * MB) + (size_t)tn * 112 * S_ + tm * 128 + w * 32, 112 * S_ * 2 - tm * 256 - w * 64);
#pragma unroll
            for (int n = 0; n < 7; ++n)
                __builtin_amdgcn_raw_buffer_store_b128(pk8(acc[0][7 + n] * ra + ma, acc[1][7 + n] * rb + mb), rC, (n * 16 + fr) * S_ * 2 + fq * 16, 0, STORE_AUX);
            dep_signal(c + 2 * 8192, (lane * 16 + tn) * 32 + tm, 16, lds, lane);
            STAMP(true, l * 5 + 3, 8 + w);
        }
        {
            PHASE_VARS();
            float b[2];
#pragma unroll
            for (int m = 0; m < 2; ++m) b[m] = bf_lo(bsp[tm * 128 + w * 32 + perm32(m * 16 + fr)]);
            f32x4 acc[2][7];
            compute_phase<CG4, 2, 7, 7, false, 4, 5>(lds, w * 32, 0, lane, acc, [](int) {} ST_PASS(l * 5 + 4));
            const __amdgpu_buffer_rsrc_t rC = rsrc((u16*)(ws + W_H + l * 8 * MB) + (size_t)(tm * 128 + w * 32) * FH, 32 * FH * 2);
#pragma unroll
            for (int m = 0; m < 2; ++m) {
                const int ro = perm32(m * 16 + fr) * FH * 2;
#pragma unroll
                for (int q = 0; q < 3; ++q)
                    __builtin_amdgcn_raw_buffer_store_b128(pk8(unpk4(zz[2 * q][m * 2], zz[2 * q][m * 2 + 1]) * (acc[m][2 * q] + b[m]), unpk4(zz[2 * q + 1][m * 2], zz[2 * q + 1][m * 2 + 1]) * (acc[m][2 * q + 1] + b[m])),
                                                           rC, ro + q * 64 + fq * 16, tn * 192, STORE_AUX);
                const f32x4 v = unpk4(zz[6][m * 2], zz[6][m * 2 + 1]) * (acc[m][6] + b[m]);
                __builtin_amdgcn_raw_buffer_store_b64((u32x2){pk_bf16(v[0], v[1]), pk_bf16(v[2], v[3])}, rC, ro + 3072 + fq * 8, tn * 32, STORE_AUX);
            }
            dep_signal(c + 3 * 8192, (tm * 16 + lane) * 32 + tn, 16, lds, lane);
            STAMP(true, l * 5 + 4, 8 + w);
        }
        {
            PHASE_VARS();
            res_phase<CG5>(lds, xa, l == 0 ? (u16*)(ws + W_XB) : out, sp + 131072, tm, tn, w, lane, [](int) {} ST_PASS(l * 5 + 5));
            dep_signal(c + 4 * 8192, (lane * 16 + tm) * 32 + tn, 16, lds, lane);
            STAMP(true, l * 5 + 5, 8 + w);
        }
    }
    asm volatile(".Lk_main_e1:");
}

extern "C" void kernel_launch(void* const* d_in, const int* in_sizes, int n_in, void* d_out, int out_size, void* d_ws, size_t ws_size, hipStream_t stream) {
    const u16* x0 = (const u16*)d_in[0];
    const u16* wsre = (const u16*)d_in[1];
    const u16* wsim = (const u16*)d_in[2];
    const u16* wdre = (const u16*)d_in[3];
    const u16* wdim = (const u16*)d_in[4];
    const u16* win = (const u16*)d_in[6];
    const u16* wsp = (const u16*)d_in[7];
    const u16* bsp = (const u16*)d_in[8];
    const u16* wout = (const u16*)d_in[9];
    char* ws = (char*)d_ws;
    (void)hipFuncSetAttribute((const void*)k_main, hipFuncAttributeMaxDynamicSharedMemorySize, LDS_TOTAL);
    k_main<<<256, 512, LDS_TOTAL, stream>>>(x0, wsre, wsim, wdre, wdim, win, wsp, bsp, wout, ws, (u16*)d_out);
}
```

```cpp
#define TILE_AUX 2
#define PFSTART 2
#define XPH 98
#define XI 0
#include <hip/hip_runtime.h>
#include <hip/hip_bf16.h>
#include <utility>

typedef unsigned short u16;
typedef unsigned long long u64;
typedef short bf16x8 __attribute__((ext_vector_type(8)));
typedef short s16x4 __attribute__((ext_vector_type(4)));
typedef float f32x2 __attribute__((ext_vector_type(2)));
typedef float f32x4 __attribute__((ext_vector_type(4)));
typedef unsigned u32x2 __attribute__((ext_vector_type(2)));
typedef unsigned u32x4 __attribute__((ext_vector_type(4)));
#define LAS __attribute__((address_space(3)))
#define GAS __attribute__((address_space(1)))
#define DEV __device__ __forceinline__

constexpr int waitimm(int vm, int lgkm) { return (vm & 15) | (7 << 4) | ((lgkm & 15) << 8) | ((vm >> 4) << 14); }
#define WAITCNT(vm, lgkm) __builtin_amdgcn_s_waitcnt(waitimm(vm, lgkm))
#define BAR() do { asm volatile("" ::: "memory"); __builtin_amdgcn_s_barrier(); asm volatile("" ::: "memory"); } while (0)
#define SB0() __builtin_amdgcn_sched_barrier(0)
#define STAMP(on, ph, i) do { if ((i) == XI && (on) && (ph) == XPH) { SB0(); const unsigned long long t_ = __builtin_amdgcn_s_memrealtime(); __builtin_amdgcn_s_waitcnt(0xC07F); \
    *stp = t_; SB0(); } } while (0)
#define ST_PARAM , volatile LAS unsigned long long* stp, int ph, bool ston
#define ST_PASS(p) , stp, p, ston

constexpr int S_ = 2048, D_ = 1024, F_ = 3584, FH = 1792;
#define T_IM 12
#define T_IN 0
#define T_STEP 2
#define T_OUT 20
#define T_SIG 40
#define XM2 ~0u
#define XM16 ~0u
#define POLL_SLEEP 2
constexpr float LN_EPS = 1e-5f;
constexpr int SPIN_MAX = 1 << 17;
constexpr int STORE_AUX = 16;

DEV unsigned pk_bf16(float lo, float hi) { unsigned r; asm("v_cvt_pk_bf16_f32 %0, %1, %2" : "=v"(r) : "v"(lo), "v"(hi)); return r; }
DEV float bf_lo(unsigned v) { return __uint_as_float(v << 16); }
DEV float bf_hi(unsigned v) { return __uint_as_float(v & 0xffff0000u); }
DEV __amdgpu_buffer_rsrc_t rsrc(const void* p, unsigned bytes) { return __builtin_amdgcn_make_buffer_rsrc((void*)p, 0, bytes, 0x00020000); }
DEV f32x4 gelu_tanh(f32x4 x) {
    const float c0 = -2.3022081986f, c1 = c0 * 0.044715f;
    const f32x4 a = x * (x * x * c1 + c0);
    f32x4 d = (f32x4){__builtin_amdgcn_exp2f(a[0]), __builtin_amdgcn_exp2f(a[1]), __builtin_amdgcn_exp2f(a[2]), __builtin_amdgcn_exp2f(a[3])} + 1.0f;
    return x * (f32x4){__builtin_amdgcn_rcpf(d[0]), __builtin_amdgcn_rcpf(d[1]), __builtin_amdgcn_rcpf(d[2]), __builtin_amdgcn_rcpf(d[3])};
}
DEV u32x4 pk8(f32x4 a, f32x4 b) { return (u32x4){pk_bf16(a[0], a[1]), pk_bf16(a[2], a[3]), pk_bf16(b[0], b[1]), pk_bf16(b[2], b[3])}; }
DEV f32x4 unpk4(unsigned a, unsigned b) { return (f32x4){bf_lo(a), bf_hi(a), bf_lo(b), bf_hi(b)}; }
__host__ __device__ constexpr int perm32(int slot) { return 8 * ((slot & 15) >> 2) + 4 * ((slot >> 4) & 1) + (slot & 3); }
__host__ __device__ constexpr int inv32(int row) { return 16 * ((row >> 2) & 1) + 4 * ((row >> 3) & 3) + (row & 3); }
DEV float fq_sum(float a) {
    const auto p = __builtin_amdgcn_permlane16_swap(__float_as_uint(a), __float_as_uint(a), false, false);
    a = __uint_as_float(p[0]) + __uint_as_float(p[1]);
    const auto q = __builtin_amdgcn_permlane32_swap(__float_as_uint(a), __float_as_uint(a), false, false);
    return __uint_as_float(q[0]) + __uint_as_float(q[1]);
}
template <int CTRL> DEV float dpp_mov(float a) { return __builtin_bit_cast(float, __builtin_amdgcn_update_dpp(0, __builtin_bit_cast(int, a), CTRL, 0xf, 0xf, true)); }
template <int CTRL> DEV float dpp_add(float a) { return a + __builtin_bit_cast(float, __builtin_amdgcn_update_dpp(0, __builtin_bit_cast(int, a), CTRL, 0xf, 0xf, true)); }
DEV float row16_sum(float a) { a = dpp_add<0x128>(a); a = dpp_add<0x124>(a); a = dpp_add<0x122>(a); return dpp_add<0x121>(a); }
DEV float oct_sum(float a) { a = dpp_add<0xb1>(a); a = dpp_add<0x4e>(a); return dpp_add<0x141>(a); }
DEV float quad_sum(float a) { a = dpp_add<0xb1>(a); return dpp_add<0x4e>(a); }
constexpr unsigned XCH_EMPTY = 0xffffffffu;
DEV unsigned xch_word(float v) { const unsigned u = __float_as_uint(v); return u == XCH_EMPTY ? 0x7fc00000u : u; }

DEV int opaque_v(int v) { asm volatile("" : "+v"(v)); return v; }
DEV int opaque_s(int v) { asm volatile("" : "+s"(v)); return v; }
template <class F, int... I> DEV void static_for_impl(F&& f, std::integer_sequence<int, I...>) { (f(std::integral_constant<int, I>{}), ...); }
template <int N, class F> DEV void static_for(F&& f) { static_for_impl(f, std::make_integer_sequence<int, N>{}); }

template <int BM_, int BN_, int NS_, int BASE_, int LDP_, int LDQ_, int NT_, int KSW_, bool PERMP_, bool PERMQ_, bool WISP_> struct Cfg {
    static constexpr int BM = BM_, BN = BN_, NS = NS_, LDP = LDP_, LDQ = LDQ_, NT = NT_, KSW = KSW_;
    static constexpr bool PERMP = PERMP_, PERMQ = PERMQ_, WISP = WISP_;
    static constexpr int PB = BM * 128, QB = BN * 128, PBASE = BASE_, QBASE = PBASE + NS * PB, RING = QBASE + NS * QB;
    static constexpr int LP = BM / 32, LQ = BN / 32;
    static constexpr int LW = WISP ? LP : LQ, LA = WISP ? LQ : LP;
};
constexpr int RING_MAX = 147456;
constexpr int L_R = RING_MAX, L_M = L_R + 512, L_WD = L_M + 512, L_WS = L_WD + 512, L_Z = L_WS + 1024, L_JUNK = L_Z + 1024, L_SYNC = L_JUNK + 256, L_TAG = L_SYNC + 48, LDS_TOTAL = L_SYNC + 64;
constexpr int L_TIN0 = 0, L_TIN1 = 131072, L_TOUT = 114688;

template <class C, int N0, int N1, class Dep, class Mid, class Tail>
DEV void loader_phase(LAS unsigned char* lds, const u16* P0, const u16* P1, const u16* Q, int lw, int lane, unsigned drained, Dep&& dep, Mid&& mid, Tail&& tail ST_PARAM) {
    constexpr int NS = C::NS, NT = C::NT, LA = C::LA, LW = C::LW;
    static_assert(C::RING <= RING_MAX && NS >= 3 && NT >= 2 * NS && C::BM % 32 == 0 && !(C::PERMQ && C::BN % 32), "");
    lane = opaque_v(lane); lw = opaque_s(lw);
    const __amdgpu_buffer_rsrc_t rP0 = rsrc(P0, C::BM * C::LDP * 2), rP1 = rsrc(P1, C::BM * C::LDP * 2), rQ = rsrc(Q, C::BN * C::LDQ * 2);
    const int ch = ((lane & 7) ^ (((lw & 1) * 4 + (lane >> 4)) & 7)) * 16, row = lw * 8 + (lane >> 3);
    const int vP = (C::PERMP ? perm32(row) : row) * C::LDP * 2 + ch, vQ = (C::PERMQ ? perm32(row) : row) * C::LDQ * 2 + ch;
    auto issueP = [&](int slot, int kt) {
        LAS unsigned char* dst = lds + C::PBASE + slot * C::PB + lw * 1024;
        const bool second = C::KSW && kt >= C::KSW;
        const __amdgpu_buffer_rsrc_t rP = second ? rP1 : rP0;
        const int kp = (second ? kt - C::KSW : kt) * 128;
#pragma unroll
        for (int i = 0; i < C::LP; ++i) __builtin_amdgcn_raw_ptr_buffer_load_lds(rP, (LAS void*)(dst + i * 4096), 16, vP, i * 32 * C::LDP * 2 + kp, 0, C::WISP ? 0 : 16);
    };
    auto issueQ = [&](int slot, int kt) {
        LAS unsigned char* dst = lds + C::QBASE + slot * C::QB + lw * 1024;
#pragma unroll
        for (int j = 0; j < (C::BN + 31) / 32; ++j)
            if (j < C::LQ || j * 4 + lw < C::BN / 8) __builtin_amdgcn_raw_ptr_buffer_load_lds(rQ, (LAS void*)(dst + j * 4096), 16, vQ, j * 32 * C::LDQ * 2 + kt * 128, 0, 16);
    };
    auto issueW = [&](int slot, int kt) { if (C::WISP) issueP(slot, kt); else issueQ(slot, kt); };
    auto issueA = [&](int slot, int kt) { if (C::WISP) issueQ(slot, kt); else issueP(slot, kt); };
    if (drained) {
        while ((unsigned)__builtin_amdgcn_readfirstlane(*(volatile LAS unsigned*)(lds + L_SYNC + 16)) < drained) __builtin_amdgcn_s_sleep(2);
#pragma unroll 1
        for (int s = 0; s < NS; ++s) issueW(s, s);
        dep();
        STAMP(ston, ph, 4);
        issueA(0, 0); issueA(1, 1);
        WAITCNT(LA, 15);
        BAR();
#pragma unroll
        for (int s = 2; s < NS; ++s) issueA(s, s);
    } else {
        issueW(0, 0); issueA(0, 0); issueW(1, 1); issueA(1, 1);
        STAMP(ston, ph, 4);
        WAITCNT(LA + LW, 15);
        BAR();
#pragma unroll 1
        for (int s = 2; s < NS; ++s) { issueW(s, s); issueA(s, s); }
    }
    static_for<NS - 2>([&](auto ic) {
        constexpr int t = decltype(ic)::value;
        WAITCNT((NS - 2 - t) * LA + t * (LA + LW), 15);
        BAR();
        issueW(t, t + NS); issueA(t, t + NS);
    });
    int sl = NS - 2;
#pragma unroll 1
    for (int t = NS - 2; t < NT - NS; ++t) {
        WAITCNT((NS - 2) * (LA + LW), 15);
        BAR();
        issueW(sl, t + NS); issueA(sl, t + NS);
        sl = (sl + 1 == NS) ? 0 : sl + 1;
        mid(t);
    }
    tail(std::integral_constant<int, 0>{});
    static_for<NS - 1>([&](auto ic) {
        constexpr int i = decltype(ic)::value;
        WAITCNT((NS - 2 - i) * (LA + LW) + N0 + (i ? N1 : 0), 15);
        BAR();
        if constexpr (i == 0) tail(std::integral_constant<int, 1>{});
    });
    tail(std::integral_constant<int, 2>{});
    WAITCNT(63, 0);
    BAR();
}

template <class C, int MT, int NTL, int NSWAP, bool ROLL, int RNUM, int RDEN, class Hook>
DEV void compute_phase(LAS unsigned char* lds, int prow0, int qrow0, int lane, f32x4 (&acc)[MT][NTL], Hook&& hook ST_PARAM) {
    constexpr int NR = MT + NTL, NM = MT * NTL, NT = C::NT;
    const int fr = lane & 15, fq = lane >> 4;
    int aoff[2], boff[2];
#pragma unroll
    for (int k = 0; k < 2; ++k) {
        const int ch = ((k * 4 + fq) ^ ((fr >> 1) & 7)) * 16;
        aoff[k] = C::PBASE + (prow0 + fr) * 128 + ch;
        boff[k] = C::QBASE + (qrow0 + fr) * 128 + ch;
    }
#pragma unroll
    for (int m = 0; m < MT; ++m)
#pragma unroll
        for (int n = 0; n < NTL; ++n) { acc[m][n] = (f32x4){0.f, 0.f, 0.f, 0.f}; asm volatile("" : "+v"(acc[m][n])); }
    auto mfma = [&](int m, int n, const bf16x8& a, const bf16x8& b) {
        if (n >= NSWAP) asm volatile("v_mfma_f32_16x16x32_bf16 %0, %1, %2, %0" : "+v"(acc[m][n]) : "v"(a), "v"(b));
        else asm volatile("v_mfma_f32_16x16x32_bf16 %0, %1, %2, %0" : "+v"(acc[m][n]) : "v"(b), "v"(a));
    };
    int sp = 0, sq = 0;
    auto next = [&]() { sp = (sp + C::PB == C::NS * C::PB) ? 0 : sp + C::PB; sq = (sq + C::QB == C::NS * C::QB) ? 0 : sq + C::QB; };
    BAR();
    STAMP(ston, ph, 1);
    if constexpr (!ROLL) {
        bf16x8 fa[4][MT], fb[4][NTL];
        auto rd1 = [&](int set, int k, int i) {
            if (i == 0) fa[set][0] = *(const LAS bf16x8*)(lds + sp + aoff[k]);
            else if (i <= NTL) fb[set][i - 1] = *(const LAS bf16x8*)(lds + sq + boff[k] + (i - 1) * 2048);
            else fa[set][i - NTL] = *(const LAS bf16x8*)(lds + sp + aoff[k] + (i - NTL) * 2048);
        };
        auto stg = [&](int set, bool rd) {
#pragma unroll
            for (int i = 0; i < 2 * NM; ++i) {
                const int st = set + i / NM, m = (i % NM) / NTL, n = i % NTL;
                mfma(m, n, fa[st][m], fb[st][n]); SB0();
                if (rd) {
#pragma unroll
                    for (int r = (i * RNUM + RDEN - 1) / RDEN; r < ((i + 1) * RNUM + RDEN - 1) / RDEN; ++r) if (r < 2 * NR) rd1((set ^ 2) + r / NR, r / NR, r % NR);
                    SB0();
                }
            }
        };
        static_assert((2 * NM * RNUM + RDEN - 1) / RDEN >= 2 * NR && NT % 2 == 0, "");
#pragma unroll
        for (int r = 0; r < 2 * NR; ++r) rd1(r / NR, r / NR, r % NR);
#pragma unroll 1
        for (int t = 0; t < NT - 2; t += 2) {
            next(); WAITCNT(63, 0); BAR(); SB0();
            stg(0, true);
            next(); WAITCNT(63, 0); BAR(); SB0();
            stg(2, true);
            hook(t); SB0();
        }
        next();
        stg(0, false);
        BAR(); SB0();
#pragma unroll
        for (int r = 0; r < 2 * NR; ++r) rd1(2 + r / NR, r / NR, r % NR);
        WAITCNT(63, 0); BAR(); SB0();
        stg(2, false);
    } else {
        static_assert(MT == 2, "");
        constexpr int G = 8;
        bf16x8 fa[2][2], fb[NTL];
        auto rda = [&](int set, int k, int m) { fa[set][m] = *(const LAS bf16x8*)(lds + sp + aoff[k] + m * 2048); };
        auto rdb = [&](int k, int n) { fb[n] = *(const LAS bf16x8*)(lds + sq + boff[k] + n * 2048); };
        auto half0 = [&]() {
#pragma unroll
            for (int j = 0; j < NM; ++j) {
                mfma(j & 1, j >> 1, fa[0][j & 1], fb[j >> 1]); SB0();
                if (j & 1) rdb(1, j >> 1); else if (j < 4) rda(1, 1, j >> 1);
                SB0();
            }
        };
        auto half1 = [&](bool rd) {
#pragma unroll
            for (int j = 0; j < NM; ++j) {
                if (j == G) { next(); WAITCNT(63, 0); BAR(); SB0(); }
                mfma(j & 1, j >> 1, fa[1][j & 1], fb[j >> 1]); SB0();
                if (rd && j >= G) {
                    if (j < G + 2) rda(0, 0, j - G);
#pragma unroll
                    for (int n = 0; n < NTL; ++n) if ((G + 2 + n > 2 * n + 1 ? G + 2 + n : 2 * n + 1) == j) rdb(0, n);
                    SB0();
                }
            }
        };
        rda(0, 0, 0); rda(0, 0, 1);
#pragma unroll
        for (int n = 0; n < NTL; ++n) rdb(0, n);
        SB0();
#pragma unroll 1
        for (int t = 0; t < NT - 1; ++t) { half0(); half1(true); }
        half0(); half1(false);
    }
    asm volatile("s_nop 15" ::: "memory");
#pragma unroll
    for (int m = 0; m < MT; ++m)
#pragma unroll
        for (int n = 0; n < NTL; ++n) asm volatile("" : "+v"(acc[m][n]));
    STAMP(ston, ph, 2);
}

DEV u64 launch_tag(LAS unsigned char* lds) { return *(volatile LAS u64*)(lds + L_TAG); }
DEV void box_poll(const u64* box, int nprod, u64 tag, int lane) {
#pragma unroll 1
    for (int spin = 0; spin < SPIN_MAX; ++spin) {
        const u64 v = __hip_atomic_load((const GAS u64*)box + (lane & (nprod - 1)), __ATOMIC_RELAXED, __HIP_MEMORY_SCOPE_AGENT);
        if (__all(v == tag)) break;
        __builtin_amdgcn_s_sleep(POLL_SLEEP);
    }
}
DEV void dep_wait(const u64* box, int nprod, LAS unsigned char* lds, unsigned epoch, int lw, int lane) {
    volatile LAS unsigned* flag = (volatile LAS unsigned*)(lds + L_SYNC);
    if (lw == 0) {
        box_poll(box, nprod, launch_tag(lds), lane);
        *flag = epoch;
    } else {
        while ((unsigned)__builtin_amdgcn_readfirstlane(*flag) < epoch) __builtin_amdgcn_s_sleep(1);
    }
    asm volatile("" ::: "memory");
}
template <int CNT = 16> DEV void dep_signal(u64* boxes, int word, int ncons, LAS unsigned char* lds, int lane) {
    const u64 tag = launch_tag(lds);
    asm volatile("s_waitcnt vmcnt(0)" ::: "memory");
    unsigned old = 0;
    if (lane == 0) old = __hip_atomic_fetch_add((LAS unsigned*)(lds + L_SYNC + CNT), 1u, __ATOMIC_RELAXED, __HIP_MEMORY_SCOPE_WORKGROUP);
    if ((__builtin_amdgcn_readfirstlane(old) & 3) == 3 && lane < ncons) __hip_atomic_store((GAS u64*)boxes + word, tag, __ATOMIC_RELAXED, __HIP_MEMORY_SCOPE_AGENT);
}
DEV void touch(LAS unsigned char* lds, const u16* p, unsigned bytes, int off) {
    __builtin_amdgcn_raw_ptr_buffer_load_lds(rsrc(p, bytes), (LAS void*)(lds + L_JUNK), 4, off, 0, 0, 0);
}

DEV void row_tables_load(const float* sp, unsigned bytes, int tid, f32x4 (&v)[4]) {
    const __amdgpu_buffer_rsrc_t r = rsrc(sp, bytes);
#pragma unroll
    for (int i = 0; i < 4; ++i) v[i] = __builtin_bit_cast(f32x4, __builtin_amdgcn_raw_buffer_load_b128(r, i * 4096 + tid * 16, 0, 16));
}
DEV void row_tables_finish(LAS unsigned char* lds, int tid, const f32x4 (&v)[4], float sign) {
#pragma unroll
    for (int i = 0; i < 4; ++i) {
        const float s = oct_sum(v[i][0] + v[i][2]), q = oct_sum(v[i][1] + v[i][3]);
        const float mean = s * (1.0f / D_), var = fmaxf(q * (1.0f / D_) - mean * mean, 0.f), r = sign * __builtin_amdgcn_rsqf(var + LN_EPS);
        if ((tid & 7) == 0) { ((LAS float*)(lds + L_R))[i * 32 + (tid >> 3)] = r; ((LAS float*)(lds + L_M))[i * 32 + (tid >> 3)] = -mean * r; }
    }
}

constexpr size_t MB = 1 << 20;
constexpr size_t W_T = 0, W_XA = 16 * MB, W_XB = 24 * MB, W_Z2T = 28 * MB, W_H = 44 * MB, W_WINT = 60 * MB, W_WOUTT = 68 * MB;
constexpr size_t W_SP = 72 * MB, W_GRAN = 74 * MB, W_WSPIN = 74 * MB + 65536, W_XCH = 74 * MB + 512 * 1024, W_CTR = 75 * MB, W_NONCE = 75 * MB + 768 * 1024;

typedef Cfg<128, 128, 4, 0, D_, D_, D_ / 64, 0, false, true, true> CG1;
typedef Cfg<128, 64, 4, 16384, S_, 4096, 64, 32, false, false, true> CG2;
typedef Cfg<128, 224, 3, 0, D_, D_, D_ / 64, 0, true, false, false> CG3;
typedef Cfg<128, 112, 4, 0, S_, S_, S_ / 64, 0, true, false, true> CG4;
typedef Cfg<128, 64, 4, 0, FH, FH, FH / 64, 0, false, false, false> CG5;

template <class C, class Hook>
DEV void res_phase(LAS unsigned char* lds, const u16* res, u16* out, float* sp, int tm, int tn, int w, int lane, Hook&& hook ST_PARAM) {
    const int fr = lane & 15, fq = lane >> 4;
    const __amdgpu_buffer_rsrc_t rX = rsrc(res + (size_t)(tm * 128 + w * 32) * D_ + tn * 64, 32 * D_ * 2 - tn * 128);
    u32x4 xr[2][2];
#pragma unroll
    for (int m = 0; m < 2; ++m)
#pragma unroll
        for (int h = 0; h < 2; ++h) xr[m][h] = __builtin_amdgcn_raw_buffer_load_b128(rX, (m * 16 + fr) * D_ * 2 + h * 64 + fq * 16, 0, 16);
    f32x4 acc[2][4];
    compute_phase<C, 2, 4, 4, false, 1, 1>(lds, w * 32, 0, lane, acc, hook ST_PASS(ph));
    const __amdgpu_buffer_rsrc_t rC = rsrc(out + (size_t)(tm * 128 + w * 32) * D_ + tn * 64, 32 * D_ * 2 - tn * 128);
    const __amdgpu_buffer_rsrc_t rS = rsrc(sp + (size_t)(tm * 128 + w * 32) * 32, 32 * 128);
#pragma unroll
    for (int m = 0; m < 2; ++m) {
        f32x4 s = (f32x4){0.f, 0.f, 0.f, 0.f}, qq = s;
#pragma unroll
        for (int h = 0; h < 2; ++h) {
            const u32x4 o = pk8(acc[m][2 * h] + unpk4(xr[m][h][0], xr[m][h][1]), acc[m][2 * h + 1] + unpk4(xr[m][h][2], xr[m][h][3]));
            __builtin_amdgcn_raw_buffer_store_b128(o, rC, (m * 16 + fr) * D_ * 2 + h * 64 + fq * 16, 0, STORE_AUX);
            const f32x4 a = unpk4(o[0], o[1]), b = unpk4(o[2], o[3]);
            s += a + b; qq += a * a + b * b;
        }
        const float st = fq_sum((s[0] + s[1]) + (s[2] + s[3])), qt = fq_sum((qq[0] + qq[1]) + (qq[2] + qq[3]));
        if (fq == 0) __builtin_amdgcn_raw_buffer_store_b64((u32x2){__float_as_uint(st), __float_as_uint(qt)}, rS, (m * 16 + fr) * 128 + tn * 8, 0, 16);
    }
}

__global__ __launch_bounds__(512, 2) void k_main(const u16* __restrict__ x0, const u16* __restrict__ wsre, const u16* __restrict__ wsim, const u16* __restrict__ wdre,
                                                 const u16* __restrict__ wdim, const u16* __restrict__ win, const u16* __restrict__ wsp, const u16* __restrict__ bsp,
                                                 const u16* __restrict__ wout, char* ws, u16* out) {
    extern __shared__ __attribute__((aligned(1024))) unsigned char shm[];
    LAS unsigned char* lds = (LAS unsigned char*)shm;
    const int tid = threadIdx.x, lane0 = tid & 63, w0 = __builtin_amdgcn_readfirstlane(tid >> 6);
    const int tm0 = ((blockIdx.x & 7) >> 1) * 4 + (blockIdx.x >> 6), tn0 = (blockIdx.x & 1) * 8 + ((blockIdx.x >> 3) & 7);
#define PHASE_VARS() const int lane = opaque_v(lane0), w = opaque_s(w0), tm = opaque_s(tm0), tn = opaque_s(tn0), fr = lane & 15, fq = lane >> 4; (void)fr; (void)fq; (void)w
    u64* ctr = (u64*)(ws + W_CTR);
    volatile LAS unsigned long long* stp = (volatile LAS unsigned long long*)(lds + L_SYNC + 56); const bool ston = (w0 & 3) == 0;
    const unsigned long long xt0 = __builtin_amdgcn_s_memrealtime();
    if (w0 >= 4) {
        const int lw = w0 - 4;
        auto none = [&](auto) {};
#pragma unroll 1
        for (int l = 0; l < 2; ++l) {
            const u16* x = l == 0 ? x0 : (const u16*)(ws + W_XB);
            const u16* T = (const u16*)(ws + W_T + l * 8 * MB); const u16* xa = (const u16*)(ws + W_XA + l * 4 * MB);
            const u16* z2t = (const u16*)(ws + W_Z2T + l * 8 * MB); const u16* h = (const u16*)(ws + W_H + l * 8 * MB);
            const float* sp = (const float*)(ws + W_SP + l * 512 * 1024);
            const u64* c = ctr + l * 5 * 8192;
            const unsigned first = l == 0 ? ~0u : 0u;
            f32x4 v[4], u[4];
            { PHASE_VARS(); const int lt = (w - 4) * 64 + lane;
            const u16* pw = (tm < 8 ? wdre : wdim) + (size_t)(tm & 7) * 128 * D_;
            const __amdgpu_buffer_rsrc_t rg = rsrc(ws + W_GRAN, 65536 & first);
            const int goff = (lt < 128 ? tn * 128 + lt : 1920 + tm * 128 + lt) * 16;
            u32x4 g;
            loader_phase<CG1, 6, 7>(lds, pw, pw, x + (size_t)tn * 128 * D_, lw, lane, l * 20, [&]() { dep_wait(ctr + 4 * 8192 + (tm * 16 + tn) * 32, 16, lds, 5, lw, lane); }, none, [&](auto ic) {
                constexpr int i = decltype(ic)::value;
                if constexpr (i == 0) {
                    row_tables_load(sp + tn * 128 * 32, 16384 & ~first, lt, v);
                    g = __builtin_amdgcn_raw_buffer_load_b128(rg, goff, 0, 16);
                    touch(lds, wsre + (size_t)(tm * 128 + tn * 8 + lw * 2) * S_, (PFSTART & 1) ? 0 : 8192 & first, lane * 128);
                } else if constexpr (i == 1) {
                    const __amdgpu_buffer_rsrc_t r = rsrc(win + (size_t)(tm * 64 + (lw >> 1) * 32) * F_, (32 * F_ * 2) & first & XM2);
#pragma unroll
                    for (int j = 0; j < 7; ++j) {
                        const int q = (lw & 1) * 7 + j, cb = q * 64 + lane, row = cb / 28, ch = cb - row * 28;
                        __builtin_amdgcn_raw_ptr_buffer_load_lds(r, (LAS void*)(lds + (lw < 2 ? L_TIN0 : L_TIN1) + q * 1024), 16, row * F_ * 2 + (ch < 14 ? ch * 16 : FH * 2 - 224 + ch * 16), tn * 224, 0, TILE_AUX);
                    }
                } else {
                    const float sign = tm < 8 ? 1.0f : -1.0f;
                    if (l) { row_tables_finish(lds, lt, v, sign); return; }
                    const u64 tag = launch_tag(lds);
                    if (!__all(g[1] == (unsigned)tag && g[3] == (unsigned)(tag >> 32))) {
                        asm volatile("" ::: "memory");
#pragma unroll 1
                        for (int spin = 0; spin < SPIN_MAX; ++spin) {
                            g = __builtin_amdgcn_raw_buffer_load_b128(rg, goff, 0, 16);
                            if (__all(g[1] == (unsigned)tag && g[3] == (unsigned)(tag >> 32))) break;
                            __builtin_amdgcn_s_sleep(POLL_SLEEP);
                        }
                    }
                    const float mean = __uint_as_float(g[0]) * (1.0f / D_), var = fmaxf(__uint_as_float(g[2]) * (1.0f / D_) - mean * mean, 0.f), rs = sign * __builtin_amdgcn_rsqf(var + LN_EPS);
                    if (lt < 128) { ((LAS float*)(lds + L_R))[lt] = rs; ((LAS float*)(lds + L_M))[lt] = -mean * rs; }
                    else ((LAS float*)(lds + L_WD))[lt - 128] = __uint_as_float(g[0]);
                }
            } ST_PASS(l * 5 + 1)); }
            { PHASE_VARS();
            u64 tw;
            const u64* tbox = ctr + 8192 + (tm * 16 + tn) * 32 + 16;
            loader_phase<CG2, 1, 0>(lds, wsre + (size_t)tm * 128 * S_, wsim + (size_t)tm * 128 * S_, T + (size_t)tn * 64 * 4096, lw, lane, l * 20 + 4,
                              [&]() { dep_wait(c + (tm * 16 + tn) * 32, 32, lds, l * 5 + 1, lw, lane); },
                              [&](int t) {
                                  if (t != T_IM || l) return;
                                  if (!(PFSTART & 2)) touch(lds, wsim + (size_t)(tm * 128 + tn * 8 + lw * 2) * S_, 8192, lane * 128);
                                  const __amdgpu_buffer_rsrc_t r = rsrc(wout + (size_t)tm * 112 * D_, (112 * D_ * 2) & XM2);
#pragma unroll
                                  for (int j = 0; j < 4; ++j)
                                      if (j < 3 || lw < 2) __builtin_amdgcn_raw_ptr_buffer_load_lds(r, (LAS void*)(lds + L_TOUT + (j * 4 + lw) * 1024), 16, ((j * 4 + lw) * 8 + (lane >> 3)) * D_ * 2 + (lane & 7) * 16, tn * 128, 0, TILE_AUX);
                              },
                              [&](auto ic) {
                                  constexpr int i = decltype(ic)::value;
                                  if constexpr (i == 0) tw = __hip_atomic_load((const GAS u64*)tbox + (lane & 15), __ATOMIC_RELAXED, __HIP_MEMORY_SCOPE_AGENT);
                                  if constexpr (i == 2) { const u64 tag = launch_tag(lds); if (!__all(tw == tag)) box_poll(tbox, 16, tag, lane); }
                              } ST_PASS(l * 5 + 2)); }
            { PHASE_VARS(); const int lt = (w - 4) * 64 + lane;
            loader_phase<CG3, 9, 0>(lds, xa + (size_t)tm * 128 * D_, xa, (const u16*)(ws + W_WINT) + (size_t)tn * 224 * D_, lw, lane, l * 20 + 8,
                              [&]() { dep_wait(c + 8192 + (tm * 16 + tn) * 32, 16, lds, l * 5 + 2, lw, lane);
                                      if (blockIdx.x == 0 && l == 0 && lt == 0) __hip_atomic_store((GAS u64*)(ws + W_NONCE), launch_tag(lds), __ATOMIC_RELAXED, __HIP_MEMORY_SCOPE_AGENT); },
                              none, [&](auto ic) {
                                  constexpr int i = decltype(ic)::value;
                                  if constexpr (i == 0) {
                                      row_tables_load(sp + 65536 + tm * 128 * 32, 16384, lt, v);
                                      const __amdgpu_buffer_rsrc_t r = rsrc(ws + W_WSPIN + (size_t)tn * 224 * 64, 224 * 64);
#pragma unroll
                                      for (int j = 0; j < 4; ++j) u[j] = __builtin_bit_cast(f32x4, __builtin_amdgcn_raw_buffer_load_b128(r, j * 4096 + lt * 16, 0, 16));
                                      touch(lds, wsp + (size_t)(tm * 128 + tn * 8 + lw * 2) * S_, 8192 & first, lane * 128);
                                  } else if constexpr (i == 2) {
                                      row_tables_finish(lds, lt, v, 1.0f);
#pragma unroll
                                      for (int j = 0; j < 4; ++j) { const float s = quad_sum((u[j][0] + u[j][1]) + (u[j][2] + u[j][3])); if ((lt & 3) == 0) ((LAS float*)(lds + L_WS))[j * 64 + (lt >> 2)] = s; }
                                  }
                              } ST_PASS(l * 5 + 3)); }
            { PHASE_VARS();
            loader_phase<CG4, 0, 0>(lds, wsp + (size_t)tm * 128 * S_, wsp, z2t + (size_t)tn * 112 * S_, lw, lane, l * 20 + 12, [&]() { dep_wait(c + 2 * 8192 + (tm * 16 + tn) * 32, 16, lds, l * 5 + 3, lw, lane); }, none, none ST_PASS(l * 5 + 4)); }
            { PHASE_VARS();
            loader_phase<CG5, 0, 0>(lds, h + (size_t)tm * 128 * FH, h, (const u16*)(ws + W_WOUTT) + (size_t)tn * 64 * FH, lw, lane, l * 20 + 16,
                              [&]() { dep_wait(c + 3 * 8192 + (tm * 16 + tn) * 32, 16, lds, l * 5 + 4, lw, lane); }, none, none ST_PASS(l * 5 + 5)); }
        }
        asm volatile(".Lk_main_e0:");
        return;
    }
    {
        PHASE_VARS();
        u64 z = __hip_atomic_load((const GAS u64*)(ws + W_NONCE), __ATOMIC_RELAXED, __HIP_MEMORY_SCOPE_AGENT);
        const int xr = tn * 128 + tm * 8 + w * 2, wr = (tm & 7) * 128 + tn * 8 + w * 2;
        const __amdgpu_buffer_rsrc_t rx = rsrc(x0 + (size_t)xr * D_, 4096), rw = rsrc((tm < 8 ? wdre : wdim) + (size_t)wr * D_, 4096);
        u32x4 a[4], b[4];
#pragma unroll
        for (int i = 0; i < 4; ++i) { a[i] = __builtin_amdgcn_raw_buffer_load_b128(rx, i * 1024 + lane * 16, 0, 0); b[i] = __builtin_amdgcn_raw_buffer_load_b128(rw, i * 1024 + lane * 16, 0, 0); }
        u64 pc; unsigned len;
        asm volatile("s_getpc_b64 %0\n.Lk_main_pc:\n\ts_max_i32 %1, .Lk_main_e0-.Lk_main_pc, 0\n\ts_max_i32 %1, .Lk_main_e1-.Lk_main_pc, %1" : "=s"(pc), "=s"(len) :: "scc");
        touch(lds, (const u16*)pc, len, (w * 64 + lane) * 128); touch(lds, (const u16*)pc, len, (256 + w * 64 + lane) * 128);
        if (PFSTART & 1) touch(lds, wsre + (size_t)(tm * 128 + tn * 8 + w * 2) * S_, 8192, lane * 128);
        if (PFSTART & 2) touch(lds, wsim + (size_t)(tm * 128 + tn * 8 + w * 2) * S_, 8192, lane * 128);
        STAMP(ston, 1, 5);
        if (tid < 128) __builtin_amdgcn_raw_buffer_store_b128((u32x4){XCH_EMPTY, XCH_EMPTY, XCH_EMPTY, XCH_EMPTY}, rsrc(ws + W_XCH + blockIdx.x * 2048, 2048), tid * 16, 0, 16);
        if (tid < 12) *(LAS unsigned*)(lds + L_SYNC + tid * 4) = 0u;
        z += 0x9E3779B97F4A7C15ull; z = (z ^ (z >> 30)) * 0xBF58476D1CE4E5B9ull; z = (z ^ (z >> 27)) * 0x94D049BB133111EBull; z ^= z >> 31;
        if (tid == 0) *(LAS u64*)(lds + L_TAG) = z;
        STAMP(ston, 1, 6);
        float g0 = 0.f, g1 = 0.f;
#pragma unroll
        for (int r = 0; r < 2; ++r) {
            f32x4 sx = (f32x4){0.f, 0.f, 0.f, 0.f}, qx = sx, sw = sx;
#pragma unroll
            for (int i = 0; i < 2; ++i) {
                const f32x4 a0 = unpk4(a[2 * r + i][0], a[2 * r + i][1]), a1 = unpk4(a[2 * r + i][2], a[2 * r + i][3]);
                sx += a0 + a1; qx += a0 * a0 + a1 * a1;
                sw += unpk4(b[2 * r + i][0], b[2 * r + i][1]) + unpk4(b[2 * r + i][2], b[2 * r + i][3]);
            }
            const float s1 = fq_sum(row16_sum((sx[0] + sx[1]) + (sx[2] + sx[3]))), q1 = fq_sum(row16_sum((qx[0] + qx[1]) + (qx[2] + qx[3]))), s2 = fq_sum(row16_sum((sw[0] + sw[1]) + (sw[2] + sw[3])));
            if (lane == r) { g0 = s1; g1 = q1; }
            if (lane == 2 + r) g0 = s2;
        }
        if (lane < 4) __builtin_amdgcn_raw_buffer_store_b128((u32x4){__float_as_uint(g0), (unsigned)z, __float_as_uint(g1), (unsigned)(z >> 32)}, rsrc(ws + W_GRAN, 65536),
                                                             (lane < 2 ? xr + lane : 2046 + (tm >> 3) * 1024 + wr + lane) * 16, 0, 16);
        STAMP(ston, 1, 7);
    }
    WAITCNT(63, 0);
    const LAS float* R = (const LAS float*)(lds + L_R); const LAS float* M = (const LAS float*)(lds + L_M); const LAS float* WS = (const LAS float*)(lds + L_WS);
#pragma unroll 1
    for (int l = 0; l < 2; ++l) {
        const u16* x = l == 0 ? x0 : (const u16*)(ws + W_XB);
        u16* xa = (u16*)(ws + W_XA + l * 4 * MB);
        float* sp = (float*)(ws + W_SP + l * 512 * 1024);
        u64* c = ctr + l * 5 * 8192;
        {
            PHASE_VARS();
            const int wm = w >> 1, wn = w & 1;
            f32x4 acc[4][4];
            compute_phase<CG1, 4, 4, 4, false, 2, 3>(lds, wm * 64, wn * 64, lane, acc, [](int) {} ST_PASS(l * 5 + 1));
            float wsum[4];
#pragma unroll
            for (int m = 0; m < 4; ++m) wsum[m] = ((const LAS float*)(lds + L_WD))[wm * 64 + m * 16 + fr];
            const __amdgpu_buffer_rsrc_t rC = rsrc((u16*)(ws + W_T + l * 8 * MB) + (size_t)(tm & 7) * 128 * 4096 + (tm < 8 ? 0 : 2048) + tn * 128, 128 * 4096 * 2 - (2048 + tn * 128) * 2);
#pragma unroll
            for (int q = 0; q < 2; ++q) {
                const int cc = wn * 64 + q * 32 + fq * 8;
                const f32x4 r0 = *(const LAS f32x4*)(R + cc), r1 = *(const LAS f32x4*)(R + cc + 4), m0 = *(const LAS f32x4*)(M + cc), m1 = *(const LAS f32x4*)(M + cc + 4);
#pragma unroll
                for (int m = 0; m < 4; ++m) {
                    const int row = wm * 64 + (m >> 1) * 32 + inv32((m & 1) * 16 + fr);
                    __builtin_amdgcn_raw_buffer_store_b128(pk8(acc[m][2 * q] * r0 + m0 * wsum[m], acc[m][2 * q + 1] * r1 + m1 * wsum[m]), rC, row * 8192 + cc * 2, 0, STORE_AUX);
                }
            }
            dep_signal(c, ((lane >> 1) * 16 + (tm & 7) * 2 + (lane & 1)) * 32 + (tm >> 3) * 16 + tn, 32, lds, lane);
            STAMP(true, l * 5 + 1, 8 + w);
        }
        {
            PHASE_VARS();
            res_phase<CG2>(lds, x, xa, sp + 65536, tm, tn, w, lane, [&](int t) {
                if (l) return;
                auto rdt = [&](int at, int stride) {
                    LAS unsigned char* p = lds + at + (fr >> 2) * stride + (fr & 3) * 8;
                    const u32x2 a = __builtin_bit_cast(u32x2, __builtin_amdgcn_ds_read_tr16_b64_v4i16((LAS s16x4*)p)), b = __builtin_bit_cast(u32x2, __builtin_amdgcn_ds_read_tr16_b64_v4i16((LAS s16x4*)(p + 4 * stride)));
                    return (u32x4){a[0], a[1], b[0], b[1]};
                };
                const int g = (t - T_IN) / T_STEP * 4 + w, j = (t - T_OUT) / T_STEP;
                if (t >= T_IN && g < 14 && (t - T_IN) % T_STEP == 0) {
                    const __amdgpu_buffer_rsrc_t ri = rsrc((u16*)(ws + W_WINT) + (size_t)tn * 224 * D_, (224 * D_ * 2) & XM16), rs = rsrc(ws + W_WSPIN + (size_t)tn * 224 * 64, (224 * 64) & XM16);
                    const int half = g >= 7, cc = (g - half * 7) * 16 + fr, orow = half * 112 + (cc < 96 ? (cc & ~31) + inv32(cc & 31) : cc);
                    const u32x4 v0 = rdt(L_TIN0 + fq * 8 * 448 + g * 32, 448), v1 = rdt(L_TIN1 + fq * 8 * 448 + g * 32, 448);
                    __builtin_amdgcn_raw_buffer_store_b128(v0, ri, orow * D_ * 2 + fq * 16, tm * 128, STORE_AUX);
                    __builtin_amdgcn_raw_buffer_store_b128(v1, ri, orow * D_ * 2 + 64 + fq * 16, tm * 128, STORE_AUX);
                    const f32x4 sm = (unpk4(v0[0], v0[1]) + unpk4(v0[2], v0[3])) + (unpk4(v1[0], v1[1]) + unpk4(v1[2], v1[3]));
                    const float st = fq_sum((sm[0] + sm[1]) + (sm[2] + sm[3]));
                    if (fq == 0) __builtin_amdgcn_raw_buffer_store_b32(__float_as_uint(st), rs, orow * 64 + tm * 4, 0, STORE_AUX);
                } else if (t >= T_OUT && j < 2 && (t - T_OUT) % T_STEP == 0) {
                    const __amdgpu_buffer_rsrc_t ro = rsrc((u16*)(ws + W_WOUTT) + (size_t)tn * 64 * FH, (64 * FH * 2) & XM16);
                    const int orow = ((w >> 1) * 32 + inv32((w & 1) * 16 + fr)) * FH * 2, k0 = j * 64 + fq * 8, k1 = j ? 96 + (fq & 1) * 8 : 32 + fq * 8;
                    const u32x4 v0 = rdt(L_TOUT + k0 * 128 + w * 32, 128), v1 = rdt(L_TOUT + k1 * 128 + w * 32, 128);
                    __builtin_amdgcn_raw_buffer_store_b128(v0, ro, orow + (tm * 96 + k0) * 2, 0, STORE_AUX);
                    if (j == 0 || fq < 2) __builtin_amdgcn_raw_buffer_store_b128(v1, ro, orow + (j ? 1440 + tm * 16 + k1 : tm * 96 + k1) * 2, 0, STORE_AUX);
                } else if (t == T_SIG) {
                    dep_signal<32>(ctr + 8192, (lane * 16 + tn) * 32 + 16 + tm, 16, lds, lane);
                }
            } ST_PASS(l * 5 + 2));
            dep_signal(c + 8192, (tm * 16 + lane) * 32 + tn, 16, lds, lane);
            STAMP(true, l * 5 + 2, 8 + w);
        }
        u32x4 zz[7];
        {
            PHASE_VARS();
            f32x4 acc[2][14];
            compute_phase<CG3, 2, 14, 7, true, 1, 1>(lds, w * 32, 0, lane, acc, [](int) {} ST_PASS(l * 5 + 3));
            f32x4 s[2], q[2];
#pragma unroll
            for (int m = 0; m < 2; ++m) {
                const f32x4 r = *(const LAS f32x4*)(R + w * 32 + fq * 8 + m * 4), mr = *(const LAS f32x4*)(M + w * 32 + fq * 8 + m * 4);
                s[m] = (f32x4){0.f, 0.f, 0.f, 0.f}; q[m] = s[m];
#pragma unroll
                for (int n = 7; n < 14; ++n) {
                    const f32x4 z = gelu_tanh(acc[m][n] * r + mr * WS[n * 16 + fr]);
                    acc[m][n] = z; s[m] += z; q[m] += z * z;
                }
            }
            float v8[8], v4[4], v2[2];
#pragma unroll
            for (int i = 0; i < 8; ++i) {
                const float lo = (i & 1) ? q[0][i >> 1] : s[0][i >> 1], hi = (i & 1) ? q[1][i >> 1] : s[1][i >> 1];
                v8[i] = ((fr & 8) ? hi : lo) + dpp_mov<0x128>((fr & 8) ? lo : hi);
            }
#pragma unroll
            for (int i = 0; i < 4; ++i) v4[i] = ((fr & 4) ? v8[i + 4] : v8[i]) + dpp_mov<0x141>(dpp_mov<0x1b>((fr & 4) ? v8[i] : v8[i + 4]));
#pragma unroll
            for (int i = 0; i < 2; ++i) v2[i] = ((fr & 2) ? v4[i + 2] : v4[i]) + dpp_mov<0x4e>((fr & 2) ? v4[i] : v4[i + 2]);
            const float pv = ((fr & 1) ? v2[1] : v2[0]) + dpp_mov<0xb1>((fr & 1) ? v2[0] : v2[1]);
            const __amdgpu_buffer_rsrc_t rX = rsrc(ws + W_XCH + l * 262144 + (size_t)(tm * 4 + w) * 4096, 4096);
            __builtin_amdgcn_raw_buffer_store_b32(xch_word(pv), rX, tn * 256 + lane * 4, 0, 16);
            SB0();
            STAMP(ston, l * 5 + 3, 5);
            {
                const float r0 = R[w * 32 + perm32(fr)], r1 = R[w * 32 + perm32(16 + fr)], m0 = M[w * 32 + perm32(fr)], m1 = M[w * 32 + perm32(16 + fr)];
#pragma unroll
                for (int n = 0; n < 7; ++n) {
                    const f32x4 wv = *(const LAS f32x4*)(WS + n * 16 + fq * 4);
                    zz[n] = pk8(gelu_tanh(acc[0][n] * r0 + wv * m0), gelu_tanh(acc[1][n] * r1 + wv * m1));
                    asm volatile("" : "+v"(zz[n]));
                }
            }
            SB0();
            STAMP(ston, l * 5 + 3, 6);
            f32x4 t;
            for (int spin = 0;; ++spin) {
                u32x4 g[4]; bool ok = true; t = (f32x4){0.f, 0.f, 0.f, 0.f};
#pragma unroll
                for (int i = 0; i < 4; ++i) g[i] = __builtin_amdgcn_raw_buffer_load_b128(rX, lane * 16 + i * 1024, 0, 16);
#pragma unroll
                for (int i = 0; i < 4; ++i) {
                    ok &= (g[i][0] != XCH_EMPTY) & (g[i][1] != XCH_EMPTY) & (g[i][2] != XCH_EMPTY) & (g[i][3] != XCH_EMPTY);
                    t += __builtin_bit_cast(f32x4, g[i]);
                }
                if (__all(ok) || spin > SPIN_MAX) break;
                __builtin_amdgcn_s_sleep(4);
            }
            STAMP(ston, l * 5 + 3, 7);
            const float mean0 = fq_sum(t[0]) * (1.0f / FH), mean1 = fq_sum(t[2]) * (1.0f / FH);
            const float r0 = __builtin_amdgcn_rsqf(fmaxf(fq_sum(t[1]) * (1.0f / FH) - mean0 * mean0, 0.f) + LN_EPS);
            const float r1 = __builtin_amdgcn_rsqf(fmaxf(fq_sum(t[3]) * (1.0f / FH) - mean1 * mean1, 0.f) + LN_EPS);
            LAS float* Z = (LAS float*)(lds + L_Z) + w * 64;
            if (lane < 16) { *(LAS f32x2*)(Z + lane * 2) = (f32x2){r0, r1}; *(LAS f32x2*)(Z + 32 + lane * 2) = (f32x2){-mean0 * r0, -mean1 * r1}; }
            WAITCNT(63, 0);
            const f32x4 ra = *(const LAS f32x4*)(Z + fq * 8), rb = *(const LAS f32x4*)(Z + fq * 8 + 4), ma = *(const LAS f32x4*)(Z + 32 + fq * 8), mb = *(const LAS f32x4*)(Z + 32 + fq * 8 + 4);
            const __amdgpu_buffer_rsrc_t rC = rsrc((u16*)(ws + W_Z2T + l * 8 * MB) + (size_t)tn * 112 * S_ + tm * 128 + w * 32, 112 * S_ * 2 - tm * 256 - w * 64);
#pragma unroll
            for (int n = 0; n < 7; ++n)
                __builtin_amdgcn_raw_buffer_store_b128(pk8(acc[0][7 + n] * ra + ma, acc[1][7 + n] * rb + mb), rC, (n * 16 + fr) * S_ * 2 + fq * 16, 0, STORE_AUX);
            dep_signal(c + 2 * 8192, (lane * 16 + tn) * 32 + tm, 16, lds, lane);
            STAMP(true, l * 5 + 3, 8 + w);
        }
        {
            PHASE_VARS();
            float b[2];
#pragma unroll
            for (int m = 0; m < 2; ++m) b[m] = bf_lo(bsp[tm * 128 + w * 32 + perm32(m * 16 + fr)]);
            f32x4 acc[2][7];
            compute_phase<CG4, 2, 7, 7, false, 4, 5>(lds, w * 32, 0, lane, acc, [](int) {} ST_PASS(l * 5 + 4));
            const __amdgpu_buffer_rsrc_t rC = rsrc((u16*)(ws + W_H + l * 8 * MB) + (size_t)(tm * 128 + w * 32) * FH, 32 * FH * 2);
#pragma unroll
            for (int m = 0; m < 2; ++m) {
                const int ro = perm32(m * 16 + fr) * FH * 2;
#pragma unroll
                for (int q = 0; q < 3; ++q)
                    __builtin_amdgcn_raw_buffer_store_b128(pk8(unpk4(zz[2 * q][m * 2], zz[2 * q][m * 2 + 1]) * (acc[m][2 * q] + b[m]), unpk4(zz[2 * q + 1][m * 2], zz[2 * q + 1][m * 2 + 1]) * (acc[m][2 * q + 1] + b[m])),
                                                           rC, ro + q * 64 + fq * 16, tn * 192, STORE_AUX);
                const f32x4 v = unpk4(zz[6][m * 2], zz[6][m * 2 + 1]) * (acc[m][6] + b[m]);
                __builtin_amdgcn_raw_buffer_store_b64((u32x2){pk_bf16(v[0], v[1]), pk_bf16(v[2], v[3])}, rC, ro + 3072 + fq * 8, tn * 32, STORE_AUX);
            }
            dep_signal(c + 3 * 8192, (tm * 16 + lane) * 32 + tn, 16, lds, lane);
            STAMP(true, l * 5 + 4, 8 + w);
        }
        {
            PHASE_VARS();
            res_phase<CG5>(lds, xa, l == 0 ? (u16*)(ws + W_XB) : out, sp + 131072, tm, tn, w, lane, [](int) {} ST_PASS(l * 5 + 5));
            dep_signal(c + 4 * 8192, (lane * 16 + tm) * 32 + tn, 16, lds, lane);
            STAMP(true, l * 5 + 5, 8 + w);
        }
    }
    if (w0 == 0 && blockIdx.x == 77) {
        const unsigned long long te = __builtin_amdgcn_s_memrealtime();
        unsigned long long d = XPH == 99 ? te - xt0 : XPH == 98 ? 0 : *stp - xt0;
        d = d > 20000 ? 20000 : d;
        while (__builtin_amdgcn_s_memrealtime() - te < 200 + d * 4) __builtin_amdgcn_s_sleep(1);
    }
    asm volatile(".Lk_main_e1:");
}

extern "C" void kernel_launch(void* const* d_in, const int* in_sizes, int n_in, void* d_out, int out_size, void* d_ws, size_t ws_size, hipStream_t stream) {
    const u16* x0 = (const u16*)d_in[0];
    const u16* wsre = (const u16*)d_in[1];
    const u16* wsim = (const u16*)d_in[2];
    const u16* wdre = (const u16*)d_in[3];
    const u16* wdim = (const u16*)d_in[4];
    const u16* win = (const u16*)d_in[6];
    const u16* wsp = (const u16*)d_in[7];
    const u16* bsp = (const u16*)d_in[8];
    const u16* wout = (const u16*)d_in[9];
    char* ws = (char*)d_ws;
    (void)hipFuncSetAttribute((const void*)k_main, hipFuncAttributeMaxDynamicSharedMemorySize, LDS_TOTAL);
    k_main<<<256, 512, LDS_TOTAL, stream>>>(x0, wsre, wsim, wdre, wdim, win, wsp, bsp, wout, ws, (u16*)d_out);
}
```

```cpp
#define TILE_AUX 2
#define PFSTART 2
#include <hip/hip_runtime.h>
#include <hip/hip_bf16.h>
#include <utility>

typedef unsigned short u16;
typedef unsigned long long u64;
typedef short bf16x8 __attribute__((ext_vector_type(8)));
typedef short s16x4 __attribute__((ext_vector_type(4)));
typedef float f32x2 __attribute__((ext_vector_type(2)));
typedef float f32x4 __attribute__((ext_vector_type(4)));
typedef unsigned u32x2 __attribute__((ext_vector_type(2)));
typedef unsigned u32x4 __attribute__((ext_vector_type(4)));
#define LAS __attribute__((address_space(3)))
#define GAS __attribute__((address_space(1)))
#define DEV __device__ __forceinline__

constexpr int waitimm(int vm, int lgkm) { return (vm & 15) | (7 << 4) | ((lgkm & 15) << 8) | ((vm >> 4) << 14); }
#define WAITCNT(vm, lgkm) __builtin_amdgcn_s_waitcnt(waitimm(vm, lgkm))
#define BAR() do { asm volatile("" ::: "memory"); __builtin_amdgcn_s_barrier(); asm volatile("" ::: "memory"); } while (0)
#define SB0() __builtin_amdgcn_sched_barrier(0)
#define STAMP(on, ph, i)
#define ST_PARAM
#define ST_PASS(p)

constexpr int S_ = 2048, D_ = 1024, F_ = 3584, FH = 1792;
#define T_IM 12
#define T_IN 0
#define T_STEP 2
#define T_OUT 20
#define T_SIG 40
#define NOPF 0
#define XM2 ~0u
#define XM16 ~0u
#define POLL_SLEEP 2
constexpr float LN_EPS = 1e-5f;
constexpr int SPIN_MAX = 1 << 17;
constexpr int STORE_AUX = 16;

DEV unsigned pk_bf16(float lo, float hi) { unsigned r; asm("v_cvt_pk_bf16_f32 %0, %1, %2" : "=v"(r) : "v"(lo), "v"(hi)); return r; }
DEV float bf_lo(unsigned v) { return __uint_as_float(v << 16); }
DEV float bf_hi(unsigned v) { return __uint_as_float(v & 0xffff0000u); }
DEV __amdgpu_buffer_rsrc_t rsrc(const void* p, unsigned bytes) { return __builtin_amdgcn_make_buffer_rsrc((void*)p, 0, bytes, 0x00020000); }
DEV f32x4 gelu_tanh(f32x4 x) {
    const float c0 = -2.3022081986f, c1 = c0 * 0.044715f;
    const f32x4 a = x * (x * x * c1 + c0);
    f32x4 d = (f32x4){__builtin_amdgcn_exp2f(a[0]), __builtin_amdgcn_exp2f(a[1]), __builtin_amdgcn_exp2f(a[2]), __builtin_amdgcn_exp2f(a[3])} + 1.0f;
    return x * (f32x4){__builtin_amdgcn_rcpf(d[0]), __builtin_amdgcn_rcpf(d[1]), __builtin_amdgcn_rcpf(d[2]), __builtin_amdgcn_rcpf(d[3])};
}
DEV u32x4 pk8(f32x4 a, f32x4 b) { return (u32x4){pk_bf16(a[0], a[1]), pk_bf16(a[2], a[3]), pk_bf16(b[0], b[1]), pk_bf16(b[2], b[3])}; }
DEV f32x4 unpk4(unsigned a, unsigned b) { return (f32x4){bf_lo(a), bf_hi(a), bf_lo(b), bf_hi(b)}; }
__host__ __device__ constexpr int perm32(int slot) { return 8 * ((slot & 15) >> 2) + 4 * ((slot >> 4) & 1) + (slot & 3); }
__host__ __device__ constexpr int inv32(int row) { return 16 * ((row >> 2) & 1) + 4 * ((row >> 3) & 3) + (row & 3); }
DEV float fq_sum(float a) {
    const auto p = __builtin_amdgcn_permlane16_swap(__float_as_uint(a), __float_as_uint(a), false, false);
    a = __uint_as_float(p[0]) + __uint_as_float(p[1]);
    const auto q = __builtin_amdgcn_permlane32_swap(__float_as_uint(a), __float_as_uint(a), false, false);
    return __uint_as_float(q[0]) + __uint_as_float(q[1]);
}
template <int CTRL> DEV float dpp_mov(float a) { return __builtin_bit_cast(float, __builtin_amdgcn_update_dpp(0, __builtin_bit_cast(int, a), CTRL, 0xf, 0xf, true)); }
template <int CTRL> DEV float dpp_add(float a) { return a + __builtin_bit_cast(float, __builtin_amdgcn_update_dpp(0, __builtin_bit_cast(int, a), CTRL, 0xf, 0xf, true)); }
DEV float row16_sum(float a) { a = dpp_add<0x128>(a); a = dpp_add<0x124>(a); a = dpp_add<0x122>(a); return dpp_add<0x121>(a); }
DEV float oct_sum(float a) { a = dpp_add<0xb1>(a); a = dpp_add<0x4e>(a); return dpp_add<0x141>(a); }
DEV float quad_sum(float a) { a = dpp_add<0xb1>(a); return dpp_add<0x4e>(a); }
constexpr unsigned XCH_EMPTY = 0xffffffffu;
DEV unsigned xch_word(float v) { const unsigned u = __float_as_uint(v); return u == XCH_EMPTY ? 0x7fc00000u : u; }

DEV int opaque_v(int v) { asm volatile("" : "+v"(v)); return v; }
DEV int opaque_s(int v) { asm volatile("" : "+s"(v)); return v; }
template <class F, int... I> DEV void static_for_impl(F&& f, std::integer_sequence<int, I...>) { (f(std::integral_constant<int, I>{}), ...); }
template <int N, class F> DEV void static_for(F&& f) { static_for_impl(f, std::make_integer_sequence<int, N>{}); }

template <int BM_, int BN_, int NS_, int BASE_, int LDP_, int LDQ_, int NT_, int KSW_, bool PERMP_, bool PERMQ_, bool WISP_> struct Cfg {
    static constexpr int BM = BM_, BN = BN_, NS = NS_, LDP = LDP_, LDQ = LDQ_, NT = NT_, KSW = KSW_;
    static constexpr bool PERMP = PERMP_, PERMQ = PERMQ_, WISP = WISP_;
    static constexpr int PB = BM * 128, QB = BN * 128, PBASE = BASE_, QBASE = PBASE + NS * PB, RING = QBASE + NS * QB;
    static constexpr int LP = BM / 32, LQ = BN / 32;
    static constexpr int LW = WISP ? LP : LQ, LA = WISP ? LQ : LP;
};
constexpr int RING_MAX = 147456;
constexpr int L_R = RING_MAX, L_M = L_R + 512, L_WD = L_M + 512, L_WS = L_WD + 512, L_Z = L_WS + 1024, L_JUNK = L_Z + 1024, L_SYNC = L_JUNK + 256, L_TAG = L_SYNC + 48, LDS_TOTAL = L_SYNC + 64;
constexpr int L_TIN0 = 0, L_TIN1 = 131072, L_TOUT = 114688;

template <class C, int N0, int N1, class Dep, class Mid, class Tail>
DEV void loader_phase(LAS unsigned char* lds, const u16* P0, const u16* P1, const u16* Q, int lw, int lane, unsigned drained, Dep&& dep, Mid&& mid, Tail&& tail ST_PARAM) {
    constexpr int NS = C::NS, NT = C::NT, LA = C::LA, LW = C::LW;
    static_assert(C::RING <= RING_MAX && NS >= 3 && NT >= 2 * NS && C::BM % 32 == 0 && !(C::PERMQ && C::BN % 32), "");
    lane = opaque_v(lane); lw = opaque_s(lw);
    const __amdgpu_buffer_rsrc_t rP0 = rsrc(P0, C::BM * C::LDP * 2), rP1 = rsrc(P1, C::BM * C::LDP * 2), rQ = rsrc(Q, C::BN * C::LDQ * 2);
    const int ch = ((lane & 7) ^ (((lw & 1) * 4 + (lane >> 4)) & 7)) * 16, row = lw * 8 + (lane >> 3);
    const int vP = (C::PERMP ? perm32(row) : row) * C::LDP * 2 + ch, vQ = (C::PERMQ ? perm32(row) : row) * C::LDQ * 2 + ch;
    auto issueP = [&](int slot, int kt) {
        LAS unsigned char* dst = lds + C::PBASE + slot * C::PB + lw * 1024;
        const bool second = C::KSW && kt >= C::KSW;
        const __amdgpu_buffer_rsrc_t rP = second ? rP1 : rP0;
        const int kp = (second ? kt - C::KSW : kt) * 128;
#pragma unroll
        for (int i = 0; i < C::LP; ++i) __builtin_amdgcn_raw_ptr_buffer_load_lds(rP, (LAS void*)(dst + i * 4096), 16, vP, i * 32 * C::LDP * 2 + kp, 0, C::WISP ? 0 : 16);
    };
    auto issueQ = [&](int slot, int kt) {
        LAS unsigned char* dst = lds + C::QBASE + slot * C::QB + lw * 1024;
#pragma unroll
        for (int j = 0; j < (C::BN + 31) / 32; ++j)
            if (j < C::LQ || j * 4 + lw < C::BN / 8) __builtin_amdgcn_raw_ptr_buffer_load_lds(rQ, (LAS void*)(dst + j * 4096), 16, vQ, j * 32 * C::LDQ * 2 + kt * 128, 0, 16);
    };
    auto issueW = [&](int slot, int kt) { if (C::WISP) issueP(slot, kt); else issueQ(slot, kt); };
    auto issueA = [&](int slot, int kt) { if (C::WISP) issueQ(slot, kt); else issueP(slot, kt); };
    if (drained) {
        while ((unsigned)__builtin_amdgcn_readfirstlane(*(volatile LAS unsigned*)(lds + L_SYNC + 16)) < drained) __builtin_amdgcn_s_sleep(2);
#pragma unroll 1
        for (int s = 0; s < NS; ++s) issueW(s, s);
        dep();
        STAMP(ston, ph, 4);
        issueA(0, 0); issueA(1, 1);
        WAITCNT(LA, 15);
        BAR();
#pragma unroll
        for (int s = 2; s < NS; ++s) issueA(s, s);
    } else {
        issueW(0, 0); issueA(0, 0); issueW(1, 1); issueA(1, 1);
        STAMP(ston, ph, 4);
        WAITCNT(LA + LW, 15);
        BAR();
#pragma unroll 1
        for (int s = 2; s < NS; ++s) { issueW(s, s); issueA(s, s); }
    }
    static_for<NS - 2>([&](auto ic) {
        constexpr int t = decltype(ic)::value;
        WAITCNT((NS - 2 - t) * LA + t * (LA + LW), 15);
        BAR();
        issueW(t, t + NS); issueA(t, t + NS);
    });
    int sl = NS - 2;
#pragma unroll 1
    for (int t = NS - 2; t < NT - NS; ++t) {
        WAITCNT((NS - 2) * (LA + LW), 15);
        BAR();
        issueW(sl, t + NS); issueA(sl, t + NS);
        sl = (sl + 1 == NS) ? 0 : sl + 1;
        mid(t);
    }
    tail(std::integral_constant<int, 0>{});
    static_for<NS - 1>([&](auto ic) {
        constexpr int i = decltype(ic)::value;
        WAITCNT((NS - 2 - i) * (LA + LW) + N0 + (i ? N1 : 0), 15);
        BAR();
        if constexpr (i == 0) tail(std::integral_constant<int, 1>{});
    });
    tail(std::integral_constant<int, 2>{});
    WAITCNT(63, 0);
    BAR();
}

template <class C, int MT, int NTL, int NSWAP, bool ROLL, int RNUM, int RDEN, class Hook>
DEV void compute_phase(LAS unsigned char* lds, int prow0, int qrow0, int lane, f32x4 (&acc)[MT][NTL], Hook&& hook ST_PARAM) {
    constexpr int NR = MT + NTL, NM = MT * NTL, NT = C::NT;
    const int fr = lane & 15, fq = lane >> 4;
    int aoff[2], boff[2];
#pragma unroll
    for (int k = 0; k < 2; ++k) {
        const int ch = ((k * 4 + fq) ^ ((fr >> 1) & 7)) * 16;
        aoff[k] = C::PBASE + (prow0 + fr) * 128 + ch;
        boff[k] = C::QBASE + (qrow0 + fr) * 128 + ch;
    }
#pragma unroll
    for (int m = 0; m < MT; ++m)
#pragma unroll
        for (int n = 0; n < NTL; ++n) { acc[m][n] = (f32x4){0.f, 0.f, 0.f, 0.f}; asm volatile("" : "+v"(acc[m][n])); }
    auto mfma = [&](int m, int n, const bf16x8& a, const bf16x8& b) {
        if (n >= NSWAP) asm volatile("v_mfma_f32_16x16x32_bf16 %0, %1, %2, %0" : "+v"(acc[m][n]) : "v"(a), "v"(b));
        else asm volatile("v_mfma_f32_16x16x32_bf16 %0, %1, %2, %0" : "+v"(acc[m][n]) : "v"(b), "v"(a));
    };
    int sp = 0, sq = 0;
    auto next = [&]() { sp = (sp + C::PB == C::NS * C::PB) ? 0 : sp + C::PB; sq = (sq + C::QB == C::NS * C::QB) ? 0 : sq + C::QB; };
    BAR();
    STAMP(ston, ph, 1);
    if constexpr (!ROLL) {
        bf16x8 fa[4][MT], fb[4][NTL];
        auto rd1 = [&](int set, int k, int i) {
            if (i == 0) fa[set][0] = *(const LAS bf16x8*)(lds + sp + aoff[k]);
            else if (i <= NTL) fb[set][i - 1] = *(const LAS bf16x8*)(lds + sq + boff[k] + (i - 1) * 2048);
            else fa[set][i - NTL] = *(const LAS bf16x8*)(lds + sp + aoff[k] + (i - NTL) * 2048);
        };
        auto stg = [&](int set, bool rd) {
#pragma unroll
            for (int i = 0; i < 2 * NM; ++i) {
                const int st = set + i / NM, m = (i % NM) / NTL, n = i % NTL;
                mfma(m, n, fa[st][m], fb[st][n]); SB0();
                if (rd) {
#pragma unroll
                    for (int r = (i * RNUM + RDEN - 1) / RDEN; r < ((i + 1) * RNUM + RDEN - 1) / RDEN; ++r) if (r < 2 * NR) rd1((set ^ 2) + r / NR, r / NR, r % NR);
                    SB0();
                }
            }
        };
        static_assert((2 * NM * RNUM + RDEN - 1) / RDEN >= 2 * NR && NT % 2 == 0, "");
#pragma unroll
        for (int r = 0; r < 2 * NR; ++r) rd1(r / NR, r / NR, r % NR);
#pragma unroll 1
        for (int t = 0; t < NT - 2; t += 2) {
            next(); WAITCNT(63, 0); BAR(); SB0();
            stg(0, true);
            next(); WAITCNT(63, 0); BAR(); SB0();
            stg(2, true);
            hook(t); SB0();
        }
        next();
        stg(0, false);
        BAR(); SB0();
#pragma unroll
        for (int r = 0; r < 2 * NR; ++r) rd1(2 + r / NR, r / NR, r % NR);
        WAITCNT(63, 0); BAR(); SB0();
        stg(2, false);
    } else {
        static_assert(MT == 2, "");
        constexpr int G = 8;
        bf16x8 fa[2][2], fb[NTL];
        auto rda = [&](int set, int k, int m) { fa[set][m] = *(const LAS bf16x8*)(lds + sp + aoff[k] + m * 2048); };
        auto rdb = [&](int k, int n) { fb[n] = *(const LAS bf16x8*)(lds + sq + boff[k] + n * 2048); };
        auto half0 = [&]() {
#pragma unroll
            for (int j = 0; j < NM; ++j) {
                mfma(j & 1, j >> 1, fa[0][j & 1], fb[j >> 1]); SB0();
                if (j & 1) rdb(1, j >> 1); else if (j < 4) rda(1, 1, j >> 1);
                SB0();
            }
        };
        auto half1 = [&](bool rd) {
#pragma unroll
            for (int j = 0; j < NM; ++j) {
                if (j == G) { next(); WAITCNT(63, 0); BAR(); SB0(); }
                mfma(j & 1, j >> 1, fa[1][j & 1], fb[j >> 1]); SB0();
                if (rd && j >= G) {
                    if (j < G + 2) rda(0, 0, j - G);
#pragma unroll
                    for (int n = 0; n < NTL; ++n) if ((G + 2 + n > 2 * n + 1 ? G + 2 + n : 2 * n + 1) == j) rdb(0, n);
                    SB0();
                }
            }
        };
        rda(0, 0, 0); rda(0, 0, 1);
#pragma unroll
        for (int n = 0; n < NTL; ++n) rdb(0, n);
        SB0();
#pragma unroll 1
        for (int t = 0; t < NT - 1; ++t) { half0(); half1(true); }
        half0(); half1(false);
    }
    asm volatile("s_nop 15" ::: "memory");
#pragma unroll
    for (int m = 0; m < MT; ++m)
#pragma unroll
        for (int n = 0; n < NTL; ++n) asm volatile("" : "+v"(acc[m][n]));
    STAMP(ston, ph, 2);
}

DEV u64 launch_tag(LAS unsigned char* lds) { return *(volatile LAS u64*)(lds + L_TAG); }
DEV void box_poll(const u64* box, int nprod, u64 tag, int lane) {
#pragma unroll 1
    for (int spin = 0; spin < SPIN_MAX; ++spin) {
        const u64 v = __hip_atomic_load((const GAS u64*)box + (lane & (nprod - 1)), __ATOMIC_RELAXED, __HIP_MEMORY_SCOPE_AGENT);
        if (__all(v == tag)) break;
        __builtin_amdgcn_s_sleep(POLL_SLEEP);
    }
}
DEV void dep_wait(const u64* box, int nprod, LAS unsigned char* lds, unsigned epoch, int lw, int lane) {
    volatile LAS unsigned* flag = (volatile LAS unsigned*)(lds + L_SYNC);
    if (lw == 0) {
        box_poll(box, nprod, launch_tag(lds), lane);
        *flag = epoch;
    } else {
        while ((unsigned)__builtin_amdgcn_readfirstlane(*flag) < epoch) __builtin_amdgcn_s_sleep(1);
    }
    asm volatile("" ::: "memory");
}
template <int CNT = 16> DEV void dep_signal(u64* boxes, int word, int ncons, LAS unsigned char* lds, int lane) {
    const u64 tag = launch_tag(lds);
    asm volatile("s_waitcnt vmcnt(0)" ::: "memory");
    unsigned old = 0;
    if (lane == 0) old = __hip_atomic_fetch_add((LAS unsigned*)(lds + L_SYNC + CNT), 1u, __ATOMIC_RELAXED, __HIP_MEMORY_SCOPE_WORKGROUP);
    if ((__builtin_amdgcn_readfirstlane(old) & 3) == 3 && lane < ncons) __hip_atomic_store((GAS u64*)boxes + word, tag, __ATOMIC_RELAXED, __HIP_MEMORY_SCOPE_AGENT);
}
DEV void touch(LAS unsigned char* lds, const u16* p, unsigned bytes, int off) {
    __builtin_amdgcn_raw_ptr_buffer_load_lds(rsrc(p, bytes), (LAS void*)(lds + L_JUNK), 4, off, 0, 0, 0);
}

DEV void row_tables_load(const float* sp, unsigned bytes, int tid, f32x4 (&v)[4]) {
    const __amdgpu_buffer_rsrc_t r = rsrc(sp, bytes);
#pragma unroll
    for (int i = 0; i < 4; ++i) v[i] = __builtin_bit_cast(f32x4, __builtin_amdgcn_raw_buffer_load_b128(r, i * 4096 + tid * 16, 0, 16));
}
DEV void row_tables_finish(LAS unsigned char* lds, int tid, const f32x4 (&v)[4], float sign) {
#pragma unroll
    for (int i = 0; i < 4; ++i) {
        const float s = oct_sum(v[i][0] + v[i][2]), q = oct_sum(v[i][1] + v[i][3]);
        const float mean = s * (1.0f / D_), var = fmaxf(q * (1.0f / D_) - mean * mean, 0.f), r = sign * __builtin_amdgcn_rsqf(var + LN_EPS);
        if ((tid & 7) == 0) { ((LAS float*)(lds + L_R))[i * 32 + (tid >> 3)] = r; ((LAS float*)(lds + L_M))[i * 32 + (tid >> 3)] = -mean * r; }
    }
}

constexpr size_t MB = 1 << 20;
constexpr size_t W_T = 0, W_XA = 16 * MB, W_XB = 24 * MB, W_Z2T = 28 * MB, W_H = 44 * MB, W_WINT = 60 * MB, W_WOUTT = 68 * MB;
constexpr size_t W_SP = 72 * MB, W_GRAN = 74 * MB, W_WSPIN = 74 * MB + 65536, W_XCH = 74 * MB + 512 * 1024, W_CTR = 75 * MB, W_NONCE = 75 * MB + 768 * 1024;

typedef Cfg<128, 128, 4, 0, D_, D_, D_ / 64, 0, false, true, true> CG1;
typedef Cfg<128, 64, 4, 16384, S_, 4096, 64, 32, false, false, true> CG2;
typedef Cfg<128, 224, 3, 0, D_, D_, D_ / 64, 0, true, false, false> CG3;
typedef Cfg<128, 112, 4, 0, S_, S_, S_ / 64, 0, true, false, true> CG4;
typedef Cfg<128, 64, 4, 0, FH, FH, FH / 64, 0, false, false, false> CG5;

template <class C, class Hook>
DEV void res_phase(LAS unsigned char* lds, const u16* res, u16* out, float* sp, int tm, int tn, int w, int lane, Hook&& hook ST_PARAM) {
    const int fr = lane & 15, fq = lane >> 4;
    const __amdgpu_buffer_rsrc_t rX = rsrc(res + (size_t)(tm * 128 + w * 32) * D_ + tn * 64, 32 * D_ * 2 - tn * 128);
    u32x4 xr[2][2];
#pragma unroll
    for (int m = 0; m < 2; ++m)
#pragma unroll
        for (int h = 0; h < 2; ++h) xr[m][h] = __builtin_amdgcn_raw_buffer_load_b128(rX, (m * 16 + fr) * D_ * 2 + h * 64 + fq * 16, 0, 16);
    f32x4 acc[2][4];
    compute_phase<C, 2, 4, 4, false, 1, 1>(lds, w * 32, 0, lane, acc, hook ST_PASS(ph));
    const __amdgpu_buffer_rsrc_t rC = rsrc(out + (size_t)(tm * 128 + w * 32) * D_ + tn * 64, 32 * D_ * 2 - tn * 128);
    const __amdgpu_buffer_rsrc_t rS = rsrc(sp + (size_t)(tm * 128 + w * 32) * 32, 32 * 128);
#pragma unroll
    for (int m = 0; m < 2; ++m) {
        f32x4 s = (f32x4){0.f, 0.f, 0.f, 0.f}, qq = s;
#pragma unroll
        for (int h = 0; h < 2; ++h) {
            const u32x4 o = pk8(acc[m][2 * h] + unpk4(xr[m][h][0], xr[m][h][1]), acc[m][2 * h + 1] + unpk4(xr[m][h][2], xr[m][h][3]));
            __builtin_amdgcn_raw_buffer_store_b128(o, rC, (m * 16 + fr) * D_ * 2 + h * 64 + fq * 16, 0, STORE_AUX);
            const f32x4 a = unpk4(o[0], o[1]), b = unpk4(o[2], o[3]);
            s += a + b; qq += a * a + b * b;
        }
        const float st = fq_sum((s[0] + s[1]) + (s[2] + s[3])), qt = fq_sum((qq[0] + qq[1]) + (qq[2] + qq[3]));
        if (fq == 0) __builtin_amdgcn_raw_buffer_store_b64((u32x2){__float_as_uint(st), __float_as_uint(qt)}, rS, (m * 16 + fr) * 128 + tn * 8, 0, 16);
    }
}

__global__ __launch_bounds__(512, 2) void k_main(const u16* __restrict__ x0, const u16* __restrict__ wsre, const u16* __restrict__ wsim, const u16* __restrict__ wdre,
                                                 const u16* __restrict__ wdim, const u16* __restrict__ win, const u16* __restrict__ wsp, const u16* __restrict__ bsp,
                                                 const u16* __restrict__ wout, char* ws, u16* out) {
    extern __shared__ __attribute__((aligned(1024))) unsigned char shm[];
    LAS unsigned char* lds = (LAS unsigned char*)shm;
    const int tid = threadIdx.x, lane0 = tid & 63, w0 = __builtin_amdgcn_readfirstlane(tid >> 6);
    const int tm0 = ((blockIdx.x & 7) >> 1) * 4 + (blockIdx.x >> 6), tn0 = (blockIdx.x & 1) * 8 + ((blockIdx.x >> 3) & 7);
#define PHASE_VARS() const int lane = opaque_v(lane0), w = opaque_s(w0), tm = opaque_s(tm0), tn = opaque_s(tn0), fr = lane & 15, fq = lane >> 4; (void)fr; (void)fq; (void)w
    u64* ctr = (u64*)(ws + W_CTR);
    if (w0 >= 4) {
        const int lw = w0 - 4;
        auto none = [&](auto) {};
#pragma unroll 1
        for (int l = 0; l < 2; ++l) {
            const u16* x = l == 0 ? x0 : (const u16*)(ws + W_XB);
            const u16* T = (const u16*)(ws + W_T + l * 8 * MB); const u16* xa = (const u16*)(ws + W_XA + l * 4 * MB);
            const u16* z2t = (const u16*)(ws + W_Z2T + l * 8 * MB); const u16* h = (const u16*)(ws + W_H + l * 8 * MB);
            const float* sp = (const float*)(ws + W_SP + l * 512 * 1024);
            const u64* c = ctr + l * 5 * 8192;
            const unsigned first = l == 0 ? ~0u : 0u;
            f32x4 v[4], u[4];
            { PHASE_VARS(); const int lt = (w - 4) * 64 + lane;
            const u16* pw = (tm < 8 ? wdre : wdim) + (size_t)(tm & 7) * 128 * D_;
            const __amdgpu_buffer_rsrc_t rg = rsrc(ws + W_GRAN, 65536 & first);
            const int goff = (lt < 128 ? tn * 128 + lt : 1920 + tm * 128 + lt) * 16;
            u32x4 g;
            loader_phase<CG1, 6, 7>(lds, pw, pw, x + (size_t)tn * 128 * D_, lw, lane, l * 20, [&]() { dep_wait(ctr + 4 * 8192 + (tm * 16 + tn) * 32, 16, lds, 5, lw, lane); }, none, [&](auto ic) {
                constexpr int i = decltype(ic)::value;
                if constexpr (i == 0) {
                    row_tables_load(sp + tn * 128 * 32, 16384 & ~first, lt, v);
                    g = __builtin_amdgcn_raw_buffer_load_b128(rg, goff, 0, 16);
                    touch(lds, wsre + (size_t)(tm * 128 + tn * 8 + lw * 2) * S_, (PFSTART & 1) ? 0 : 8192 & first, lane * 128);
                } else if constexpr (i == 1) {
                    const __amdgpu_buffer_rsrc_t r = rsrc(win + (size_t)(tm * 64 + (lw >> 1) * 32) * F_, (32 * F_ * 2) & first & XM2);
#pragma unroll
                    for (int j = 0; j < 7; ++j) {
                        const int q = (lw & 1) * 7 + j, cb = q * 64 + lane, row = cb / 28, ch = cb - row * 28;
                        __builtin_amdgcn_raw_ptr_buffer_load_lds(r, (LAS void*)(lds + (lw < 2 ? L_TIN0 : L_TIN1) + q * 1024), 16, row * F_ * 2 + (ch < 14 ? ch * 16 : FH * 2 - 224 + ch * 16), tn * 224, 0, TILE_AUX);
                    }
                } else {
                    const float sign = tm < 8 ? 1.0f : -1.0f;
                    if (l) { row_tables_finish(lds, lt, v, sign); return; }
                    const u64 tag = launch_tag(lds);
                    if (!__all(g[1] == (unsigned)tag && g[3] == (unsigned)(tag >> 32))) {
                        asm volatile("" ::: "memory");
#pragma unroll 1
                        for (int spin = 0; spin < SPIN_MAX; ++spin) {
                            g = __builtin_amdgcn_raw_buffer_load_b128(rg, goff, 0, 16);
                            if (__all(g[1] == (unsigned)tag && g[3] == (unsigned)(tag >> 32))) break;
                            __builtin_amdgcn_s_sleep(POLL_SLEEP);
                        }
                    }
                    const float mean = __uint_as_float(g[0]) * (1.0f / D_), var = fmaxf(__uint_as_float(g[2]) * (1.0f / D_) - mean * mean, 0.f), rs = sign * __builtin_amdgcn_rsqf(var + LN_EPS);
                    if (lt < 128) { ((LAS float*)(lds + L_R))[lt] = rs; ((LAS float*)(lds + L_M))[lt] = -mean * rs; }
                    else ((LAS float*)(lds + L_WD))[lt - 128] = __uint_as_float(g[0]);
                }
            } ST_PASS(l * 5 + 1)); }
            { PHASE_VARS();
            u64 tw;
            const u64* tbox = ctr + 8192 + (tm * 16 + tn) * 32 + 16;
            loader_phase<CG2, 1, 0>(lds, wsre + (size_t)tm * 128 * S_, wsim + (size_t)tm * 128 * S_, T + (size_t)tn * 64 * 4096, lw, lane, l * 20 + 4,
                              [&]() { dep_wait(c + (tm * 16 + tn) * 32, 32, lds, l * 5 + 1, lw, lane); },
                              [&](int t) {
                                  if (t != T_IM || l) return;
                                  if (!(PFSTART & 2)) touch(lds, wsim + (size_t)(tm * 128 + tn * 8 + lw * 2) * S_, 8192, lane * 128);
                                  const __amdgpu_buffer_rsrc_t r = rsrc(wout + (size_t)tm * 112 * D_, (112 * D_ * 2) & XM2);
#pragma unroll
                                  for (int j = 0; j < 4; ++j)
                                      if (j < 3 || lw < 2) __builtin_amdgcn_raw_ptr_buffer_load_lds(r, (LAS void*)(lds + L_TOUT + (j * 4 + lw) * 1024), 16, ((j * 4 + lw) * 8 + (lane >> 3)) * D_ * 2 + (lane & 7) * 16, tn * 128, 0, TILE_AUX);
                              },
                              [&](auto ic) {
                                  constexpr int i = decltype(ic)::value;
                                  if constexpr (i == 0) tw = __hip_atomic_load((const GAS u64*)tbox + (lane & 15), __ATOMIC_RELAXED, __HIP_MEMORY_SCOPE_AGENT);
                                  if constexpr (i == 2) { const u64 tag = launch_tag(lds); if (!__all(tw == tag)) box_poll(tbox, 16, tag, lane); }
                              } ST_PASS(l * 5 + 2)); }
            { PHASE_VARS(); const int lt = (w - 4) * 64 + lane;
            loader_phase<CG3, 9, 0>(lds, xa + (size_t)tm * 128 * D_, xa, (const u16*)(ws + W_WINT) + (size_t)tn * 224 * D_, lw, lane, l * 20 + 8,
                              [&]() { dep_wait(c + 8192 + (tm * 16 + tn) * 32, 16, lds, l * 5 + 2, lw, lane);
                                      if (blockIdx.x == 0 && l == 0 && lt == 0) __hip_atomic_store((GAS u64*)(ws + W_NONCE), launch_tag(lds), __ATOMIC_RELAXED, __HIP_MEMORY_SCOPE_AGENT); },
                              none, [&](auto ic) {
                                  constexpr int i = decltype(ic)::value;
                                  if constexpr (i == 0) {
                                      row_tables_load(sp + 65536 + tm * 128 * 32, 16384, lt, v);
                                      const __amdgpu_buffer_rsrc_t r = rsrc(ws + W_WSPIN + (size_t)tn * 224 * 64, 224 * 64);
#pragma unroll
                                      for (int j = 0; j < 4; ++j) u[j] = __builtin_bit_cast(f32x4, __builtin_amdgcn_raw_buffer_load_b128(r, j * 4096 + lt * 16, 0, 16));
                                      touch(lds, wsp + (size_t)(tm * 128 + tn * 8 + lw * 2) * S_, (NOPF & 4) ? 0 : 8192 & first, lane * 128);
                                  } else if constexpr (i == 2) {
                                      row_tables_finish(lds, lt, v, 1.0f);
#pragma unroll
                                      for (int j = 0; j < 4; ++j) { const float s = quad_sum((u[j][0] + u[j][1]) + (u[j][2] + u[j][3])); if ((lt & 3) == 0) ((LAS float*)(lds + L_WS))[j * 64 + (lt >> 2)] = s; }
                                  }
                              } ST_PASS(l * 5 + 3)); }
            { PHASE_VARS();
            loader_phase<CG4, 0, 0>(lds, wsp + (size_t)tm * 128 * S_, wsp, z2t + (size_t)tn * 112 * S_, lw, lane, l * 20 + 12, [&]() { dep_wait(c + 2 * 8192 + (tm * 16 + tn) * 32, 16, lds, l * 5 + 3, lw, lane); }, none, none ST_PASS(l * 5 + 4)); }
            { PHASE_VARS();
            loader_phase<CG5, 0, 0>(lds, h + (size_t)tm * 128 * FH, h, (const u16*)(ws + W_WOUTT) + (size_t)tn * 64 * FH, lw, lane, l * 20 + 16,
                              [&]() { dep_wait(c + 3 * 8192 + (tm * 16 + tn) * 32, 16, lds, l * 5 + 4, lw, lane); }, none, none ST_PASS(l * 5 + 5)); }
        }
        asm volatile(".Lk_main_e0:");
        return;
    }
    {
        PHASE_VARS();
        u64 z = __hip_atomic_load((const GAS u64*)(ws + W_NONCE), __ATOMIC_RELAXED, __HIP_MEMORY_SCOPE_AGENT);
        const int xr = tn * 128 + tm * 8 + w * 2, wr = (tm & 7) * 128 + tn * 8 + w * 2;
        const __amdgpu_buffer_rsrc_t rx = rsrc(x0 + (size_t)xr * D_, 4096), rw = rsrc((tm < 8 ? wdre : wdim) + (size_t)wr * D_, 4096);
        u32x4 a[4], b[4];
#pragma unroll
        for (int i = 0; i < 4; ++i) { a[i] = __builtin_amdgcn_raw_buffer_load_b128(rx, i * 1024 + lane * 16, 0, 0); b[i] = __builtin_amdgcn_raw_buffer_load_b128(rw, i * 1024 + lane * 16, 0, 0); }
        u64 pc; unsigned len;
        asm volatile("s_getpc_b64 %0\n.Lk_main_pc:\n\ts_max_i32 %1, .Lk_main_e0-.Lk_main_pc, 0\n\ts_max_i32 %1, .Lk_main_e1-.Lk_main_pc, %1" : "=s"(pc), "=s"(len) :: "scc");
        touch(lds, (const u16*)pc, len, (w * 64 + lane) * 128); touch(lds, (const u16*)pc, len, (256 + w * 64 + lane) * 128);
        if (PFSTART & 1 & ~NOPF) touch(lds, wsre + (size_t)(tm * 128 + tn * 8 + w * 2) * S_, 8192, lane * 128);
        if (PFSTART & 2 & ~NOPF) touch(lds, wsim + (size_t)(tm * 128 + tn * 8 + w * 2) * S_, 8192, lane * 128);
        STAMP(ston, 1, 5);
        if (tid < 128) __builtin_amdgcn_raw_buffer_store_b128((u32x4){XCH_EMPTY, XCH_EMPTY, XCH_EMPTY, XCH_EMPTY}, rsrc(ws + W_XCH + blockIdx.x * 2048, 2048), tid * 16, 0, 16);
        if (tid < 12) *(LAS unsigned*)(lds + L_SYNC + tid * 4) = 0u;
        z += 0x9E3779B97F4A7C15ull; z = (z ^ (z >> 30)) * 0xBF58476D1CE4E5B9ull; z = (z ^ (z >> 27)) * 0x94D049BB133111EBull; z ^= z >> 31;
        if (tid == 0) *(LAS u64*)(lds + L_TAG) = z;
        STAMP(ston, 1, 6);
        float g0 = 0.f, g1 = 0.f;
#pragma unroll
        for (int r = 0; r < 2; ++r) {
            f32x4 sx = (f32x4){0.f, 0.f, 0.f, 0.f}, qx = sx, sw = sx;
#pragma unroll
            for (int i = 0; i < 2; ++i) {
                const f32x4 a0 = unpk4(a[2 * r + i][0], a[2 * r + i][1]), a1 = unpk4(a[2 * r + i][2], a[2 * r + i][3]);
                sx += a0 + a1; qx += a0 * a0 + a1 * a1;
                sw += unpk4(b[2 * r + i][0], b[2 * r + i][1]) + unpk4(b[2 * r + i][2], b[2 * r + i][3]);
            }
            const float s1 = fq_sum(row16_sum((sx[0] + sx[1]) + (sx[2] + sx[3]))), q1 = fq_sum(row16_sum((qx[0] + qx[1]) + (qx[2] + qx[3]))), s2 = fq_sum(row16_sum((sw[0] + sw[1]) + (sw[2] + sw[3])));
            if (lane == r) { g0 = s1; g1 = q1; }
            if (lane == 2 + r) g0 = s2;
        }
        if (lane < 4) __builtin_amdgcn_raw_buffer_store_b128((u32x4){__float_as_uint(g0), (unsigned)z, __float_as_uint(g1), (unsigned)(z >> 32)}, rsrc(ws + W_GRAN, 65536),
                                                             (lane < 2 ? xr + lane : 2046 + (tm >> 3) * 1024 + wr + lane) * 16, 0, 16);
        STAMP(ston, 1, 7);
    }
    WAITCNT(63, 0);
    const LAS float* R = (const LAS float*)(lds + L_R); const LAS float* M = (const LAS float*)(lds + L_M); const LAS float* WS = (const LAS float*)(lds + L_WS);
#pragma unroll 1
    for (int l = 0; l < 2; ++l) {
        const u16* x = l == 0 ? x0 : (const u16*)(ws + W_XB);
        u16* xa = (u16*)(ws + W_XA + l * 4 * MB);
        float* sp = (float*)(ws + W_SP + l * 512 * 1024);
        u64* c = ctr + l * 5 * 8192;
        {
            PHASE_VARS();
            const int wm = w >> 1, wn = w & 1;
            f32x4 acc[4][4];
            compute_phase<CG1, 4, 4, 4, false, 2, 3>(lds, wm * 64, wn * 64, lane, acc, [](int) {} ST_PASS(l * 5 + 1));
            float wsum[4];
#pragma unroll
            for (int m = 0; m < 4; ++m) wsum[m] = ((const LAS float*)(lds + L_WD))[wm * 64 + m * 16 + fr];
            const __amdgpu_buffer_rsrc_t rC = rsrc((u16*)(ws + W_T + l * 8 * MB) + (size_t)(tm & 7) * 128 * 4096 + (tm < 8 ? 0 : 2048) + tn * 128, 128 * 4096 * 2 - (2048 + tn * 128) * 2);
#pragma unroll
            for (int q = 0; q < 2; ++q) {
                const int cc = wn * 64 + q * 32 + fq * 8;
                const f32x4 r0 = *(const LAS f32x4*)(R + cc), r1 = *(const LAS f32x4*)(R + cc + 4), m0 = *(const LAS f32x4*)(M + cc), m1 = *(const LAS f32x4*)(M + cc + 4);
#pragma unroll
                for (int m = 0; m < 4; ++m) {
                    const int row = wm * 64 + (m >> 1) * 32 + inv32((m & 1) * 16 + fr);
                    __builtin_amdgcn_raw_buffer_store_b128(pk8(acc[m][2 * q] * r0 + m0 * wsum[m], acc[m][2 * q + 1] * r1 + m1 * wsum[m]), rC, row * 8192 + cc * 2, 0, STORE_AUX);
                }
            }
            dep_signal(c, ((lane >> 1) * 16 + (tm & 7) * 2 + (lane & 1)) * 32 + (tm >> 3) * 16 + tn, 32, lds, lane);
            STAMP(true, l * 5 + 1, 8 + w);
        }
        {
            PHASE_VARS();
            res_phase<CG2>(lds, x, xa, sp + 65536, tm, tn, w, lane, [&](int t) {
                if (l) return;
                auto rdt = [&](int at, int stride) {
                    LAS unsigned char* p = lds + at + (fr >> 2) * stride + (fr & 3) * 8;
                    const u32x2 a = __builtin_bit_cast(u32x2, __builtin_amdgcn_ds_read_tr16_b64_v4i16((LAS s16x4*)p)), b = __builtin_bit_cast(u32x2, __builtin_amdgcn_ds_read_tr16_b64_v4i16((LAS s16x4*)(p + 4 * stride)));
                    return (u32x4){a[0], a[1], b[0], b[1]};
                };
                const int g = (t - T_IN) / T_STEP * 4 + w, j = (t - T_OUT) / T_STEP;
                if (t >= T_IN && g < 14 && (t - T_IN) % T_STEP == 0) {
                    const __amdgpu_buffer_rsrc_t ri = rsrc((u16*)(ws + W_WINT) + (size_t)tn * 224 * D_, (224 * D_ * 2) & XM16), rs = rsrc(ws + W_WSPIN + (size_t)tn * 224 * 64, (224 * 64) & XM16);
                    const int half = g >= 7, cc = (g - half * 7) * 16 + fr, orow = half * 112 + (cc < 96 ? (cc & ~31) + inv32(cc & 31) : cc);
                    const u32x4 v0 = rdt(L_TIN0 + fq * 8 * 448 + g * 32, 448), v1 = rdt(L_TIN1 + fq * 8 * 448 + g * 32, 448);
                    __builtin_amdgcn_raw_buffer_store_b128(v0, ri, orow * D_ * 2 + fq * 16, tm * 128, STORE_AUX);
                    __builtin_amdgcn_raw_buffer_store_b128(v1, ri, orow * D_ * 2 + 64 + fq * 16, tm * 128, STORE_AUX);
                    const f32x4 sm = (unpk4(v0[0], v0[1]) + unpk4(v0[2], v0[3])) + (unpk4(v1[0], v1[1]) + unpk4(v1[2], v1[3]));
                    const float st = fq_sum((sm[0] + sm[1]) + (sm[2] + sm[3]));
                    if (fq == 0) __builtin_amdgcn_raw_buffer_store_b32(__float_as_uint(st), rs, orow * 64 + tm * 4, 0, STORE_AUX);
                } else if (t >= T_OUT && j < 2 && (t - T_OUT) % T_STEP == 0) {
                    const __amdgpu_buffer_rsrc_t ro = rsrc((u16*)(ws + W_WOUTT) + (size_t)tn * 64 * FH, (64 * FH * 2) & XM16);
                    const int orow = ((w >> 1) * 32 + inv32((w & 1) * 16 + fr)) * FH * 2, k0 = j * 64 + fq * 8, k1 = j ? 96 + (fq & 1) * 8 : 32 + fq * 8;
                    const u32x4 v0 = rdt(L_TOUT + k0 * 128 + w * 32, 128), v1 = rdt(L_TOUT + k1 * 128 + w * 32, 128);
                    __builtin_amdgcn_raw_buffer_store_b128(v0, ro, orow + (tm * 96 + k0) * 2, 0, STORE_AUX);
                    if (j == 0 || fq < 2) __builtin_amdgcn_raw_buffer_store_b128(v1, ro, orow + (j ? 1440 + tm * 16 + k1 : tm * 96 + k1) * 2, 0, STORE_AUX);
                } else if (t == T_SIG) {
                    dep_signal<32>(ctr + 8192, (lane * 16 + tn) * 32 + 16 + tm, 16, lds, lane);
                }
            } ST_PASS(l * 5 + 2));
            dep_signal(c + 8192, (tm * 16 + lane) * 32 + tn, 16, lds, lane);
            STAMP(true, l * 5 + 2, 8 + w);
        }
        u32x4 zz[7];
        {
            PHASE_VARS();
            f32x4 acc[2][14];
            compute_phase<CG3, 2, 14, 7, true, 1, 1>(lds, w * 32, 0, lane, acc, [](int) {} ST_PASS(l * 5 + 3));
            f32x4 s[2], q[2];
#pragma unroll
            for (int m = 0; m < 2; ++m) {
                const f32x4 r = *(const LAS f32x4*)(R + w * 32 + fq * 8 + m * 4), mr = *(const LAS f32x4*)(M + w * 32 + fq * 8 + m * 4);
                s[m] = (f32x4){0.f, 0.f, 0.f, 0.f}; q[m] = s[m];
#pragma unroll
                for (int n = 7; n < 14; ++n) {
                    const f32x4 z = gelu_tanh(acc[m][n] * r + mr * WS[n * 16 + fr]);
                    acc[m][n] = z; s[m] += z; q[m] += z * z;
                }
            }
            float v8[8], v4[4], v2[2];
#pragma unroll
            for (int i = 0; i < 8; ++i) {
                const float lo = (i & 1) ? q[0][i >> 1] : s[0][i >> 1], hi = (i & 1) ? q[1][i >> 1] : s[1][i >> 1];
                v8[i] = ((fr & 8) ? hi : lo) + dpp_mov<0x128>((fr & 8) ? lo : hi);
            }
#pragma unroll
            for (int i = 0; i < 4; ++i) v4[i] = ((fr & 4) ? v8[i + 4] : v8[i]) + dpp_mov<0x141>(dpp_mov<0x1b>((fr & 4) ? v8[i] : v8[i + 4]));
#pragma unroll
            for (int i = 0; i < 2; ++i) v2[i] = ((fr & 2) ? v4[i + 2] : v4[i]) + dpp_mov<0x4e>((fr & 2) ? v4[i] : v4[i + 2]);
            const float pv = ((fr & 1) ? v2[1] : v2[0]) + dpp_mov<0xb1>((fr & 1) ? v2[0] : v2[1]);
            const __amdgpu_buffer_rsrc_t rX = rsrc(ws + W_XCH + l * 262144 + (size_t)(tm * 4 + w) * 4096, 4096);
            __builtin_amdgcn_raw_buffer_store_b32(xch_word(pv), rX, tn * 256 + lane * 4, 0, 16);
            SB0();
            STAMP(ston, l * 5 + 3, 5);
            {
                const float r0 = R[w * 32 + perm32(fr)], r1 = R[w * 32 + perm32(16 + fr)], m0 = M[w * 32 + perm32(fr)], m1 = M[w * 32 + perm32(16 + fr)];
#pragma unroll
                for (int n = 0; n < 7; ++n) {
                    const f32x4 wv = *(const LAS f32x4*)(WS + n * 16 + fq * 4);
                    zz[n] = pk8(gelu_tanh(acc[0][n] * r0 + wv * m0), gelu_tanh(acc[1][n] * r1 + wv * m1));
                    asm volatile("" : "+v"(zz[n]));
                }
            }
            SB0();
            STAMP(ston, l * 5 + 3, 6);
            f32x4 t;
            for (int spin = 0;; ++spin) {
                u32x4 g[4]; bool ok = true; t = (f32x4){0.f, 0.f, 0.f, 0.f};
#pragma unroll
                for (int i = 0; i < 4; ++i) g[i] = __builtin_amdgcn_raw_buffer_load_b128(rX, lane * 16 + i * 1024, 0, 16);
#pragma unroll
                for (int i = 0; i < 4; ++i) {
                    ok &= (g[i][0] != XCH_EMPTY) & (g[i][1] != XCH_EMPTY) & (g[i][2] != XCH_EMPTY) & (g[i][3] != XCH_EMPTY);
                    t += __builtin_bit_cast(f32x4, g[i]);
                }
                if (__all(ok) || spin > SPIN_MAX) break;
                __builtin_amdgcn_s_sleep(4);
            }
            STAMP(ston, l * 5 + 3, 7);
            const float mean0 = fq_sum(t[0]) * (1.0f / FH), mean1 = fq_sum(t[2]) * (1.0f / FH);
            const float r0 = __builtin_amdgcn_rsqf(fmaxf(fq_sum(t[1]) * (1.0f / FH) - mean0 * mean0, 0.f) + LN_EPS);
            const float r1 = __builtin_amdgcn_rsqf(fmaxf(fq_sum(t[3]) * (1.0f / FH) - mean1 * mean1, 0.f) + LN_EPS);
            LAS float* Z = (LAS float*)(lds + L_Z) + w * 64;
            if (lane < 16) { *(LAS f32x2*)(Z + lane * 2) = (f32x2){r0, r1}; *(LAS f32x2*)(Z + 32 + lane * 2) = (f32x2){-mean0 * r0, -mean1 * r1}; }
            WAITCNT(63, 0);
            const f32x4 ra = *(const LAS f32x4*)(Z + fq * 8), rb = *(const LAS f32x4*)(Z + fq * 8 + 4), ma = *(const LAS f32x4*)(Z + 32 + fq * 8), mb = *(const LAS f32x4*)(Z + 32 + fq * 8 + 4);
            const __amdgpu_buffer_rsrc_t rC = rsrc((u16*)(ws + W_Z2T + l * 8 * MB) + (size_t)tn * 112 * S_ + tm * 128 + w * 32, 112 * S_ * 2 - tm * 256 - w * 64);
#pragma unroll
            for (int n = 0; n < 7; ++n)
                __builtin_amdgcn_raw_buffer_store_b128(pk8(acc[0][7 + n] * ra + ma, acc[1][7 + n] * rb + mb), rC, (n * 16 + fr) * S_ * 2 + fq * 16, 0, STORE_AUX);
            dep_signal(c + 2 * 8192, (lane * 16 + tn) * 32 + tm, 16, lds, lane);
            STAMP(true, l * 5 + 3, 8 + w);
        }
        {
            PHASE_VARS();
            float b[2];
#pragma unroll
            for (int m = 0; m < 2; ++m) b[m] = bf_lo(bsp[tm * 128 + w * 32 + perm32(m * 16 + fr)]);
            f32x4 acc[2][7];
            compute_phase<CG4, 2, 7, 7, false, 4, 5>(lds, w * 32, 0, lane, acc, [](int) {} ST_PASS(l * 5 + 4));
            const __amdgpu_buffer_rsrc_t rC = rsrc((u16*)(ws + W_H + l * 8 * MB) + (size_t)(tm * 128 + w * 32) * FH, 32 * FH * 2);
#pragma unroll
            for (int m = 0; m < 2; ++m) {
                const int ro = perm32(m * 16 + fr) * FH * 2;
#pragma unroll
                for (int q = 0; q < 3; ++q)
                    __builtin_amdgcn_raw_buffer_store_b128(pk8(unpk4(zz[2 * q][m * 2], zz[2 * q][m * 2 + 1]) * (acc[m][2 * q] + b[m]), unpk4(zz[2 * q + 1][m * 2], zz[2 * q + 1][m * 2 + 1]) * (acc[m][2 * q + 1] + b[m])),
                                                           rC, ro + q * 64 + fq * 16, tn * 192, STORE_AUX);
                const f32x4 v = unpk4(zz[6][m * 2], zz[6][m * 2 + 1]) * (acc[m][6] + b[m]);
                __builtin_amdgcn_raw_buffer_store_b64((u32x2){pk_bf16(v[0], v[1]), pk_bf16(v[2], v[3])}, rC, ro + 3072 + fq * 8, tn * 32, STORE_AUX);
            }
            dep_signal(c + 3 * 8192, (tm * 16 + lane) * 32 + tn, 16, lds, lane);
            STAMP(true, l * 5 + 4, 8 + w);
        }
        {
            PHASE_VARS();
            res_phase<CG5>(lds, xa, l == 0 ? (u16*)(ws + W_XB) : out, sp + 131072, tm, tn, w, lane, [](int) {} ST_PASS(l * 5 + 5));
            dep_signal(c + 4 * 8192, (lane * 16 + tm) * 32 + tn, 16, lds, lane);
            STAMP(true, l * 5 + 5, 8 + w);
        }
    }
    asm volatile(".Lk_main_e1:");
}

extern "C" void kernel_launch(void* const* d_in, const int* in_sizes, int n_in, void* d_out, int out_size, void* d_ws, size_t ws_size, hipStream_t stream) {
    const u16* x0 = (const u16*)d_in[0];
    const u16* wsre = (const u16*)d_in[1];
    const u16* wsim = (const u16*)d_in[2];
    const u16* wdre = (const u16*)d_in[3];
    const u16* wdim = (const u16*)d_in[4];
    const u16* win = (const u16*)d_in[6];
    const u16* wsp = (const u16*)d_in[7];
    const u16* bsp = (const u16*)d_in[8];
    const u16* wout = (const u16*)d_in[9];
    char* ws = (char*)d_ws;
    (void)hipFuncSetAttribute((const void*)k_main, hipFuncAttributeMaxDynamicSharedMemorySize, LDS_TOTAL);
    k_main<<<256, 512, LDS_TOTAL, stream>>>(x0, wsre, wsim, wdre, wdim, win, wsp, bsp, wout, ws, (u16*)d_out);
}
```

```cpp
#include <hip/hip_runtime.h>
#include <hip/hip_bf16.h>
#include <utility>

typedef unsigned short u16;
typedef unsigned long long u64;
typedef short bf16x8 __attribute__((ext_vector_type(8)));
typedef short s16x4 __attribute__((ext_vector_type(4)));
typedef float f32x2 __attribute__((ext_vector_type(2)));
typedef float f32x4 __attribute__((ext_vector_type(4)));
typedef unsigned u32x2 __attribute__((ext_vector_type(2)));
typedef unsigned u32x4 __attribute__((ext_vector_type(4)));
#define LAS __attribute__((address_space(3)))
#define GAS __attribute__((address_space(1)))
#define DEV __device__ __forceinline__

constexpr int waitimm(int vm, int lgkm) { return (vm & 15) | (7 << 4) | ((lgkm & 15) << 8) | ((vm >> 4) << 14); }
#define WAITCNT(vm, lgkm) __builtin_amdgcn_s_waitcnt(waitimm(vm, lgkm))
#define BAR() do { asm volatile("" ::: "memory"); __builtin_amdgcn_s_barrier(); asm volatile("" ::: "memory"); } while (0)
#define SB0() __builtin_amdgcn_sched_barrier(0)
#define STAMP(on, ph, i)
#define ST_PARAM
#define ST_PASS(p)

constexpr int S_ = 2048, D_ = 1024, F_ = 3584, FH = 1792;
#define WP1 4
#define WP2 4
#define WP3 1
#define WP4 4
#define WP5 4
#define TILE_AUX 2
#define Z1A 3
#define Z1B 4
#define XSL 2
#define WO_AUX 0
#define EX_SLEEP 1
#define POLL_SLEEP 2
constexpr float LN_EPS = 1e-5f;
constexpr int SPIN_MAX = 1 << 17;

typedef __bf16 bf16x2 __attribute__((ext_vector_type(2)));
DEV unsigned pk_bf16(float lo, float hi) { return __builtin_bit_cast(unsigned, __builtin_convertvector((f32x2){lo, hi}, bf16x2)); }
DEV float bf_lo(unsigned v) { return __uint_as_float(v << 16); }
DEV float bf_hi(unsigned v) { return __uint_as_float(v & 0xffff0000u); }
DEV __amdgpu_buffer_rsrc_t rsrc(const void* p, unsigned bytes) { return __builtin_amdgcn_make_buffer_rsrc((void*)p, 0, bytes, 0x00020000); }
DEV void st16(u32x4 v, __amdgpu_buffer_rsrc_t r, int off) { __builtin_amdgcn_raw_buffer_store_b128(v, r, off, 0, 16); }
DEV f32x4 gelu_tanh(f32x4 x) {
    const float c0 = -2.3022081986f, c1 = c0 * 0.044715f;
    const f32x4 a = x * (x * x * c1 + c0);
    f32x4 d = (f32x4){__builtin_amdgcn_exp2f(a[0]), __builtin_amdgcn_exp2f(a[1]), __builtin_amdgcn_exp2f(a[2]), __builtin_amdgcn_exp2f(a[3])} + 1.0f;
    return x * (f32x4){__builtin_amdgcn_rcpf(d[0]), __builtin_amdgcn_rcpf(d[1]), __builtin_amdgcn_rcpf(d[2]), __builtin_amdgcn_rcpf(d[3])};
}
DEV u32x4 pk8(f32x4 a, f32x4 b) { return (u32x4){pk_bf16(a[0], a[1]), pk_bf16(a[2], a[3]), pk_bf16(b[0], b[1]), pk_bf16(b[2], b[3])}; }
DEV f32x4 unpk4(unsigned a, unsigned b) { return (f32x4){bf_lo(a), bf_hi(a), bf_lo(b), bf_hi(b)}; }
__host__ __device__ constexpr int perm32(int slot) { return 8 * ((slot & 15) >> 2) + 4 * ((slot >> 4) & 1) + (slot & 3); }
__host__ __device__ constexpr int inv32(int row) { return 16 * ((row >> 2) & 1) + 4 * ((row >> 3) & 3) + (row & 3); }
DEV float fq_sum(float a) {
    const auto p = __builtin_amdgcn_permlane16_swap(__float_as_uint(a), __float_as_uint(a), false, false);
    a = __uint_as_float(p[0]) + __uint_as_float(p[1]);
    const auto q = __builtin_amdgcn_permlane32_swap(__float_as_uint(a), __float_as_uint(a), false, false);
    return __uint_as_float(q[0]) + __uint_as_float(q[1]);
}
template <int CTRL> DEV float dpp_mov(float a) { return __builtin_bit_cast(float, __builtin_amdgcn_update_dpp(0, __builtin_bit_cast(int, a), CTRL, 0xf, 0xf, true)); }
template <int CTRL> DEV float dpp_add(float a) { return a + __builtin_bit_cast(float, __builtin_amdgcn_update_dpp(0, __builtin_bit_cast(int, a), CTRL, 0xf, 0xf, true)); }
DEV float row16_sum(float a) { a = dpp_add<0x128>(a); a = dpp_add<0x124>(a); a = dpp_add<0x122>(a); return dpp_add<0x121>(a); }
DEV float oct_sum(float a) { a = dpp_add<0xb1>(a); a = dpp_add<0x4e>(a); return dpp_add<0x141>(a); }
DEV float quad_sum(float a) { a = dpp_add<0xb1>(a); return dpp_add<0x4e>(a); }
constexpr unsigned XCH_EMPTY = 0xffffffffu;
DEV unsigned xch_word(float v) { const unsigned u = __float_as_uint(v); return u == XCH_EMPTY ? 0x7fc00000u : u; }

DEV int opaque_v(int v) { asm volatile("" : "+v"(v)); return v; }
DEV int opaque_s(int v) { asm volatile("" : "+s"(v)); return v; }
template <class F, int... I> DEV void static_for_impl(F&& f, std::integer_sequence<int, I...>) { (f(std::integral_constant<int, I>{}), ...); }
template <int N, class F> DEV void static_for(F&& f) { static_for_impl(f, std::make_integer_sequence<int, N>{}); }

template <int BM_, int BN_, int NS_, int NT_, int LDP_> struct Cfg {
    static constexpr int BM = BM_, BN = BN_, NS = NS_, NT = NT_, LDP = LDP_;
    static constexpr bool WISP = LDP_ != 0;
    static constexpr int PB = BM * 128, QB = BN * 128, QBASE = NS * PB, RING = QBASE + NS * QB;
    static constexpr int LP = BM / 32, LQ = BN / 32;
    static constexpr int LW = WISP ? LP : LQ, LA = WISP ? LQ : LP;
};
constexpr int RING_MAX = 135168;
constexpr int L_TIN = 131072, L_TOUT = 135168, L_Z = 151552;
constexpr int L_R = 159744, L_M = L_R + 512, L_WD = L_M + 512, L_WS = L_WD + 512, L_JUNK = L_WS + 1024, L_SYNC = L_JUNK + 256, L_TAG = L_SYNC + 48, LDS_TOTAL = L_SYNC + 64;

template <class C, int WPRE, int N0, class Dep, class Tail>
DEV void loader_phase(LAS unsigned char* lds, const void* P, const void* Q, bool qrow, int psw, int lw, int lane, unsigned drained, Dep&& dep, Tail&& tail ST_PARAM) {
    constexpr int NS = C::NS, NT = C::NT, LA = C::LA, LW = C::LW;
    static_assert(C::RING <= RING_MAX && NS >= 3 && NT >= 2 * NS && C::BM % 32 == 0, "");
    lane = opaque_v(lane); lw = opaque_s(lw);
    const __amdgpu_buffer_rsrc_t rP = rsrc(P, NT * C::PB), rQ = rsrc(Q, NT * C::QB);
    const int vrow = perm32(lw * 8 + (lane >> 3)) * NT * 128 + ((lane & 7) ^ (((lw & 1) * 4 + (lane >> 4)) & 7)) * 16, vfrag = lw * 1024 + lane * 16;
    const int vP = C::WISP ? vrow : vfrag, vQ = qrow ? vrow : vfrag, sq = qrow ? NT * 4096 : 4096, kq = qrow ? 128 : C::QB;
    auto issueP = [&](int slot, int kt) {
        LAS unsigned char* dst = lds + slot * C::PB + lw * 1024;
#pragma unroll
        for (int i = 0; i < C::LP; ++i)
            __builtin_amdgcn_raw_ptr_buffer_load_lds(rP, (LAS void*)(dst + (i ^ psw) * 4096), 16, vP, C::WISP ? i * NT * 4096 + kt * 128 : i * 4096 + kt * C::PB, 0, C::WISP ? 0 : 16);
    };
    auto issueQ = [&](int slot, int kt) {
        LAS unsigned char* dst = lds + C::QBASE + slot * C::QB + lw * 1024;
#pragma unroll
        for (int j = 0; j < (C::BN + 31) / 32; ++j)
            if (j < C::LQ || j * 4 + lw < C::BN / 8) __builtin_amdgcn_raw_ptr_buffer_load_lds(rQ, (LAS void*)(dst + j * 4096), 16, vQ, j * sq + kt * kq, 0, 16);
    };
    auto issueW = [&](int slot, int kt) { if (C::WISP) issueP(slot, kt); else issueQ(slot, kt); };
    auto issueA = [&](int slot, int kt) { if (C::WISP) issueQ(slot, kt); else issueP(slot, kt); };
    if (drained) {
        while ((unsigned)__builtin_amdgcn_readfirstlane(*(volatile LAS unsigned*)(lds + L_SYNC + 16)) < drained) __builtin_amdgcn_s_sleep(2);
#pragma unroll 1
        for (int s = 0; s < WPRE; ++s) issueW(s, s);
        dep();
        STAMP(ston, ph, 4);
        issueA(0, 0);
        if constexpr (WPRE < 2) issueW(1, 1);
        issueA(1, 1);
        WAITCNT(LA + (WPRE < 2 ? LW : 0), 15);
        BAR();
#pragma unroll
        for (int s = 2; s < NS; ++s) { if (s >= WPRE) issueW(s, s); issueA(s, s); }
    } else {
        issueW(0, 0); issueA(0, 0); issueW(1, 1); issueA(1, 1);
        STAMP(ston, ph, 4);
        WAITCNT(LA + LW, 15);
        BAR();
#pragma unroll 1
        for (int s = 2; s < NS; ++s) { issueW(s, s); issueA(s, s); }
    }
    static_for<NS - 2>([&](auto ic) {
        constexpr int t = decltype(ic)::value, late = NS - (WPRE > t + 2 ? WPRE : t + 2);
        WAITCNT((NS - 2 - t) * LA + late * LW + t * (LA + LW), 15);
        BAR();
        issueW(t, t + NS); issueA(t, t + NS);
    });
    int sl = NS - 2;
#pragma unroll 1
    for (int t = NS - 2; t < NT - NS; ++t) {
        WAITCNT((NS - 2) * (LA + LW), 15);
        BAR();
        issueW(sl, t + NS); issueA(sl, t + NS);
        sl = (sl + 1 == NS) ? 0 : sl + 1;
    }
    tail(std::integral_constant<int, 0>{});
    static_for<NS - 1>([&](auto ic) {
        constexpr int i = decltype(ic)::value;
        WAITCNT((NS - 2 - i) * (LA + LW) + N0, 15);
        BAR();
    });
    tail(std::integral_constant<int, 1>{});
    WAITCNT(63, 0);
    BAR();
}

template <class C, int MT, int NTL, int NSWAP, bool ROLL, int RNUM, int RDEN, class Hook>
DEV void compute_phase(LAS unsigned char* lds, int prow0, int qrow0, bool qrow, int lane, f32x4 (&acc)[MT][NTL], Hook&& hook ST_PARAM) {
    constexpr int NR = MT + NTL, NM = MT * NTL, NT = C::NT;
    const int fr = lane & 15, fq = lane >> 4;
    int aoff[2], boff[2];
#pragma unroll
    for (int k = 0; k < 2; ++k) {
        const int orow = fr * 128 + ((k * 4 + fq) ^ ((fr >> 1) & 7)) * 16, ofrag = k * 1024 + lane * 16;
        aoff[k] = prow0 * 128 + (C::WISP ? orow : ofrag);
        boff[k] = C::QBASE + qrow0 * 128 + (qrow ? orow : ofrag);
    }
#pragma unroll
    for (int m = 0; m < MT; ++m)
#pragma unroll
        for (int n = 0; n < NTL; ++n) { acc[m][n] = (f32x4){0.f, 0.f, 0.f, 0.f}; asm volatile("" : "+v"(acc[m][n])); }
    auto mfma = [&](int m, int n, const bf16x8& a, const bf16x8& b) {
        if (n >= NSWAP) asm volatile("v_mfma_f32_16x16x32_bf16 %0, %1, %2, %0" : "+v"(acc[m][n]) : "v"(a), "v"(b));
        else asm volatile("v_mfma_f32_16x16x32_bf16 %0, %1, %2, %0" : "+v"(acc[m][n]) : "v"(b), "v"(a));
    };
    int sp = 0, sq = 0;
    auto next = [&]() { sp = (sp + C::PB == C::NS * C::PB) ? 0 : sp + C::PB; sq = (sq + C::QB == C::NS * C::QB) ? 0 : sq + C::QB; };
    BAR();
    STAMP(ston, ph, 1);
    if constexpr (!ROLL) {
        bf16x8 fa[4][MT], fb[4][NTL];
        auto rd1 = [&](int set, int k, int i) {
            if (i == 0) fa[set][0] = *(const LAS bf16x8*)(lds + sp + aoff[k]);
            else if (i <= NTL) fb[set][i - 1] = *(const LAS bf16x8*)(lds + sq + boff[k] + (i - 1) * 2048);
            else fa[set][i - NTL] = *(const LAS bf16x8*)(lds + sp + aoff[k] + (i - NTL) * 2048);
        };
        auto stg = [&](int set, bool rd) {
#pragma unroll
            for (int i = 0; i < 2 * NM; ++i) {
                const int st = set + i / NM, m = (i % NM) / NTL, n = i % NTL;
                mfma(m, n, fa[st][m], fb[st][n]); SB0();
                if (rd) {
#pragma unroll
                    for (int r = (i * RNUM + RDEN - 1) / RDEN; r < ((i + 1) * RNUM + RDEN - 1) / RDEN; ++r) if (r < 2 * NR) rd1((set ^ 2) + r / NR, r / NR, r % NR);
                    SB0();
                }
            }
        };
        static_assert((2 * NM * RNUM + RDEN - 1) / RDEN >= 2 * NR && NT % 2 == 0, "");
#pragma unroll
        for (int r = 0; r < 2 * NR; ++r) rd1(r / NR, r / NR, r % NR);
#pragma unroll 1
        for (int t = 0; t < NT - 2; t += 2) {
            next(); WAITCNT(63, 0); BAR(); SB0();
            stg(0, true);
            next(); WAITCNT(63, 0); BAR(); SB0();
            stg(2, true);
            hook(t); SB0();
        }
        hook(-1); SB0();
        next();
        stg(0, false);
        BAR(); SB0();
#pragma unroll
        for (int r = 0; r < 2 * NR; ++r) rd1(2 + r / NR, r / NR, r % NR);
        WAITCNT(63, 0); BAR(); SB0();
        stg(2, false);
    } else {
        static_assert(MT == 2, "");
        constexpr int G = 8;
        bf16x8 fa[2][2], fb[NTL];
        auto rda = [&](int set, int k, int m) { fa[set][m] = *(const LAS bf16x8*)(lds + sp + aoff[k] + m * 2048); };
        auto rdb = [&](int k, int n) { fb[n] = *(const LAS bf16x8*)(lds + sq + boff[k] + n * 2048); };
        auto half0 = [&]() {
#pragma unroll
            for (int j = 0; j < NM; ++j) {
                mfma(j & 1, j >> 1, fa[0][j & 1], fb[j >> 1]); SB0();
                if (j & 1) rdb(1, j >> 1); else if (j < 4) rda(1, 1, j >> 1);
                SB0();
            }
        };
        auto half1 = [&](bool rd) {
#pragma unroll
            for (int j = 0; j < NM; ++j) {
                if (j == G) { next(); WAITCNT(63, 0); BAR(); SB0(); }
                mfma(j & 1, j >> 1, fa[1][j & 1], fb[j >> 1]); SB0();
                if (rd && j >= G) {
                    if (j < G + 2) rda(0, 0, j - G);
#pragma unroll
                    for (int n = 0; n < NTL; ++n) if ((G + 2 + n > 2 * n + 1 ? G + 2 + n : 2 * n + 1) == j) rdb(0, n);
                    SB0();
                }
            }
        };
        rda(0, 0, 0); rda(0, 0, 1);
#pragma unroll
        for (int n = 0; n < NTL; ++n) rdb(0, n);
        SB0();
#pragma unroll 1
        for (int t = 0; t < NT - 1; ++t) { half0(); half1(true); }
        half0(); half1(false);
    }
    asm volatile("s_nop 15" ::: "memory");
#pragma unroll
    for (int m = 0; m < MT; ++m)
#pragma unroll
        for (int n = 0; n < NTL; ++n) asm volatile("" : "+v"(acc[m][n]));
    STAMP(ston, ph, 2);
}

DEV u64 launch_tag(LAS unsigned char* lds) { return *(volatile LAS u64*)(lds + L_TAG); }
DEV void box_poll(const u64* box, int nprod, u64 tag, int lane) {
#pragma unroll 1
    for (int spin = 0; spin < SPIN_MAX; ++spin) {
        const u64 v = __hip_atomic_load((const GAS u64*)box + min(lane, nprod - 1), __ATOMIC_RELAXED, __HIP_MEMORY_SCOPE_AGENT);
        if (__all(v == tag)) break;
        __builtin_amdgcn_s_sleep(POLL_SLEEP);
    }
}
DEV void dep_wait(const u64* box, int nprod, LAS unsigned char* lds, unsigned epoch, int lw, int lane) {
    volatile LAS unsigned* flag = (volatile LAS unsigned*)(lds + L_SYNC);
    if (lw == 0) {
        box_poll(box, nprod, launch_tag(lds), lane);
        *flag = epoch;
    } else {
        while ((unsigned)__builtin_amdgcn_readfirstlane(*flag) < epoch) __builtin_amdgcn_s_sleep(1);
    }
    asm volatile("" ::: "memory");
}
template <int CNT = 16> DEV void dep_signal(u64* boxes, int word, int ncons, LAS unsigned char* lds, int lane) {
    const u64 tag = launch_tag(lds);
    asm volatile("s_waitcnt vmcnt(0)" ::: "memory");
    unsigned old = 0;
    if (lane == 0) old = __hip_atomic_fetch_add((LAS unsigned*)(lds + L_SYNC + CNT), 1u, __ATOMIC_RELAXED, __HIP_MEMORY_SCOPE_WORKGROUP);
    if ((__builtin_amdgcn_readfirstlane(old) & 3) == 3 && lane < ncons) __hip_atomic_store((GAS u64*)boxes + word, tag, __ATOMIC_RELAXED, __HIP_MEMORY_SCOPE_AGENT);
}
DEV void touch(LAS unsigned char* lds, const u16* p, unsigned bytes, int off) {
    __builtin_amdgcn_raw_ptr_buffer_load_lds(rsrc(p, bytes), (LAS void*)(lds + L_JUNK), 4, off, 0, 0, 0);
}

template <int STRIDE> DEV u32x4 read_tr(LAS unsigned char* p) {
    const u32x2 a = __builtin_bit_cast(u32x2, __builtin_amdgcn_ds_read_tr16_b64_v4i16((LAS s16x4*)p)), b = __builtin_bit_cast(u32x2, __builtin_amdgcn_ds_read_tr16_b64_v4i16((LAS s16x4*)(p + 4 * STRIDE)));
    return (u32x4){a[0], a[1], b[0], b[1]};
}
DEV void block_sums_load(const void* p, unsigned bytes, int lw, int lane, f32x4 (&v)[4]) {
    const __amdgpu_buffer_rsrc_t r = rsrc(p, bytes);
#pragma unroll
    for (int i = 0; i < 4; ++i) v[i] = __builtin_bit_cast(f32x4, __builtin_amdgcn_raw_buffer_load_b128(r, (lane >> 4) * 1024 + (lane & 15) * 16, i * 4096 + lw * 256, 16));
}
DEV f32x4 block_sums(const f32x4 (&v)[4]) { const f32x4 s = (v[0] + v[1]) + (v[2] + v[3]); return (f32x4){fq_sum(s[0]), fq_sum(s[1]), fq_sum(s[2]), fq_sum(s[3])}; }
DEV void row_tables(LAS unsigned char* lds, int lw, int lane, const f32x4 (&v)[4], float sign) {
    const f32x4 t = block_sums(v);
    const float s = (lane & 16) ? t[2] : t[0], q = (lane & 16) ? t[3] : t[1];
    const float mean = s * (1.0f / D_), var = fmaxf(q * (1.0f / D_) - mean * mean, 0.f), r = sign * __builtin_amdgcn_rsqf(var + LN_EPS);
    ((LAS float*)(lds + (lane < 32 ? L_R : L_M)))[lw * 32 + perm32(lane & 31)] = lane < 32 ? r : -mean * r;
}

constexpr size_t MB = 1 << 20;
constexpr size_t LS = 30 * MB;
constexpr size_t W_T = 0, W_XA = 8 * MB, W_Z2T = 12 * MB, W_H = 19 * MB, W_EX = 26 * MB, W_XB = 60 * MB, W_WINT = 64 * MB, W_WOUTT = 71 * MB;
constexpr size_t W_SP = 75 * MB, W_GRAN = 77 * MB, W_WSPIN = 77 * MB + 65536, W_XCH = 77 * MB + 512 * 1024, W_CTR = 78 * MB, W_NONCE = 78 * MB + 768 * 1024;

typedef Cfg<128, 128, 4, D_ / 64, D_> CG1;
typedef Cfg<128, 128, 4, S_ / 64, S_> CG2;
typedef Cfg<128, 224, 3, D_ / 64, 0> CG3;
typedef Cfg<128, 112, 4, S_ / 64, S_> CG4;
typedef Cfg<128, 64, 4, FH / 64, 0> CG5;
constexpr int XP = 128 * D_ * 2;

struct Piece { __amdgpu_buffer_rsrc_t r; int v, s0, sm, sh; };
DEV Piece piece(const void* panel, bool row, int r0, int cb, int lane) {
    return Piece{rsrc(panel, XP), row ? perm32(lane & 15) * D_ * 2 + (lane >> 4) * 16 : lane * 16, row ? r0 * D_ * 2 + cb * 128 : cb * 16384 + r0 * 128, row ? 8 * D_ : 2048, row ? 64 : 1024};
}
DEV void res_load(const Piece& p, u32x4 (&xr)[4]) {
#pragma unroll
    for (int i = 0; i < 4; ++i) xr[i] = __builtin_amdgcn_raw_buffer_load_b128(p.r, p.v, p.s0 + (i >> 1) * p.sm + (i & 1) * p.sh, 16);
}
DEV void res_store(f32x4 (*acc)[4], const u32x4 (&xr)[4], const Piece& p, float* sp, int lane) {
    u32x4 sums;
#pragma unroll
    for (int m = 0; m < 2; ++m) {
        f32x4 s = (f32x4){0.f, 0.f, 0.f, 0.f}, qq = s;
#pragma unroll
        for (int h = 0; h < 2; ++h) {
            const u32x4 x = xr[m * 2 + h], o = pk8(acc[m][2 * h] + unpk4(x[0], x[1]), acc[m][2 * h + 1] + unpk4(x[2], x[3]));
            st16(o, p.r, p.v + (p.s0 + m * p.sm + h * p.sh));
            const f32x4 a = unpk4(o[0], o[1]), b = unpk4(o[2], o[3]);
            s += a + b; qq += a * a + b * b;
        }
        sums[2 * m] = __float_as_uint(fq_sum((s[0] + s[1]) + (s[2] + s[3]))); sums[2 * m + 1] = __float_as_uint(fq_sum((qq[0] + qq[1]) + (qq[2] + qq[3])));
    }
    if (lane < 16) st16(sums, rsrc(sp, 256), lane * 16);
}

DEV void ex_clear(char* p, int lane) {
#pragma unroll
    for (int i = 0; i < 4; ++i) st16((u32x4){XCH_EMPTY, XCH_EMPTY, XCH_EMPTY, XCH_EMPTY}, rsrc(p, 4096), lane * 16 + i * 1024);
}

struct Args { const u16 *x0, *wsre, *wsim, *wdre, *wdim, *win, *wsp, *bsp, *wout; char* ws; u16* out; };
typedef const __attribute__((address_space(4))) Args* ArgPtr;
DEV ArgPtr opaque_a(ArgPtr p) { asm volatile("" : "+s"(p)); return p; }

__global__ __launch_bounds__(512, 2) void k_main(Args) {
    extern __shared__ __attribute__((aligned(1024))) unsigned char shm[];
    LAS unsigned char* lds = (LAS unsigned char*)shm;
    const int tid = threadIdx.x, lane0 = tid & 63, w0 = __builtin_amdgcn_readfirstlane(tid >> 6);
    ArgPtr ka = (ArgPtr)__builtin_amdgcn_kernarg_segment_ptr();
    {
        u64 pc; unsigned len, k0, k1, off = tid * 128;
        asm volatile("s_getpc_b64 %0\n.Lk_main_pc:\n\ts_load_dword %2, %5, 0x0\n\ts_load_dword %3, %5, 0x54\n\t"
                     "s_max_i32 %1, .Lk_main_e0-.Lk_main_pc, 0\n\ts_max_i32 %1, .Lk_main_e1-.Lk_main_pc, %1\n\ts_mov_b32 m0, %6\n\tv_min_u32 %4, %1, %4\n\t"
                     "global_load_lds_dword %4, %0\n\ts_waitcnt lgkmcnt(0)"
                     : "=&s"(pc), "=&s"(len), "=&s"(k0), "=&s"(k1), "+v"(off), "+s"(ka) : "s"((unsigned)(size_t)(lds + L_JUNK)) : "scc", "memory");
        SB0();
    }
    const int tm0 = ((blockIdx.x & 7) >> 1) * 4 + (blockIdx.x >> 6), tn0 = (blockIdx.x & 1) * 8 + ((blockIdx.x >> 3) & 7);
#define PHASE_VARS() const int lane = opaque_v(lane0), w = opaque_s(w0), tm = opaque_s(tm0), tn = opaque_s(tn0), fr = lane & 15, fq = lane >> 4; (void)fr; (void)fq; (void)w; \
    const ArgPtr a = opaque_a(ka); char* const ws = a->ws; char* const wl = ws + l * LS; u64* const ctr = (u64*)(ws + W_CTR); u64* const c = ctr + l * 5 * 8192; float* const sp = (float*)(ws + W_SP + l * 512 * 1024); \
    (void)wl; (void)c; (void)sp
    if (w0 >= 4) {
        const int lw = w0 - 4;
        auto none = [&](auto) {};
#pragma unroll 1
        for (int l = 0; l < 2; ++l) {
            const unsigned first = l == 0 ? ~0u : 0u;
            f32x4 v[4], u[4];
            { PHASE_VARS(); const int lt = (w - 4) * 64 + lane;
            const u16* pw = (tm < 8 ? a->wdre : a->wdim) + (size_t)(tm & 7) * 128 * D_;
            const __amdgpu_buffer_rsrc_t rg = rsrc(ws + W_GRAN, 65536 & first);
            const int goff = (lt < 128 ? tn * 128 + lt : 1920 + tm * 128 + lt) * 16;
            u32x4 g;
            loader_phase<CG1, WP1, 6>(lds, pw, (l == 0 ? (const char*)a->x0 : ws + W_XB) + tn * XP, l == 0, 0, lw, lane, l * 20, [&]() { dep_wait(ctr + 4 * 8192 + (tm * 16 + tn) * 32, 16, lds, 5, lw, lane); }, [&](auto ic) {
                constexpr int i = decltype(ic)::value;
                if constexpr (i == 0) {
                    block_sums_load(sp + tn * 4096, 16384 & ~first, lw, lane, v);
                    g = __builtin_amdgcn_raw_buffer_load_b128(rg, goff, 0, 16);
                    touch(lds, (tn < 8 ? a->wsre : a->wsim) + (size_t)(tm * 128 + (tn & 7) * 16 + lw * 4) * S_, 16384 & first, fq * 4096 + 2048 + fr * 128);
                } else {
                    const float sign = tm < 8 ? 1.0f : -1.0f;
                    if (l) { row_tables(lds, lw, lane, v, sign); return; }
                    const u64 tag = launch_tag(lds);
                    if (!__all(g[1] == (unsigned)tag && g[3] == (unsigned)(tag >> 32))) {
                        asm volatile("" ::: "memory");
#pragma unroll 1
                        for (int spin = 0; spin < SPIN_MAX; ++spin) {
                            g = __builtin_amdgcn_raw_buffer_load_b128(rg, goff, 0, 16);
                            if (__all(g[1] == (unsigned)tag && g[3] == (unsigned)(tag >> 32))) break;
                            __builtin_amdgcn_s_sleep(POLL_SLEEP);
                        }
                    }
                    const float mean = __uint_as_float(g[0]) * (1.0f / D_), var = fmaxf(__uint_as_float(g[2]) * (1.0f / D_) - mean * mean, 0.f), rs = sign * __builtin_amdgcn_rsqf(var + LN_EPS);
                    if (lt < 128) { ((LAS float*)(lds + L_R))[lt] = rs; ((LAS float*)(lds + L_M))[lt] = -mean * rs; }
                    else ((LAS float*)(lds + L_WD))[lt - 128] = __uint_as_float(g[0]);
                }
            } ST_PASS(l * 5 + 1)); }
            { PHASE_VARS();
            u64 tw;
            const u64* tbox = ctr + 8192 + (tm * 16 + tn) * 32 + 16;
            loader_phase<CG2, WP2, 1>(lds, (tn < 8 ? a->wsre : a->wsim) + (size_t)tm * 128 * S_, wl + W_T + tn * 2 * XP, false, tn >> 3, lw, lane, l * 20 + 4,
                              [&]() { dep_wait(c + (tm * 16 + tn) * 32, 17, lds, l * 5 + 1, lw, lane); },
                              [&](auto ic) {
                                  constexpr int i = decltype(ic)::value;
                                  if constexpr (i == 0) {
                                      tw = __hip_atomic_load((const GAS u64*)tbox + (lane & 15), __ATOMIC_RELAXED, __HIP_MEMORY_SCOPE_AGENT);
                                  } else { const u64 tag = launch_tag(lds); if (!__all(tw == tag)) box_poll(tbox, 16, tag, lane); }
                              } ST_PASS(l * 5 + 2)); }
            { PHASE_VARS(); const int lt = (w - 4) * 64 + lane;
            loader_phase<CG3, WP3, 13>(lds, wl + W_XA + tm * XP, ws + W_WINT + tn * 224 * D_ * 2, false, 0, lw, lane, l * 20 + 8,
                              [&]() { dep_wait(c + 8192 + (tm * 16 + tn) * 32, 16, lds, l * 5 + 2, lw, lane);
                                      if (blockIdx.x == 0 && l == 0 && lt == 0) __hip_atomic_store((GAS u64*)(ws + W_NONCE), launch_tag(lds), __ATOMIC_RELAXED, __HIP_MEMORY_SCOPE_AGENT); },
                              [&](auto ic) {
                                  constexpr int i = decltype(ic)::value;
                                  if constexpr (i == 0) {
                                      block_sums_load(sp + 65536 + tm * 4096, 16384, lw, lane, v);
                                      block_sums_load(ws + W_WSPIN + (size_t)tn * 16384, 16384 & first, lw, lane, u);
                                      const __amdgpu_buffer_rsrc_t r = rsrc(a->wout + (size_t)tm * 112 * D_, (112 * D_ * 2) & first);
#pragma unroll
                                      for (int j = 0; j < 4; ++j)
                                          __builtin_amdgcn_raw_ptr_buffer_load_lds(r, (LAS void*)(lds + L_TOUT + lw * 4096 + j * 1024), 16, (lane >> 1) * D_ * 2 + (lane & 1) * 16, j * 32 * D_ * 2 + tn * 128 + lw * 32, 0, WO_AUX);
                                      touch(lds, a->wsp + (size_t)(tm * 128 + tn * 8 + lw * 2) * S_, 8192 & first, lane * 128);
                                  } else {
                                      row_tables(lds, lw, lane, v, 1.0f);
                                      const f32x4 s = block_sums(u);
                                      if (l == 0 && lane < 16) *(LAS f32x4*)(lds + L_WS + lw * 256 + lane * 16) = s;
                                  }
                              } ST_PASS(l * 5 + 3));
            if (l == 0) {
                WAITCNT(1, 15);
                STAMP(ston, 4, 5);
                asm volatile("" ::: "memory");
                const __amdgpu_buffer_rsrc_t ro = rsrc(ws + W_WOUTT + tn * 64 * FH * 2, 64 * FH * 2);
                const int slot = (lw >> 1) * 32 + inv32((lw & 1) * 16 + fr), vo = (slot >> 4) * 2048 + fq * 256 + (slot & 15) * 16;
#pragma unroll
                for (int j = 0; j < 4; ++j) {
                    u32x2 x, y;
                    asm volatile("ds_read_b64_tr_b16 %0, %2\n\tds_read_b64_tr_b16 %1, %2 offset:128\n\ts_waitcnt lgkmcnt(0)"
                                 : "=&v"(x), "=&v"(y) : "v"((unsigned)(size_t)(lds + L_TOUT) + lw * 4096 + j * 1024 + fq * 256 + fr * 8) : "memory");
                    const int un = j < 3 ? tm * 3 + j : 48 + (tm >> 1);
                    if (j < 3 || fq < 2) st16((u32x4){x[0], x[1], y[0], y[1]}, ro, vo + ((un >> 1) * 8192 + (un & 1) * 1024 + (j < 3 ? 0 : (tm & 1) * 512)));
                }
                ex_clear(ws + LS + W_EX + ((tm * 16 + tn) * 4 + lw) * 4096, lane);
                STAMP(ston, 4, 6);
                dep_signal<32>(ctr + 3 * 8192, (lane * 16 + tn) * 32 + 16 + tm, 16, lds, lane);
                STAMP(ston, 4, 7);
            } }
            { PHASE_VARS();
            u64 tw;
            const u64* tbox = ctr + 3 * 8192 + (tm * 16 + tn) * 32 + 16;
            loader_phase<CG4, WP4, 1>(lds, a->wsp + (size_t)tm * 128 * S_, wl + W_Z2T + tn * 112 * S_ * 2, false, 0, lw, lane, l * 20 + 12, [&]() { dep_wait(c + 2 * 8192 + (tm * 16 + tn) * 32, 16, lds, l * 5 + 3, lw, lane); },
                              [&](auto ic) {
                                  if constexpr (decltype(ic)::value == 0) tw = __hip_atomic_load((const GAS u64*)tbox + (lane & 15), __ATOMIC_RELAXED, __HIP_MEMORY_SCOPE_AGENT);
                                  else { const u64 tag = launch_tag(lds); if (!__all(tw == tag)) box_poll(tbox, 16, tag, lane); }
                              } ST_PASS(l * 5 + 4)); }
            { PHASE_VARS();
            loader_phase<CG5, WP5, 0>(lds, wl + W_H + tm * 128 * FH * 2, ws + W_WOUTT + tn * 64 * FH * 2, false, 0, lw, lane, l * 20 + 16,
                              [&]() { dep_wait(c + 3 * 8192 + (tm * 16 + tn) * 32, 16, lds, l * 5 + 4, lw, lane); }, none ST_PASS(l * 5 + 5)); }
        }
        asm volatile(".Lk_main_e0:");
        return;
    }
    {
        const int l = 0;
        PHASE_VARS();
        u64 z = __hip_atomic_load((const GAS u64*)(ws + W_NONCE), __ATOMIC_RELAXED, __HIP_MEMORY_SCOPE_AGENT);
        ex_clear(ws + W_EX + ((tm * 16 + tn) * 4 + w) * 4096, lane);
        SB0();
        const int xr = tn * 128 + tm * 8 + w * 2, wr = (tm & 7) * 128 + tn * 8 + w * 2;
        const __amdgpu_buffer_rsrc_t rx = rsrc(a->x0 + (size_t)xr * D_, 4096), rw = rsrc((tm < 8 ? a->wdre : a->wdim) + (size_t)wr * D_, 4096);
        u32x4 xa[4], b[4];
#pragma unroll
        for (int i = 0; i < 4; ++i) { xa[i] = __builtin_amdgcn_raw_buffer_load_b128(rx, i * 1024 + lane * 16, 0, 0); b[i] = __builtin_amdgcn_raw_buffer_load_b128(rw, i * 1024 + lane * 16, 0, 0); }
        {
            const __amdgpu_buffer_rsrc_t r = rsrc(a->win + (size_t)tm * 64 * F_, 64 * F_ * 2);
#pragma unroll
            for (int j = 0; j < 7; ++j) {
                const int q = j * 4 + w, cb = q * 64 + lane, row = cb / 28, ch = cb - row * 28;
                __builtin_amdgcn_raw_ptr_buffer_load_lds(r, (LAS void*)(lds + L_TIN + q * 1024), 16, row * F_ * 2 + (ch < 14 ? ch * 16 : FH * 2 - 224 + ch * 16), tn * 224, 0, TILE_AUX);
            }
        }
        touch(lds, (tn < 8 ? a->wsre : a->wsim) + (size_t)(tm * 128 + (tn & 7) * 16 + w * 4) * S_, 16384, fq * 4096 + fr * 128);
        if (w == 0) __builtin_amdgcn_raw_ptr_buffer_load_lds(rsrc(ctr + (tm * 16 + tn) * 32, 9 * 65536), (LAS void*)(lds + L_JUNK), 4, (lane >> 1) * 65536 + (lane & 1) * 128, 0, 0, 16);
        STAMP(ston, 1, 5);
        if (tid < 128) st16((u32x4){XCH_EMPTY, XCH_EMPTY, XCH_EMPTY, XCH_EMPTY}, rsrc(ws + W_XCH + blockIdx.x * 2048, 2048), tid * 16);
        if (tid < 12) *(LAS unsigned*)(lds + L_SYNC + tid * 4) = 0u;
        z += 0x9E3779B97F4A7C15ull; z = (z ^ (z >> 30)) * 0xBF58476D1CE4E5B9ull; z = (z ^ (z >> 27)) * 0x94D049BB133111EBull; z ^= z >> 31;
        if (tid == 0) *(LAS u64*)(lds + L_TAG) = z;
        STAMP(ston, 1, 6);
        float g0 = 0.f, g1 = 0.f;
#pragma unroll
        for (int r = 0; r < 2; ++r) {
            f32x4 sx = (f32x4){0.f, 0.f, 0.f, 0.f}, qx = sx, sw = sx;
#pragma unroll
            for (int i = 0; i < 2; ++i) {
                const f32x4 a0 = unpk4(xa[2 * r + i][0], xa[2 * r + i][1]), a1 = unpk4(xa[2 * r + i][2], xa[2 * r + i][3]);
                sx += a0 + a1; qx += a0 * a0 + a1 * a1;
                sw += unpk4(b[2 * r + i][0], b[2 * r + i][1]) + unpk4(b[2 * r + i][2], b[2 * r + i][3]);
            }
            const float s1 = fq_sum(row16_sum((sx[0] + sx[1]) + (sx[2] + sx[3]))), q1 = fq_sum(row16_sum((qx[0] + qx[1]) + (qx[2] + qx[3]))), s2 = fq_sum(row16_sum((sw[0] + sw[1]) + (sw[2] + sw[3])));
            if (lane == r) { g0 = s1; g1 = q1; }
            if (lane == 2 + r) g0 = s2;
        }
        if (lane < 4) st16((u32x4){__float_as_uint(g0), (unsigned)z, __float_as_uint(g1), (unsigned)(z >> 32)}, rsrc(ws + W_GRAN, 65536),
                                                             (lane < 2 ? xr + lane : 2046 + (tm >> 3) * 1024 + wr + lane) * 16);
        STAMP(ston, 1, 7);
    }
    WAITCNT(63, 0);
    const LAS float* R = (const LAS float*)(lds + L_R); const LAS float* M = (const LAS float*)(lds + L_M); const LAS float* WS = (const LAS float*)(lds + L_WS);
#pragma unroll 1
    for (int l = 0; l < 2; ++l) {
        {
            PHASE_VARS();
            const int wm = w >> 1, wn = w & 1;
            f32x4 acc[4][4];
            compute_phase<CG1, 4, 4, 4, false, 2, 3>(lds, wm * 64, wn * 64, l == 0, lane, acc, [&](int t) {
                if (t == 12) WAITCNT(0, 15); } ST_PASS(l * 5 + 1));
            float wsum[4];
#pragma unroll
            for (int m = 0; m < 4; ++m) wsum[m] = ((const LAS float*)(lds + L_WD))[wm * 64 + (m >> 1) * 32 + perm32((m & 1) * 16 + fr)];
            const __amdgpu_buffer_rsrc_t rC = rsrc(wl + W_T + tm * 2 * XP + (tn * 2 + wn) * 16384 + wm * 8192, 8192);
#pragma unroll
            for (int q = 0; q < 2; ++q) {
                const int cc = wn * 64 + q * 32 + fq * 8;
                const f32x4 r0 = *(const LAS f32x4*)(R + cc), r1 = *(const LAS f32x4*)(R + cc + 4), m0 = *(const LAS f32x4*)(M + cc), m1 = *(const LAS f32x4*)(M + cc + 4);
#pragma unroll
                for (int m = 0; m < 4; ++m)
                    st16(pk8(acc[m][2 * q] * r0 + m0 * wsum[m], acc[m][2 * q + 1] * r1 + m1 * wsum[m]), rC, lane * 16 + m * 2048 + q * 1024);
            }
            dep_signal(c, lane < 16 ? (lane * 16 + tm) * 32 + tn : (tm * 16 + (tn ^ 8)) * 32 + 16, 17, lds, lane);
            STAMP(true, l * 5 + 1, 8 + w);
            if (l == 0) {
                const __amdgpu_buffer_rsrc_t ri = rsrc(ws + W_WINT + (tn * 16 + tm) * 224 * 128, 224 * 128);
#pragma unroll
                for (int j = 0; j < 4; ++j) {
                    const int g = j * 4 + w;
                    if (j == 3 && w >= 2) break;
                    const int half = g >= 7, gg = g - half * 7;
                    LAS unsigned char* p = lds + L_TIN + (fq * 8 + (fr >> 2)) * 448 + half * 224 + (gg < 6 ? (gg >> 1) * 64 + (gg & 1) * 8 + (fr & 3) * 16 : 192 + (fr & 3) * 8);
                    const u32x4 v0 = read_tr<448>(p), v1 = read_tr<448>(p + 32 * 448);
                    st16(v0, ri, lane * 16 + g * 2048);
                    st16(v1, ri, lane * 16 + g * 2048 + 1024);
                    const f32x4 sm = (unpk4(v0[0], v0[1]) + unpk4(v0[2], v0[3])) + (unpk4(v1[0], v1[1]) + unpk4(v1[2], v1[3]));
                    const float st = fq_sum((sm[0] + sm[1]) + (sm[2] + sm[3]));
                    if (fq == 0) ((LAS float*)(lds + L_WS))[g * 16 + fr] = st;
                }
                STAMP(ston, 2, 5);
            }
        }
        {
            PHASE_VARS();
            const int wm = w >> 1, wn = w & 1, rg = wm * 2 + (tn >> 3), cb = (tn & 7) * 2 + wn;
            f32x4 acc[4][4];
            u32x4 xr[4], g[4];
            compute_phase<CG2, 4, 4, 4, false, 2, 3>(lds, wm * 64, wn * 64, false, lane, acc, [&](int t) {
                if (t < 0) res_load(piece((l ? ws + W_XB : (const char*)a->x0) + tm * XP, l == 0, rg * 32, cb, lane), xr);
                if (l) return;
                if (t == 2 && w == 3 && lane < 56) st16(*(const LAS u32x4*)(lds + L_WS + lane * 16), rsrc(ws + W_WSPIN + (size_t)(tn * 16 + tm) * 1024, 1024), lane * 16);
                if (t == 12) dep_signal<32>(ctr + 8192, (lane * 16 + tn) * 32 + 16 + tm, 16, lds, lane);
            } ST_PASS(l * 5 + 2));
            const __amdgpu_buffer_rsrc_t rO = rsrc(wl + W_EX + ((tm * 16 + tn) * 4 + w) * 4096, 4096), rI = rsrc(wl + W_EX + ((tm * 16 + (tn ^ 8)) * 4 + w) * 4096, 4096);
#pragma unroll
            for (int i = 0; i < 4; ++i) {
                const u32x4 o = pk8(acc[2 + (i >> 1)][(i & 1) * 2], acc[2 + (i >> 1)][(i & 1) * 2 + 1]);
                st16((u32x4){min(o[0], XCH_EMPTY - 1), min(o[1], XCH_EMPTY - 1), min(o[2], XCH_EMPTY - 1), min(o[3], XCH_EMPTY - 1)}, rO, lane * 16 + i * 1024);
            }
#pragma unroll 1
            for (int spin = 0; spin < SPIN_MAX; ++spin) {
                unsigned mx = 0;
#pragma unroll
                for (int i = 0; i < 4; ++i) g[i] = __builtin_amdgcn_raw_buffer_load_b128(rI, lane * 16 + i * 1024, 0, 16);
#pragma unroll
                for (int i = 0; i < 4; ++i) mx = max(max(mx, max(g[i][0], g[i][1])), max(g[i][2], g[i][3]));
                if (__all(mx != XCH_EMPTY)) break;
                __builtin_amdgcn_s_sleep(EX_SLEEP);
            }
            STAMP(ston, l * 5 + 2, 7);
#pragma unroll
            for (int i = 0; i < 4; ++i) { acc[i >> 1][(i & 1) * 2] += unpk4(g[i][0], g[i][1]); acc[i >> 1][(i & 1) * 2 + 1] += unpk4(g[i][2], g[i][3]); }
            res_store(acc, xr, piece(wl + W_XA + tm * XP, false, rg * 32, cb, lane), sp + 65536 + (tm * 16 + cb) * 256 + rg * 64, lane);
            dep_signal(c + 8192, (tm * 16 + lane) * 32 + tn, 16, lds, lane);
            STAMP(true, l * 5 + 2, 8 + w);
        }
        u32x4 zz[7];
        {
            PHASE_VARS();
            f32x4 acc[2][14];
            compute_phase<CG3, 2, 14, 7, true, 1, 1>(lds, w * 32, 0, false, lane, acc, [](int) {} ST_PASS(l * 5 + 3));
            f32x4 s[2], q[2];
#pragma unroll
            for (int m = 0; m < 2; ++m) {
                const f32x4 r = *(const LAS f32x4*)(R + w * 32 + fq * 8 + m * 4), mr = *(const LAS f32x4*)(M + w * 32 + fq * 8 + m * 4);
                s[m] = (f32x4){0.f, 0.f, 0.f, 0.f}; q[m] = s[m];
#pragma unroll
                for (int n = 7; n < 14; ++n) {
                    const f32x4 z = gelu_tanh(acc[m][n] * r + mr * WS[n * 16 + fr]);
                    acc[m][n] = z; s[m] += z; q[m] += z * z;
                }
            }
            float v8[8], v4[4], v2[2];
#pragma unroll
            for (int i = 0; i < 8; ++i) {
                const float lo = (i & 1) ? q[0][i >> 1] : s[0][i >> 1], hi = (i & 1) ? q[1][i >> 1] : s[1][i >> 1];
                v8[i] = ((fr & 8) ? hi : lo) + dpp_mov<0x128>((fr & 8) ? lo : hi);
            }
#pragma unroll
            for (int i = 0; i < 4; ++i) v4[i] = ((fr & 4) ? v8[i + 4] : v8[i]) + dpp_mov<0x141>(dpp_mov<0x1b>((fr & 4) ? v8[i] : v8[i + 4]));
#pragma unroll
            for (int i = 0; i < 2; ++i) v2[i] = ((fr & 2) ? v4[i + 2] : v4[i]) + dpp_mov<0x4e>((fr & 2) ? v4[i] : v4[i + 2]);
            const float pv = ((fr & 1) ? v2[1] : v2[0]) + dpp_mov<0xb1>((fr & 1) ? v2[0] : v2[1]);
            const __amdgpu_buffer_rsrc_t rX = rsrc(ws + W_XCH + l * 262144 + (size_t)(tm * 4 + w) * 4096, 4096);
            __builtin_amdgcn_raw_buffer_store_b32(xch_word(pv), rX, tn * 256 + lane * 4, 0, 16);
            SB0();
            STAMP(ston, l * 5 + 3, 5);
            auto z1 = [&](int n0, int n1) {
                const float r0 = R[w * 32 + perm32(fr)], r1 = R[w * 32 + perm32(16 + fr)], m0 = M[w * 32 + perm32(fr)], m1 = M[w * 32 + perm32(16 + fr)];
#pragma unroll
                for (int n = 0; n < 7; ++n) {
                    if (n < n0 || n >= n1) continue;
                    const f32x4 wv = *(const LAS f32x4*)(WS + n * 16 + fq * 4);
                    zz[n] = pk8(gelu_tanh(acc[0][n] * r0 + wv * m0), gelu_tanh(acc[1][n] * r1 + wv * m1));
                    asm volatile("" : "+v"(zz[n]));
                }
                SB0();
            };
            z1(0, Z1A);
            STAMP(ston, l * 5 + 3, 6);
            f32x4 t;
            for (int spin = 0;; ++spin) {
                u32x4 g[4]; bool ok = true; t = (f32x4){0.f, 0.f, 0.f, 0.f};
#pragma unroll
                for (int i = 0; i < 4; ++i) g[i] = __builtin_amdgcn_raw_buffer_load_b128(rX, lane * 16 + i * 1024, 0, 16);
#pragma unroll
                for (int i = 0; i < 4; ++i) {
                    ok &= (g[i][0] != XCH_EMPTY) & (g[i][1] != XCH_EMPTY) & (g[i][2] != XCH_EMPTY) & (g[i][3] != XCH_EMPTY);
                    t += __builtin_bit_cast(f32x4, g[i]);
                }
                if (__all(ok) || spin > SPIN_MAX) break;
                __builtin_amdgcn_s_sleep(XSL);
            }
            STAMP(ston, l * 5 + 3, 7);
            const float mean0 = fq_sum(t[0]) * (1.0f / FH), mean1 = fq_sum(t[2]) * (1.0f / FH);
            const float r0 = __builtin_amdgcn_rsqf(fmaxf(fq_sum(t[1]) * (1.0f / FH) - mean0 * mean0, 0.f) + LN_EPS);
            const float r1 = __builtin_amdgcn_rsqf(fmaxf(fq_sum(t[3]) * (1.0f / FH) - mean1 * mean1, 0.f) + LN_EPS);
            LAS float* Z = (LAS float*)(lds + L_Z) + w * 64;
            if (lane < 16) { *(LAS f32x2*)(Z + lane * 2) = (f32x2){r0, r1}; *(LAS f32x2*)(Z + 32 + lane * 2) = (f32x2){-mean0 * r0, -mean1 * r1}; }
            WAITCNT(63, 0);
            const f32x4 ra = *(const LAS f32x4*)(Z + fq * 8), rb = *(const LAS f32x4*)(Z + fq * 8 + 4), ma = *(const LAS f32x4*)(Z + 32 + fq * 8), mb = *(const LAS f32x4*)(Z + 32 + fq * 8 + 4);
            const __amdgpu_buffer_rsrc_t rC = rsrc(wl + W_Z2T + (tn * 32 + tm * 2 + (w >> 1)) * 14336, 14336);
#pragma unroll
            for (int n = 0; n < 7; ++n)
                st16(pk8(acc[0][7 + n] * ra + ma, acc[1][7 + n] * rb + mb), rC, lane * 16 + (w & 1) * 1024 + n * 2048);
            SB0();
            z1(Z1A, Z1B);
            dep_signal(c + 2 * 8192, (lane * 16 + tn) * 32 + tm, 16, lds, lane);
            STAMP(true, l * 5 + 3, 8 + w);
            SB0();
            z1(Z1B, 7);
        }
        {
            PHASE_VARS();
            float b[2];
#pragma unroll
            for (int m = 0; m < 2; ++m) b[m] = bf_lo(a->bsp[tm * 128 + w * 32 + perm32(m * 16 + fr)]);
            f32x4 acc[2][7];
            compute_phase<CG4, 2, 7, 7, false, 4, 5>(lds, w * 32, 0, false, lane, acc, [](int) {} ST_PASS(l * 5 + 4));
            const __amdgpu_buffer_rsrc_t rC = rsrc(wl + W_H + tm * 128 * FH * 2 + w * 4096, 128 * FH * 2 - w * 4096);
            u32x2 tl[2];
#pragma unroll
            for (int m = 0; m < 2; ++m) {
#pragma unroll
                for (int q = 0; q < 3; ++q)
                    st16(pk8(unpk4(zz[2 * q][m * 2], zz[2 * q][m * 2 + 1]) * (acc[m][2 * q] + b[m]), unpk4(zz[2 * q + 1][m * 2], zz[2 * q + 1][m * 2 + 1]) * (acc[m][2 * q + 1] + b[m])),
                                                           rC, lane * 16 + (((tn * 3 + q) >> 1) * 16384 + ((tn * 3 + q) & 1) * 1024) + m * 2048);
                const f32x4 v = unpk4(zz[6][m * 2], zz[6][m * 2 + 1]) * (acc[m][6] + b[m]);
                tl[m] = (u32x2){pk_bf16(v[0], v[1]), pk_bf16(v[2], v[3])};
            }
            const auto s0 = __builtin_amdgcn_permlane16_swap(tl[0][0], tl[1][0], false, false), s1 = __builtin_amdgcn_permlane16_swap(tl[0][1], tl[1][1], false, false);
            st16((u32x4){s0[0], s1[0], s0[1], s1[1]}, rC, (fq & 1) * 2048 + (fq >> 1) * 256 + fr * 16 + ((24 + (tn >> 2)) * 16384 + (tn & 3) * 512));
            dep_signal(c + 3 * 8192, (tm * 16 + lane) * 32 + tn, 16, lds, lane);
            STAMP(true, l * 5 + 4, 8 + w);
        }
        {
            PHASE_VARS();
            u32x4 xr[4];
            res_load(piece(wl + W_XA + tm * XP, false, w * 32, tn, lane), xr);
            f32x4 acc[2][4];
            compute_phase<CG5, 2, 4, 4, false, 1, 1>(lds, w * 32, 0, false, lane, acc, [](int) {} ST_PASS(l * 5 + 5));
            res_store(acc, xr, piece((l ? (char*)a->out : ws + W_XB) + tm * XP, l == 1, w * 32, tn, lane), sp + 131072 + (tm * 16 + tn) * 256 + w * 64, lane);
            dep_signal(c + 4 * 8192, (lane * 16 + tm) * 32 + tn, 16, lds, lane);
            STAMP(true, l * 5 + 5, 8 + w);
        }
    }
    asm volatile(".Lk_main_e1:");
}

extern "C" void kernel_launch(void* const* d_in, const int* in_sizes, int n_in, void* d_out, int out_size, void* d_ws, size_t ws_size, hipStream_t stream) {
    char* ws = (char*)d_ws;
    (void)hipFuncSetAttribute((const void*)k_main, hipFuncAttributeMaxDynamicSharedMemorySize, LDS_TOTAL);
    Args a;
    a.x0 = (const u16*)d_in[0]; a.wsre = (const u16*)d_in[1]; a.wsim = (const u16*)d_in[2]; a.wdre = (const u16*)d_in[3]; a.wdim = (const u16*)d_in[4];
    a.win = (const u16*)d_in[6]; a.wsp = (const u16*)d_in[7]; a.bsp = (const u16*)d_in[8]; a.wout = (const u16*)d_in[9]; a.ws = ws; a.out = (u16*)d_out;
    k_main<<<256, 512, LDS_TOTAL, stream>>>(a);
}
```

```cpp
#include <hip/hip_runtime.h>
#include <hip/hip_bf16.h>
#include <utility>

typedef unsigned short u16;
typedef unsigned long long u64;
typedef short bf16x8 __attribute__((ext_vector_type(8)));
typedef short s16x4 __attribute__((ext_vector_type(4)));
typedef float f32x2 __attribute__((ext_vector_type(2)));
typedef float f32x4 __attribute__((ext_vector_type(4)));
typedef unsigned u32x2 __attribute__((ext_vector_type(2)));
typedef unsigned u32x4 __attribute__((ext_vector_type(4)));
#define LAS __attribute__((address_space(3)))
#define GAS __attribute__((address_space(1)))
#define DEV __device__ __forceinline__

constexpr int waitimm(int vm, int lgkm) { return (vm & 15) | (7 << 4) | ((lgkm & 15) << 8) | ((vm >> 4) << 14); }
#define WAITCNT(vm, lgkm) __builtin_amdgcn_s_waitcnt(waitimm(vm, lgkm))
#define BAR() do { asm volatile("" ::: "memory"); __builtin_amdgcn_s_barrier(); asm volatile("" ::: "memory"); } while (0)
#define SB0() __builtin_amdgcn_sched_barrier(0)
#define STAMP(on, ph, i)
#define ST_PARAM
#define ST_PASS(p)

constexpr int S_ = 2048, D_ = 1024, F_ = 3584, FH = 1792;
#define WP1 4
#define WP2 4
#define WP3 1
#define WP4 4
#define WP5 4
#define TILE_AUX 2
#define Z1A 3
#define Z1B 4
#define XSL 2
#define WO_AUX 0
#define FG1 1
#define FG2 1
#define FG4 1
#define EX_SLEEP 1
#define POLL_SLEEP 2
constexpr float LN_EPS = 1e-5f;
constexpr int SPIN_MAX = 1 << 17;

typedef __bf16 bf16x2 __attribute__((ext_vector_type(2)));
DEV unsigned pk_bf16(float lo, float hi) { return __builtin_bit_cast(unsigned, __builtin_convertvector((f32x2){lo, hi}, bf16x2)); }
DEV float bf_lo(unsigned v) { return __uint_as_float(v << 16); }
DEV float bf_hi(unsigned v) { return __uint_as_float(v & 0xffff0000u); }
DEV __amdgpu_buffer_rsrc_t rsrc(const void* p, unsigned bytes) { return __builtin_amdgcn_make_buffer_rsrc((void*)p, 0, bytes, 0x00020000); }
DEV void st16(u32x4 v, __amdgpu_buffer_rsrc_t r, int off) { __builtin_amdgcn_raw_buffer_store_b128(v, r, off, 0, 16); }
DEV f32x4 gelu_tanh(f32x4 x) {
    const float c0 = -2.3022081986f, c1 = c0 * 0.044715f;
    const f32x4 a = x * (x * x * c1 + c0);
    f32x4 d = (f32x4){__builtin_amdgcn_exp2f(a[0]), __builtin_amdgcn_exp2f(a[1]), __builtin_amdgcn_exp2f(a[2]), __builtin_amdgcn_exp2f(a[3])} + 1.0f;
    return x * (f32x4){__builtin_amdgcn_rcpf(d[0]), __builtin_amdgcn_rcpf(d[1]), __builtin_amdgcn_rcpf(d[2]), __builtin_amdgcn_rcpf(d[3])};
}
DEV u32x4 pk8(f32x4 a, f32x4 b) { return (u32x4){pk_bf16(a[0], a[1]), pk_bf16(a[2], a[3]), pk_bf16(b[0], b[1]), pk_bf16(b[2], b[3])}; }
DEV f32x4 unpk4(unsigned a, unsigned b) { return (f32x4){bf_lo(a), bf_hi(a), bf_lo(b), bf_hi(b)}; }
__host__ __device__ constexpr int perm32(int slot) { return 8 * ((slot & 15) >> 2) + 4 * ((slot >> 4) & 1) + (slot & 3); }
__host__ __device__ constexpr int inv32(int row) { return 16 * ((row >> 2) & 1) + 4 * ((row >> 3) & 3) + (row & 3); }
DEV float fq_sum(float a) {
    const auto p = __builtin_amdgcn_permlane16_swap(__float_as_uint(a), __float_as_uint(a), false, false);
    a = __uint_as_float(p[0]) + __uint_as_float(p[1]);
    const auto q = __builtin_amdgcn_permlane32_swap(__float_as_uint(a), __float_as_uint(a), false, false);
    return __uint_as_float(q[0]) + __uint_as_float(q[1]);
}
template <int CTRL> DEV float dpp_mov(float a) { return __builtin_bit_cast(float, __builtin_amdgcn_update_dpp(0, __builtin_bit_cast(int, a), CTRL, 0xf, 0xf, true)); }
template <int CTRL> DEV float dpp_add(float a) { return a + __builtin_bit_cast(float, __builtin_amdgcn_update_dpp(0, __builtin_bit_cast(int, a), CTRL, 0xf, 0xf, true)); }
DEV float row16_sum(float a) { a = dpp_add<0x128>(a); a = dpp_add<0x124>(a); a = dpp_add<0x122>(a); return dpp_add<0x121>(a); }
DEV float oct_sum(float a) { a = dpp_add<0xb1>(a); a = dpp_add<0x4e>(a); return dpp_add<0x141>(a); }
DEV float quad_sum(float a) { a = dpp_add<0xb1>(a); return dpp_add<0x4e>(a); }
constexpr unsigned XCH_EMPTY = 0xffffffffu;
DEV unsigned xch_word(float v) { const unsigned u = __float_as_uint(v); return u == XCH_EMPTY ? 0x7fc00000u : u; }

DEV int opaque_v(int v) { asm volatile("" : "+v"(v)); return v; }
DEV int opaque_s(int v) { asm volatile("" : "+s"(v)); return v; }
template <class F, int... I> DEV void static_for_impl(F&& f, std::integer_sequence<int, I...>) { (f(std::integral_constant<int, I>{}), ...); }
template <int N, class F> DEV void static_for(F&& f) { static_for_impl(f, std::make_integer_sequence<int, N>{}); }

template <int BM_, int BN_, int NS_, int NT_, int LDP_> struct Cfg {
    static constexpr int BM = BM_, BN = BN_, NS = NS_, NT = NT_, LDP = LDP_;
    static constexpr bool WISP = LDP_ != 0;
    static constexpr int PB = BM * 128, QB = BN * 128, QBASE = NS * PB, RING = QBASE + NS * QB;
    static constexpr int LP = BM / 32, LQ = BN / 32;
    static constexpr int LW = WISP ? LP : LQ, LA = WISP ? LQ : LP;
};
constexpr int RING_MAX = 135168;
constexpr int L_TIN = 131072, L_TOUT = 135168, L_Z = 151552;
constexpr int L_R = 159744, L_M = L_R + 512, L_WD = L_M + 512, L_WS = L_WD + 512, L_JUNK = L_WS + 1024, L_SYNC = L_JUNK + 256, L_TAG = L_SYNC + 48, LDS_TOTAL = L_SYNC + 64;

template <class C, int WPRE, int N0, class Dep, class Tail>
DEV void loader_phase(LAS unsigned char* lds, const void* P, const void* Q, bool qrow, int psw, int lw, int lane, unsigned drained, Dep&& dep, Tail&& tail ST_PARAM) {
    constexpr int NS = C::NS, NT = C::NT, LA = C::LA, LW = C::LW;
    static_assert(C::RING <= RING_MAX && NS >= 3 && NT >= 2 * NS && C::BM % 32 == 0, "");
    lane = opaque_v(lane); lw = opaque_s(lw);
    const __amdgpu_buffer_rsrc_t rP = rsrc(P, NT * C::PB), rQ = rsrc(Q, NT * C::QB);
    const int vrow = perm32(lw * 8 + (lane >> 3)) * NT * 128 + ((lane & 7) ^ (((lw & 1) * 4 + (lane >> 4)) & 7)) * 16, vfrag = lw * 1024 + lane * 16;
    const int vP = C::WISP ? vrow : vfrag, vQ = qrow ? vrow : vfrag, sq = qrow ? NT * 4096 : 4096, kq = qrow ? 128 : C::QB;
    auto issueP = [&](int slot, int kt) {
        LAS unsigned char* dst = lds + slot * C::PB + lw * 1024;
#pragma unroll
        for (int i = 0; i < C::LP; ++i)
            __builtin_amdgcn_raw_ptr_buffer_load_lds(rP, (LAS void*)(dst + (i ^ psw) * 4096), 16, vP, C::WISP ? i * NT * 4096 + kt * 128 : i * 4096 + kt * C::PB, 0, C::WISP ? 0 : 16);
    };
    auto issueQ = [&](int slot, int kt) {
        LAS unsigned char* dst = lds + C::QBASE + slot * C::QB + lw * 1024;
#pragma unroll
        for (int j = 0; j < (C::BN + 31) / 32; ++j)
            if (j < C::LQ || j * 4 + lw < C::BN / 8) __builtin_amdgcn_raw_ptr_buffer_load_lds(rQ, (LAS void*)(dst + j * 4096), 16, vQ, j * sq + kt * kq, 0, 16);
    };
    auto issueW = [&](int slot, int kt) { if (C::WISP) issueP(slot, kt); else issueQ(slot, kt); };
    auto issueA = [&](int slot, int kt) { if (C::WISP) issueQ(slot, kt); else issueP(slot, kt); };
    if (drained) {
        while ((unsigned)__builtin_amdgcn_readfirstlane(*(volatile LAS unsigned*)(lds + L_SYNC + 16)) < drained) __builtin_amdgcn_s_sleep(2);
#pragma unroll 1
        for (int s = 0; s < WPRE; ++s) issueW(s, s);
        dep();
        STAMP(ston, ph, 4);
        issueA(0, 0);
        if constexpr (WPRE < 2) issueW(1, 1);
        issueA(1, 1);
        WAITCNT(LA + (WPRE < 2 ? LW : 0), 15);
        BAR();
#pragma unroll
        for (int s = 2; s < NS; ++s) { if (s >= WPRE) issueW(s, s); issueA(s, s); }
    } else {
        issueW(0, 0); issueA(0, 0); issueW(1, 1); issueA(1, 1);
        STAMP(ston, ph, 4);
        WAITCNT(LA + LW, 15);
        BAR();
#pragma unroll 1
        for (int s = 2; s < NS; ++s) { issueW(s, s); issueA(s, s); }
    }
    static_for<NS - 2>([&](auto ic) {
        constexpr int t = decltype(ic)::value, late = NS - (WPRE > t + 2 ? WPRE : t + 2);
        WAITCNT((NS - 2 - t) * LA + late * LW + t * (LA + LW), 15);
        BAR();
        issueW(t, t + NS); issueA(t, t + NS);
    });
    int sl = NS - 2;
#pragma unroll 1
    for (int t = NS - 2; t < NT - NS; ++t) {
        WAITCNT((NS - 2) * (LA + LW), 15);
        BAR();
        issueW(sl, t + NS); issueA(sl, t + NS);
        sl = (sl + 1 == NS) ? 0 : sl + 1;
    }
    tail(std::integral_constant<int, 0>{});
    static_for<NS - 1>([&](auto ic) {
        constexpr int i = decltype(ic)::value;
        WAITCNT((NS - 2 - i) * (LA + LW) + N0, 15);
        BAR();
    });
    tail(std::integral_constant<int, 1>{});
    WAITCNT(63, 0);
    BAR();
}

template <class C, int MT, int NTL, int NSWAP, bool ROLL, int RNUM, int RDEN, int FG, class Hook, class Fin>
DEV void compute_phase(LAS unsigned char* lds, int prow0, int qrow0, bool qrow, int lane, f32x4 (&acc)[MT][NTL], Hook&& hook, Fin&& fin ST_PARAM) {
    constexpr int NR = MT + NTL, NM = MT * NTL, NT = C::NT;
    const int fr = lane & 15, fq = lane >> 4;
    int aoff[2], boff[2];
#pragma unroll
    for (int k = 0; k < 2; ++k) {
        const int orow = fr * 128 + ((k * 4 + fq) ^ ((fr >> 1) & 7)) * 16, ofrag = k * 1024 + lane * 16;
        aoff[k] = prow0 * 128 + (C::WISP ? orow : ofrag);
        boff[k] = C::QBASE + qrow0 * 128 + (qrow ? orow : ofrag);
    }
#pragma unroll
    for (int m = 0; m < MT; ++m)
#pragma unroll
        for (int n = 0; n < NTL; ++n) { acc[m][n] = (f32x4){0.f, 0.f, 0.f, 0.f}; asm volatile("" : "+v"(acc[m][n])); }
    auto mfma = [&](int m, int n, const bf16x8& a, const bf16x8& b) {
        if (n >= NSWAP) asm volatile("v_mfma_f32_16x16x32_bf16 %0, %1, %2, %0" : "+v"(acc[m][n]) : "v"(a), "v"(b));
        else asm volatile("v_mfma_f32_16x16x32_bf16 %0, %1, %2, %0" : "+v"(acc[m][n]) : "v"(b), "v"(a));
    };
    auto settle = [&](int m0, int cnt) {
        asm volatile("s_nop 15" ::: "memory");
#pragma unroll
        for (int m = 0; m < MT; ++m)
#pragma unroll
            for (int n = 0; n < NTL; ++n) if (m >= m0 && m < m0 + cnt) asm volatile("" : "+v"(acc[m][n]));
    };
    int sp = 0, sq = 0;
    auto next = [&]() { sp = (sp + C::PB == C::NS * C::PB) ? 0 : sp + C::PB; sq = (sq + C::QB == C::NS * C::QB) ? 0 : sq + C::QB; };
    BAR();
    STAMP(ston, ph, 1);
    if constexpr (!ROLL) {
        bf16x8 fa[4][MT], fb[4][NTL];
        auto rd1 = [&](int set, int k, int i) {
            if (i == 0) fa[set][0] = *(const LAS bf16x8*)(lds + sp + aoff[k]);
            else if (i <= NTL) fb[set][i - 1] = *(const LAS bf16x8*)(lds + sq + boff[k] + (i - 1) * 2048);
            else fa[set][i - NTL] = *(const LAS bf16x8*)(lds + sp + aoff[k] + (i - NTL) * 2048);
        };
        auto stg = [&](int set, bool rd) {
#pragma unroll
            for (int i = 0; i < 2 * NM; ++i) {
                const int st = set + i / NM, m = (i % NM) / NTL, n = i % NTL;
                mfma(m, n, fa[st][m], fb[st][n]); SB0();
                if (rd) {
#pragma unroll
                    for (int r = (i * RNUM + RDEN - 1) / RDEN; r < ((i + 1) * RNUM + RDEN - 1) / RDEN; ++r) if (r < 2 * NR) rd1((set ^ 2) + r / NR, r / NR, r % NR);
                    SB0();
                }
            }
        };
        static_assert((2 * NM * RNUM + RDEN - 1) / RDEN >= 2 * NR && NT % 2 == 0, "");
#pragma unroll
        for (int r = 0; r < 2 * NR; ++r) rd1(r / NR, r / NR, r % NR);
#pragma unroll 1
        for (int t = 0; t < NT - 2; t += 2) {
            next(); WAITCNT(63, 0); BAR(); SB0();
            stg(0, true);
            next(); WAITCNT(63, 0); BAR(); SB0();
            stg(2, true);
            hook(t); SB0();
        }
        hook(-1); SB0();
        next();
        stg(0, false);
        BAR(); SB0();
#pragma unroll
        for (int r = 0; r < 2 * NR; ++r) rd1(2 + r / NR, r / NR, r % NR);
        WAITCNT(63, 0); BAR(); SB0();
        static_for<MT / FG>([&](auto ic) {
            constexpr int m0 = MT - FG - decltype(ic)::value * FG;
#pragma unroll
            for (int i = 0; i < 2 * FG * NTL; ++i) { const int st = 2 + i / (FG * NTL), m = m0 + (i / NTL) % FG; mfma(m, i % NTL, fa[st][m], fb[st][i % NTL]); SB0(); }
            settle(m0, FG);
            if constexpr (m0 == 0) STAMP(ston, ph, 2);
            fin(std::integral_constant<int, m0>{}); SB0();
        });
    } else {
        static_assert(MT == 2, "");
        constexpr int G = 8;
        bf16x8 fa[2][2], fb[NTL];
        auto rda = [&](int set, int k, int m) { fa[set][m] = *(const LAS bf16x8*)(lds + sp + aoff[k] + m * 2048); };
        auto rdb = [&](int k, int n) { fb[n] = *(const LAS bf16x8*)(lds + sq + boff[k] + n * 2048); };
        auto half0 = [&]() {
#pragma unroll
            for (int j = 0; j < NM; ++j) {
                mfma(j & 1, j >> 1, fa[0][j & 1], fb[j >> 1]); SB0();
                if (j & 1) rdb(1, j >> 1); else if (j < 4) rda(1, 1, j >> 1);
                SB0();
            }
        };
        auto half1 = [&](bool rd) {
#pragma unroll
            for (int j = 0; j < NM; ++j) {
                if (j == G) { next(); WAITCNT(63, 0); BAR(); SB0(); }
                mfma(j & 1, j >> 1, fa[1][j & 1], fb[j >> 1]); SB0();
                if (rd && j >= G) {
                    if (j < G + 2) rda(0, 0, j - G);
#pragma unroll
                    for (int n = 0; n < NTL; ++n) if ((G + 2 + n > 2 * n + 1 ? G + 2 + n : 2 * n + 1) == j) rdb(0, n);
                    SB0();
                }
            }
        };
        rda(0, 0, 0); rda(0, 0, 1);
#pragma unroll
        for (int n = 0; n < NTL; ++n) rdb(0, n);
        SB0();
#pragma unroll 1
        for (int t = 0; t < NT - 1; ++t) { half0(); half1(true); }
        half0(); half1(false);
        settle(0, MT);
        STAMP(ston, ph, 2);
    }
}

DEV u64 launch_tag(LAS unsigned char* lds) { return *(volatile LAS u64*)(lds + L_TAG); }
DEV void box_poll(const u64* box, int nprod, u64 tag, int lane) {
#pragma unroll 1
    for (int spin = 0; spin < SPIN_MAX; ++spin) {
        const u64 v = __hip_atomic_load((const GAS u64*)box + min(lane, nprod - 1), __ATOMIC_RELAXED, __HIP_MEMORY_SCOPE_AGENT);
        if (__all(v == tag)) break;
        __builtin_amdgcn_s_sleep(POLL_SLEEP);
    }
}
DEV void dep_wait(const u64* box, int nprod, LAS unsigned char* lds, unsigned epoch, int lw, int lane) {
    volatile LAS unsigned* flag = (volatile LAS unsigned*)(lds + L_SYNC);
    if (lw == 0) {
        box_poll(box, nprod, launch_tag(lds), lane);
        *flag = epoch;
    } else {
        while ((unsigned)__builtin_amdgcn_readfirstlane(*flag) < epoch) __builtin_amdgcn_s_sleep(1);
    }
    asm volatile("" ::: "memory");
}
template <int CNT = 16> DEV void dep_signal(u64* boxes, int word, int ncons, LAS unsigned char* lds, int lane) {
    const u64 tag = launch_tag(lds);
    asm volatile("s_waitcnt vmcnt(0)" ::: "memory");
    unsigned old = 0;
    if (lane == 0) old = __hip_atomic_fetch_add((LAS unsigned*)(lds + L_SYNC + CNT), 1u, __ATOMIC_RELAXED, __HIP_MEMORY_SCOPE_WORKGROUP);
    if ((__builtin_amdgcn_readfirstlane(old) & 3) == 3 && lane < ncons) __hip_atomic_store((GAS u64*)boxes + word, tag, __ATOMIC_RELAXED, __HIP_MEMORY_SCOPE_AGENT);
}
DEV void touch(LAS unsigned char* lds, const u16* p, unsigned bytes, int off) {
    __builtin_amdgcn_raw_ptr_buffer_load_lds(rsrc(p, bytes), (LAS void*)(lds + L_JUNK), 4, off, 0, 0, 0);
}

template <int STRIDE> DEV u32x4 read_tr(LAS unsigned char* p) {
    const u32x2 a = __builtin_bit_cast(u32x2, __builtin_amdgcn_ds_read_tr16_b64_v4i16((LAS s16x4*)p)), b = __builtin_bit_cast(u32x2, __builtin_amdgcn_ds_read_tr16_b64_v4i16((LAS s16x4*)(p + 4 * STRIDE)));
    return (u32x4){a[0], a[1], b[0], b[1]};
}
DEV void block_sums_load(const void* p, unsigned bytes, int lw, int lane, f32x4 (&v)[4]) {
    const __amdgpu_buffer_rsrc_t r = rsrc(p, bytes);
#pragma unroll
    for (int i = 0; i < 4; ++i) v[i] = __builtin_bit_cast(f32x4, __builtin_amdgcn_raw_buffer_load_b128(r, (lane >> 4) * 1024 + (lane & 15) * 16, i * 4096 + lw * 256, 16));
}
DEV f32x4 block_sums(const f32x4 (&v)[4]) { const f32x4 s = (v[0] + v[1]) + (v[2] + v[3]); return (f32x4){fq_sum(s[0]), fq_sum(s[1]), fq_sum(s[2]), fq_sum(s[3])}; }
DEV void row_tables(LAS unsigned char* lds, int lw, int lane, const f32x4 (&v)[4], float sign) {
    const f32x4 t = block_sums(v);
    const float s = (lane & 16) ? t[2] : t[0], q = (lane & 16) ? t[3] : t[1];
    const float mean = s * (1.0f / D_), var = fmaxf(q * (1.0f / D_) - mean * mean, 0.f), r = sign * __builtin_amdgcn_rsqf(var + LN_EPS);
    ((LAS float*)(lds + (lane < 32 ? L_R : L_M)))[lw * 32 + perm32(lane & 31)] = lane < 32 ? r : -mean * r;
}

constexpr size_t MB = 1 << 20;
constexpr size_t LS = 30 * MB;
constexpr size_t W_T = 0, W_XA = 8 * MB, W_Z2T = 12 * MB, W_H = 19 * MB, W_EX = 26 * MB, W_XB = 60 * MB, W_WINT = 64 * MB, W_WOUTT = 71 * MB;
constexpr size_t W_SP = 75 * MB, W_GRAN = 77 * MB, W_WSPIN = 77 * MB + 65536, W_XCH = 77 * MB + 512 * 1024, W_CTR = 78 * MB, W_NONCE = 78 * MB + 768 * 1024;

typedef Cfg<128, 128, 4, D_ / 64, D_> CG1;
typedef Cfg<128, 128, 4, S_ / 64, S_> CG2;
typedef Cfg<128, 224, 3, D_ / 64, 0> CG3;
typedef Cfg<128, 112, 4, S_ / 64, S_> CG4;
typedef Cfg<128, 64, 4, FH / 64, 0> CG5;
constexpr int XP = 128 * D_ * 2;

struct Piece { __amdgpu_buffer_rsrc_t r; int v, s0, sm, sh; };
DEV Piece piece(const void* panel, bool row, int r0, int cb, int lane) {
    return Piece{rsrc(panel, XP), row ? perm32(lane & 15) * D_ * 2 + (lane >> 4) * 16 : lane * 16, row ? r0 * D_ * 2 + cb * 128 : cb * 16384 + r0 * 128, row ? 8 * D_ : 2048, row ? 64 : 1024};
}
DEV void res_load(const Piece& p, u32x4 (&xr)[4]) {
#pragma unroll
    for (int i = 0; i < 4; ++i) xr[i] = __builtin_amdgcn_raw_buffer_load_b128(p.r, p.v, p.s0 + (i >> 1) * p.sm + (i & 1) * p.sh, 16);
}
DEV void res_store(f32x4 (*acc)[4], const u32x4 (&xr)[4], const Piece& p, float* sp, int lane) {
    u32x4 sums;
#pragma unroll
    for (int m = 0; m < 2; ++m) {
        f32x4 s = (f32x4){0.f, 0.f, 0.f, 0.f}, qq = s;
#pragma unroll
        for (int h = 0; h < 2; ++h) {
            const u32x4 x = xr[m * 2 + h], o = pk8(acc[m][2 * h] + unpk4(x[0], x[1]), acc[m][2 * h + 1] + unpk4(x[2], x[3]));
            st16(o, p.r, p.v + (p.s0 + m * p.sm + h * p.sh));
            const f32x4 a = unpk4(o[0], o[1]), b = unpk4(o[2], o[3]);
            s += a + b; qq += a * a + b * b;
        }
        sums[2 * m] = __float_as_uint(fq_sum((s[0] + s[1]) + (s[2] + s[3]))); sums[2 * m + 1] = __float_as_uint(fq_sum((qq[0] + qq[1]) + (qq[2] + qq[3])));
    }
    if (lane < 16) st16(sums, rsrc(sp, 256), lane * 16);
}

DEV void ex_clear(char* p, int lane) {
#pragma unroll
    for (int i = 0; i < 4; ++i) st16((u32x4){XCH_EMPTY, XCH_EMPTY, XCH_EMPTY, XCH_EMPTY}, rsrc(p, 4096), lane * 16 + i * 1024);
}

struct Args { const u16 *x0, *wsre, *wsim, *wdre, *wdim, *win, *wsp, *bsp, *wout; char* ws; u16* out; };
typedef const __attribute__((address_space(4))) Args* ArgPtr;
DEV ArgPtr opaque_a(ArgPtr p) { asm volatile("" : "+s"(p)); return p; }

__global__ __launch_bounds__(512, 2) void k_main(Args) {
    extern __shared__ __attribute__((aligned(1024))) unsigned char shm[];
    LAS unsigned char* lds = (LAS unsigned char*)shm;
    const int tid = threadIdx.x, lane0 = tid & 63, w0 = __builtin_amdgcn_readfirstlane(tid >> 6);
    ArgPtr ka = (ArgPtr)__builtin_amdgcn_kernarg_segment_ptr();
    {
        u64 pc; unsigned len, k0, k1, off = tid * 128;
        asm volatile("s_getpc_b64 %0\n.Lk_main_pc:\n\ts_load_dword %2, %5, 0x0\n\ts_load_dword %3, %5, 0x54\n\t"
                     "s_max_i32 %1, .Lk_main_e0-.Lk_main_pc, 0\n\ts_max_i32 %1, .Lk_main_e1-.Lk_main_pc, %1\n\ts_mov_b32 m0, %6\n\tv_min_u32 %4, %1, %4\n\t"
                     "global_load_lds_dword %4, %0\n\ts_waitcnt lgkmcnt(0)"
                     : "=&s"(pc), "=&s"(len), "=&s"(k0), "=&s"(k1), "+v"(off), "+s"(ka) : "s"((unsigned)(size_t)(lds + L_JUNK)) : "scc", "memory");
        SB0();
    }
    const int tm0 = ((blockIdx.x & 7) >> 1) * 4 + (blockIdx.x >> 6), tn0 = (blockIdx.x & 1) * 8 + ((blockIdx.x >> 3) & 7);
#define PHASE_VARS() const int lane = opaque_v(lane0), w = opaque_s(w0), tm = opaque_s(tm0), tn = opaque_s(tn0), fr = lane & 15, fq = lane >> 4; (void)fr; (void)fq; (void)w; \
    const ArgPtr a = opaque_a(ka); char* const ws = a->ws; char* const wl = ws + l * LS; u64* const ctr = (u64*)(ws + W_CTR); u64* const c = ctr + l * 5 * 8192; float* const sp = (float*)(ws + W_SP + l * 512 * 1024); \
    (void)wl; (void)c; (void)sp
    if (w0 >= 4) {
        const int lw = w0 - 4;
        auto none = [&](auto) {};
#pragma unroll 1
        for (int l = 0; l < 2; ++l) {
            const unsigned first = l == 0 ? ~0u : 0u;
            f32x4 v[4], u[4];
            { PHASE_VARS(); const int lt = (w - 4) * 64 + lane;
            const u16* pw = (tm < 8 ? a->wdre : a->wdim) + (size_t)(tm & 7) * 128 * D_;
            const __amdgpu_buffer_rsrc_t rg = rsrc(ws + W_GRAN, 65536 & first);
            const int goff = (lt < 128 ? tn * 128 + lt : 1920 + tm * 128 + lt) * 16;
            u32x4 g;
            loader_phase<CG1, WP1, 6>(lds, pw, (l == 0 ? (const char*)a->x0 : ws + W_XB) + tn * XP, l == 0, 0, lw, lane, l * 20, [&]() { dep_wait(ctr + 4 * 8192 + (tm * 16 + tn) * 32, 16, lds, 5, lw, lane); }, [&](auto ic) {
                constexpr int i = decltype(ic)::value;
                if constexpr (i == 0) {
                    block_sums_load(sp + tn * 4096, 16384 & ~first, lw, lane, v);
                    g = __builtin_amdgcn_raw_buffer_load_b128(rg, goff, 0, 16);
                    touch(lds, (tn < 8 ? a->wsre : a->wsim) + (size_t)(tm * 128 + (tn & 7) * 16 + lw * 4) * S_, 16384 & first, fq * 4096 + 2048 + fr * 128);
                } else {
                    const float sign = tm < 8 ? 1.0f : -1.0f;
                    if (l) { row_tables(lds, lw, lane, v, sign); return; }
                    const u64 tag = launch_tag(lds);
                    if (!__all(g[1] == (unsigned)tag && g[3] == (unsigned)(tag >> 32))) {
                        asm volatile("" ::: "memory");
#pragma unroll 1
                        for (int spin = 0; spin < SPIN_MAX; ++spin) {
                            g = __builtin_amdgcn_raw_buffer_load_b128(rg, goff, 0, 16);
                            if (__all(g[1] == (unsigned)tag && g[3] == (unsigned)(tag >> 32))) break;
                            __builtin_amdgcn_s_sleep(POLL_SLEEP);
                        }
                    }
                    const float mean = __uint_as_float(g[0]) * (1.0f / D_), var = fmaxf(__uint_as_float(g[2]) * (1.0f / D_) - mean * mean, 0.f), rs = sign * __builtin_amdgcn_rsqf(var + LN_EPS);
                    if (lt < 128) { ((LAS float*)(lds + L_R))[lt] = rs; ((LAS float*)(lds + L_M))[lt] = -mean * rs; }
                    else ((LAS float*)(lds + L_WD))[lt - 128] = __uint_as_float(g[0]);
                }
            } ST_PASS(l * 5 + 1)); }
            { PHASE_VARS();
            u64 tw;
            const u64* tbox = ctr + 8192 + (tm * 16 + tn) * 32 + 16;
            loader_phase<CG2, WP2, 1>(lds, (tn < 8 ? a->wsre : a->wsim) + (size_t)tm * 128 * S_, wl + W_T + tn * 2 * XP, false, tn >> 3, lw, lane, l * 20 + 4,
                              [&]() { dep_wait(c + (tm * 16 + tn) * 32, 17, lds, l * 5 + 1, lw, lane); },
                              [&](auto ic) {
                                  constexpr int i = decltype(ic)::value;
                                  if constexpr (i == 0) {
                                      tw = __hip_atomic_load((const GAS u64*)tbox + (lane & 15), __ATOMIC_RELAXED, __HIP_MEMORY_SCOPE_AGENT);
                                  } else { const u64 tag = launch_tag(lds); if (!__all(tw == tag)) box_poll(tbox, 16, tag, lane); }
                              } ST_PASS(l * 5 + 2)); }
            { PHASE_VARS(); const int lt = (w - 4) * 64 + lane;
            loader_phase<CG3, WP3, 13>(lds, wl + W_XA + tm * XP, ws + W_WINT + tn * 224 * D_ * 2, false, 0, lw, lane, l * 20 + 8,
                              [&]() { dep_wait(c + 8192 + (tm * 16 + tn) * 32, 16, lds, l * 5 + 2, lw, lane);
                                      if (blockIdx.x == 0 && l == 0 && lt == 0) __hip_atomic_store((GAS u64*)(ws + W_NONCE), launch_tag(lds), __ATOMIC_RELAXED, __HIP_MEMORY_SCOPE_AGENT); },
                              [&](auto ic) {
                                  constexpr int i = decltype(ic)::value;
                                  if constexpr (i == 0) {
                                      block_sums_load(sp + 65536 + tm * 4096, 16384, lw, lane, v);
                                      block_sums_load(ws + W_WSPIN + (size_t)tn * 16384, 16384 & first, lw, lane, u);
                                      const __amdgpu_buffer_rsrc_t r = rsrc(a->wout + (size_t)tm * 112 * D_, (112 * D_ * 2) & first);
#pragma unroll
                                      for (int j = 0; j < 4; ++j)
                                          __builtin_amdgcn_raw_ptr_buffer_load_lds(r, (LAS void*)(lds + L_TOUT + lw * 4096 + j * 1024), 16, (lane >> 1) * D_ * 2 + (lane & 1) * 16, j * 32 * D_ * 2 + tn * 128 + lw * 32, 0, WO_AUX);
                                      touch(lds, a->wsp + (size_t)(tm * 128 + tn * 8 + lw * 2) * S_, 8192 & first, lane * 128);
                                  } else {
                                      row_tables(lds, lw, lane, v, 1.0f);
                                      const f32x4 s = block_sums(u);
                                      if (l == 0 && lane < 16) *(LAS f32x4*)(lds + L_WS + lw * 256 + lane * 16) = s;
                                  }
                              } ST_PASS(l * 5 + 3));
            if (l == 0) {
                WAITCNT(1, 15);
                STAMP(ston, 4, 5);
                asm volatile("" ::: "memory");
                const __amdgpu_buffer_rsrc_t ro = rsrc(ws + W_WOUTT + tn * 64 * FH * 2, 64 * FH * 2);
                const int slot = (lw >> 1) * 32 + inv32((lw & 1) * 16 + fr), vo = (slot >> 4) * 2048 + fq * 256 + (slot & 15) * 16;
#pragma unroll
                for (int j = 0; j < 4; ++j) {
                    u32x2 x, y;
                    asm volatile("ds_read_b64_tr_b16 %0, %2\n\tds_read_b64_tr_b16 %1, %2 offset:128\n\ts_waitcnt lgkmcnt(0)"
                                 : "=&v"(x), "=&v"(y) : "v"((unsigned)(size_t)(lds + L_TOUT) + lw * 4096 + j * 1024 + fq * 256 + fr * 8) : "memory");
                    const int un = j < 3 ? tm * 3 + j : 48 + (tm >> 1);
                    if (j < 3 || fq < 2) st16((u32x4){x[0], x[1], y[0], y[1]}, ro, vo + ((un >> 1) * 8192 + (un & 1) * 1024 + (j < 3 ? 0 : (tm & 1) * 512)));
                }
                ex_clear(ws + LS + W_EX + ((tm * 16 + tn) * 4 + lw) * 4096, lane);
                STAMP(ston, 4, 6);
                dep_signal<32>(ctr + 3 * 8192, (lane * 16 + tn) * 32 + 16 + tm, 16, lds, lane);
                STAMP(ston, 4, 7);
            } }
            { PHASE_VARS();
            u64 tw;
            const u64* tbox = ctr + 3 * 8192 + (tm * 16 + tn) * 32 + 16;
            loader_phase<CG4, WP4, 1>(lds, a->wsp + (size_t)tm * 128 * S_, wl + W_Z2T + tn * 112 * S_ * 2, false, 0, lw, lane, l * 20 + 12, [&]() { dep_wait(c + 2 * 8192 + (tm * 16 + tn) * 32, 16, lds, l * 5 + 3, lw, lane); },
                              [&](auto ic) {
                                  if constexpr (decltype(ic)::value == 0) tw = __hip_atomic_load((const GAS u64*)tbox + (lane & 15), __ATOMIC_RELAXED, __HIP_MEMORY_SCOPE_AGENT);
                                  else { const u64 tag = launch_tag(lds); if (!__all(tw == tag)) box_poll(tbox, 16, tag, lane); }
                              } ST_PASS(l * 5 + 4)); }
            { PHASE_VARS();
            loader_phase<CG5, WP5, 0>(lds, wl + W_H + tm * 128 * FH * 2, ws + W_WOUTT + tn * 64 * FH * 2, false, 0, lw, lane, l * 20 + 16,
                              [&]() { dep_wait(c + 3 * 8192 + (tm * 16 + tn) * 32, 16, lds, l * 5 + 4, lw, lane); }, none ST_PASS(l * 5 + 5)); }
        }
        asm volatile(".Lk_main_e0:");
        return;
    }
    {
        const int l = 0;
        PHASE_VARS();
        u64 z = __hip_atomic_load((const GAS u64*)(ws + W_NONCE), __ATOMIC_RELAXED, __HIP_MEMORY_SCOPE_AGENT);
        ex_clear(ws + W_EX + ((tm * 16 + tn) * 4 + w) * 4096, lane);
        SB0();
        const int xr = tn * 128 + tm * 8 + w * 2, wr = (tm & 7) * 128 + tn * 8 + w * 2;
        const __amdgpu_buffer_rsrc_t rx = rsrc(a->x0 + (size_t)xr * D_, 4096), rw = rsrc((tm < 8 ? a->wdre : a->wdim) + (size_t)wr * D_, 4096);
        u32x4 xa[4], b[4];
#pragma unroll
        for (int i = 0; i < 4; ++i) { xa[i] = __builtin_amdgcn_raw_buffer_load_b128(rx, i * 1024 + lane * 16, 0, 0); b[i] = __builtin_amdgcn_raw_buffer_load_b128(rw, i * 1024 + lane * 16, 0, 0); }
        {
            const __amdgpu_buffer_rsrc_t r = rsrc(a->win + (size_t)tm * 64 * F_, 64 * F_ * 2);
#pragma unroll
            for (int j = 0; j < 7; ++j) {
                const int q = j * 4 + w, cb = q * 64 + lane, row = cb / 28, ch = cb - row * 28;
                __builtin_amdgcn_raw_ptr_buffer_load_lds(r, (LAS void*)(lds + L_TIN + q * 1024), 16, row * F_ * 2 + (ch < 14 ? ch * 16 : FH * 2 - 224 + ch * 16), tn * 224, 0, TILE_AUX);
            }
        }
        touch(lds, (tn < 8 ? a->wsre : a->wsim) + (size_t)(tm * 128 + (tn & 7) * 16 + w * 4) * S_, 16384, fq * 4096 + fr * 128);
        if (w == 0) __builtin_amdgcn_raw_ptr_buffer_load_lds(rsrc(ctr + (tm * 16 + tn) * 32, 9 * 65536), (LAS void*)(lds + L_JUNK), 4, (lane >> 1) * 65536 + (lane & 1) * 128, 0, 0, 16);
        STAMP(ston, 1, 5);
        if (tid < 128) st16((u32x4){XCH_EMPTY, XCH_EMPTY, XCH_EMPTY, XCH_EMPTY}, rsrc(ws + W_XCH + blockIdx.x * 2048, 2048), tid * 16);
        if (tid < 12) *(LAS unsigned*)(lds + L_SYNC + tid * 4) = 0u;
        z += 0x9E3779B97F4A7C15ull; z = (z ^ (z >> 30)) * 0xBF58476D1CE4E5B9ull; z = (z ^ (z >> 27)) * 0x94D049BB133111EBull; z ^= z >> 31;
        if (tid == 0) *(LAS u64*)(lds + L_TAG) = z;
        STAMP(ston, 1, 6);
        float g0 = 0.f, g1 = 0.f;
#pragma unroll
        for (int r = 0; r < 2; ++r) {
            f32x4 sx = (f32x4){0.f, 0.f, 0.f, 0.f}, qx = sx, sw = sx;
#pragma unroll
            for (int i = 0; i < 2; ++i) {
                const f32x4 a0 = unpk4(xa[2 * r + i][0], xa[2 * r + i][1]), a1 = unpk4(xa[2 * r + i][2], xa[2 * r + i][3]);
                sx += a0 + a1; qx += a0 * a0 + a1 * a1;
                sw += unpk4(b[2 * r + i][0], b[2 * r + i][1]) + unpk4(b[2 * r + i][2], b[2 * r + i][3]);
            }
            const float s1 = fq_sum(row16_sum((sx[0] + sx[1]) + (sx[2] + sx[3]))), q1 = fq_sum(row16_sum((qx[0] + qx[1]) + (qx[2] + qx[3]))), s2 = fq_sum(row16_sum((sw[0] + sw[1]) + (sw[2] + sw[3])));
            if (lane == r) { g0 = s1; g1 = q1; }
            if (lane == 2 + r) g0 = s2;
        }
        if (lane < 4) st16((u32x4){__float_as_uint(g0), (unsigned)z, __float_as_uint(g1), (unsigned)(z >> 32)}, rsrc(ws + W_GRAN, 65536),
                                                             (lane < 2 ? xr + lane : 2046 + (tm >> 3) * 1024 + wr + lane) * 16);
        STAMP(ston, 1, 7);
    }
    WAITCNT(63, 0);
    const LAS float* R = (const LAS float*)(lds + L_R); const LAS float* M = (const LAS float*)(lds + L_M); const LAS float* WS = (const LAS float*)(lds + L_WS);
#pragma unroll 1
    for (int l = 0; l < 2; ++l) {
        {
            PHASE_VARS();
            const int wm = w >> 1, wn = w & 1;
            f32x4 acc[4][4];
            const __amdgpu_buffer_rsrc_t rC = rsrc(wl + W_T + tm * 2 * XP + (tn * 2 + wn) * 16384 + wm * 8192, 8192);
            f32x4 rr[4], mm[4];
            float wsum[4];
            compute_phase<CG1, 4, 4, 4, false, 2, 3, FG1>(lds, wm * 64, wn * 64, l == 0, lane, acc, [&](int t) { if (t == 12) WAITCNT(0, 15); },
                                                          [&](auto ic) {
                constexpr int m0 = decltype(ic)::value;
                if constexpr (m0 == 4 - FG1) {
#pragma unroll
                    for (int i = 0; i < 4; ++i) {
                        const int cc = wn * 64 + (i >> 1) * 32 + fq * 8 + (i & 1) * 4;
                        rr[i] = *(const LAS f32x4*)(R + cc); mm[i] = *(const LAS f32x4*)(M + cc);
                        wsum[i] = ((const LAS float*)(lds + L_WD))[wm * 64 + (i >> 1) * 32 + perm32((i & 1) * 16 + fr)];
                    }
                }
#pragma unroll
                for (int m = m0; m < m0 + FG1; ++m)
#pragma unroll
                    for (int q = 0; q < 2; ++q)
                        st16(pk8(acc[m][2 * q] * rr[2 * q] + mm[2 * q] * wsum[m], acc[m][2 * q + 1] * rr[2 * q + 1] + mm[2 * q + 1] * wsum[m]), rC, lane * 16 + m * 2048 + q * 1024);
            } ST_PASS(l * 5 + 1));
            dep_signal(c, lane < 16 ? (lane * 16 + tm) * 32 + tn : (tm * 16 + (tn ^ 8)) * 32 + 16, 17, lds, lane);
            STAMP(true, l * 5 + 1, 8 + w);
            if (l == 0) {
                const __amdgpu_buffer_rsrc_t ri = rsrc(ws + W_WINT + (tn * 16 + tm) * 224 * 128, 224 * 128);
#pragma unroll
                for (int j = 0; j < 4; ++j) {
                    const int g = j * 4 + w;
                    if (j == 3 && w >= 2) break;
                    const int half = g >= 7, gg = g - half * 7;
                    LAS unsigned char* p = lds + L_TIN + (fq * 8 + (fr >> 2)) * 448 + half * 224 + (gg < 6 ? (gg >> 1) * 64 + (gg & 1) * 8 + (fr & 3) * 16 : 192 + (fr & 3) * 8);
                    const u32x4 v0 = read_tr<448>(p), v1 = read_tr<448>(p + 32 * 448);
                    st16(v0, ri, lane * 16 + g * 2048);
                    st16(v1, ri, lane * 16 + g * 2048 + 1024);
                    const f32x4 sm = (unpk4(v0[0], v0[1]) + unpk4(v0[2], v0[3])) + (unpk4(v1[0], v1[1]) + unpk4(v1[2], v1[3]));
                    const float st = fq_sum((sm[0] + sm[1]) + (sm[2] + sm[3]));
                    if (fq == 0) ((LAS float*)(lds + L_WS))[g * 16 + fr] = st;
                }
                STAMP(ston, 2, 5);
            }
        }
        {
            PHASE_VARS();
            const int wm = w >> 1, wn = w & 1, rg = wm * 2 + (tn >> 3), cb = (tn & 7) * 2 + wn;
            f32x4 acc[4][4];
            u32x4 xr[4], g[4];
            const __amdgpu_buffer_rsrc_t rO = rsrc(wl + W_EX + ((tm * 16 + tn) * 4 + w) * 4096, 4096), rI = rsrc(wl + W_EX + ((tm * 16 + (tn ^ 8)) * 4 + w) * 4096, 4096);
            compute_phase<CG2, 4, 4, 4, false, 2, 3, FG2>(lds, wm * 64, wn * 64, false, lane, acc, [&](int t) {
                if (t < 0) res_load(piece((l ? ws + W_XB : (const char*)a->x0) + tm * XP, l == 0, rg * 32, cb, lane), xr);
                if (l) return;
                if (t == 2 && w == 3 && lane < 56) st16(*(const LAS u32x4*)(lds + L_WS + lane * 16), rsrc(ws + W_WSPIN + (size_t)(tn * 16 + tm) * 1024, 1024), lane * 16);
                if (t == 12) dep_signal<32>(ctr + 8192, (lane * 16 + tn) * 32 + 16 + tm, 16, lds, lane);
            }, [&](auto ic) {
                constexpr int m0 = decltype(ic)::value;
#pragma unroll
                for (int i = 0; i < 4; ++i) {
                    if (2 + (i >> 1) < m0 || 2 + (i >> 1) >= m0 + FG2) continue;
                    const u32x4 o = pk8(acc[2 + (i >> 1)][(i & 1) * 2], acc[2 + (i >> 1)][(i & 1) * 2 + 1]);
                    st16(__builtin_elementwise_min(o, (u32x4){XCH_EMPTY - 1, XCH_EMPTY - 1, XCH_EMPTY - 1, XCH_EMPTY - 1}), rO, lane * 16 + i * 1024);
                }
            } ST_PASS(l * 5 + 2));
#pragma unroll 1
            for (int spin = 0; spin < SPIN_MAX; ++spin) {
                unsigned mx = 0;
#pragma unroll
                for (int i = 0; i < 4; ++i) g[i] = __builtin_amdgcn_raw_buffer_load_b128(rI, lane * 16 + i * 1024, 0, 16);
#pragma unroll
                for (int i = 0; i < 4; ++i) mx = max(max(mx, max(g[i][0], g[i][1])), max(g[i][2], g[i][3]));
                if (__all(mx != XCH_EMPTY)) break;
                __builtin_amdgcn_s_sleep(EX_SLEEP);
            }
            STAMP(ston, l * 5 + 2, 7);
#pragma unroll
            for (int i = 0; i < 4; ++i) { acc[i >> 1][(i & 1) * 2] += unpk4(g[i][0], g[i][1]); acc[i >> 1][(i & 1) * 2 + 1] += unpk4(g[i][2], g[i][3]); }
            res_store(acc, xr, piece(wl + W_XA + tm * XP, false, rg * 32, cb, lane), sp + 65536 + (tm * 16 + cb) * 256 + rg * 64, lane);
            dep_signal(c + 8192, (tm * 16 + lane) * 32 + tn, 16, lds, lane);
            STAMP(true, l * 5 + 2, 8 + w);
        }
        u32x4 zz[7];
        {
            PHASE_VARS();
            f32x4 acc[2][14];
            compute_phase<CG3, 2, 14, 7, true, 1, 1, 2>(lds, w * 32, 0, false, lane, acc, [](int) {}, [](int) {} ST_PASS(l * 5 + 3));
            f32x4 s[2], q[2];
#pragma unroll
            for (int m = 0; m < 2; ++m) {
                const f32x4 r = *(const LAS f32x4*)(R + w * 32 + fq * 8 + m * 4), mr = *(const LAS f32x4*)(M + w * 32 + fq * 8 + m * 4);
                s[m] = (f32x4){0.f, 0.f, 0.f, 0.f}; q[m] = s[m];
#pragma unroll
                for (int n = 7; n < 14; ++n) {
                    const f32x4 z = gelu_tanh(acc[m][n] * r + mr * WS[n * 16 + fr]);
                    acc[m][n] = z; s[m] += z; q[m] += z * z;
                }
            }
            float v8[8], v4[4], v2[2];
#pragma unroll
            for (int i = 0; i < 8; ++i) {
                const float lo = (i & 1) ? q[0][i >> 1] : s[0][i >> 1], hi = (i & 1) ? q[1][i >> 1] : s[1][i >> 1];
                v8[i] = ((fr & 8) ? hi : lo) + dpp_mov<0x128>((fr & 8) ? lo : hi);
            }
#pragma unroll
            for (int i = 0; i < 4; ++i) v4[i] = ((fr & 4) ? v8[i + 4] : v8[i]) + dpp_mov<0x141>(dpp_mov<0x1b>((fr & 4) ? v8[i] : v8[i + 4]));
#pragma unroll
            for (int i = 0; i < 2; ++i) v2[i] = ((fr & 2) ? v4[i + 2] : v4[i]) + dpp_mov<0x4e>((fr & 2) ? v4[i] : v4[i + 2]);
            const float pv = ((fr & 1) ? v2[1] : v2[0]) + dpp_mov<0xb1>((fr & 1) ? v2[0] : v2[1]);
            const __amdgpu_buffer_rsrc_t rX = rsrc(ws + W_XCH + l * 262144 + (size_t)(tm * 4 + w) * 4096, 4096);
            __builtin_amdgcn_raw_buffer_store_b32(xch_word(pv), rX, tn * 256 + lane * 4, 0, 16);
            SB0();
            STAMP(ston, l * 5 + 3, 5);
            auto z1 = [&](int n0, int n1) {
                const float r0 = R[w * 32 + perm32(fr)], r1 = R[w * 32 + perm32(16 + fr)], m0 = M[w * 32 + perm32(fr)], m1 = M[w * 32 + perm32(16 + fr)];
#pragma unroll
                for (int n = 0; n < 7; ++n) {
                    if (n < n0 || n >= n1) continue;
                    const f32x4 wv = *(const LAS f32x4*)(WS + n * 16 + fq * 4);
                    zz[n] = pk8(gelu_tanh(acc[0][n] * r0 + wv * m0), gelu_tanh(acc[1][n] * r1 + wv * m1));
                    asm volatile("" : "+v"(zz[n]));
                }
                SB0();
            };
            z1(0, Z1A);
            STAMP(ston, l * 5 + 3, 6);
            f32x4 t;
            for (int spin = 0;; ++spin) {
                u32x4 g[4]; bool ok = true; t = (f32x4){0.f, 0.f, 0.f, 0.f};
#pragma unroll
                for (int i = 0; i < 4; ++i) g[i] = __builtin_amdgcn_raw_buffer_load_b128(rX, lane * 16 + i * 1024, 0, 16);
#pragma unroll
                for (int i = 0; i < 4; ++i) {
                    ok &= (g[i][0] != XCH_EMPTY) & (g[i][1] != XCH_EMPTY) & (g[i][2] != XCH_EMPTY) & (g[i][3] != XCH_EMPTY);
                    t += __builtin_bit_cast(f32x4, g[i]);
                }
                if (__all(ok) || spin > SPIN_MAX) break;
                __builtin_amdgcn_s_sleep(XSL);
            }
            STAMP(ston, l * 5 + 3, 7);
            const float mean0 = fq_sum(t[0]) * (1.0f / FH), mean1 = fq_sum(t[2]) * (1.0f / FH);
            const float r0 = __builtin_amdgcn_rsqf(fmaxf(fq_sum(t[1]) * (1.0f / FH) - mean0 * mean0, 0.f) + LN_EPS);
            const float r1 = __builtin_amdgcn_rsqf(fmaxf(fq_sum(t[3]) * (1.0f / FH) - mean1 * mean1, 0.f) + LN_EPS);
            LAS float* Z = (LAS float*)(lds + L_Z) + w * 64;
            if (lane < 16) { *(LAS f32x2*)(Z + lane * 2) = (f32x2){r0, r1}; *(LAS f32x2*)(Z + 32 + lane * 2) = (f32x2){-mean0 * r0, -mean1 * r1}; }
            WAITCNT(63, 0);
            const f32x4 ra = *(const LAS f32x4*)(Z + fq * 8), rb = *(const LAS f32x4*)(Z + fq * 8 + 4), ma = *(const LAS f32x4*)(Z + 32 + fq * 8), mb = *(const LAS f32x4*)(Z + 32 + fq * 8 + 4);
            const __amdgpu_buffer_rsrc_t rC = rsrc(wl + W_Z2T + (tn * 32 + tm * 2 + (w >> 1)) * 14336, 14336);
#pragma unroll
            for (int n = 0; n < 7; ++n)
                st16(pk8(acc[0][7 + n] * ra + ma, acc[1][7 + n] * rb + mb), rC, lane * 16 + (w & 1) * 1024 + n * 2048);
            SB0();
            z1(Z1A, Z1B);
            dep_signal(c + 2 * 8192, (lane * 16 + tn) * 32 + tm, 16, lds, lane);
            STAMP(true, l * 5 + 3, 8 + w);
            SB0();
            z1(Z1B, 7);
        }
        {
            PHASE_VARS();
            float b[2];
#pragma unroll
            for (int m = 0; m < 2; ++m) b[m] = bf_lo(a->bsp[tm * 128 + w * 32 + perm32(m * 16 + fr)]);
            f32x4 acc[2][7];
            const __amdgpu_buffer_rsrc_t rC = rsrc(wl + W_H + tm * 128 * FH * 2 + w * 4096, 128 * FH * 2 - w * 4096);
            u32x2 tl[2];
            compute_phase<CG4, 2, 7, 7, false, 4, 5, FG4>(lds, w * 32, 0, false, lane, acc, [](int) {}, [&](auto ic) {
#pragma unroll
                for (int m = decltype(ic)::value; m < decltype(ic)::value + FG4; ++m) {
#pragma unroll
                    for (int q = 0; q < 3; ++q)
                        st16(pk8(unpk4(zz[2 * q][m * 2], zz[2 * q][m * 2 + 1]) * (acc[m][2 * q] + b[m]), unpk4(zz[2 * q + 1][m * 2], zz[2 * q + 1][m * 2 + 1]) * (acc[m][2 * q + 1] + b[m])),
                             rC, lane * 16 + (((tn * 3 + q) >> 1) * 16384 + ((tn * 3 + q) & 1) * 1024) + m * 2048);
                    const f32x4 v = unpk4(zz[6][m * 2], zz[6][m * 2 + 1]) * (acc[m][6] + b[m]);
                    tl[m] = (u32x2){pk_bf16(v[0], v[1]), pk_bf16(v[2], v[3])};
                }
            } ST_PASS(l * 5 + 4));
            const auto s0 = __builtin_amdgcn_permlane16_swap(tl[0][0], tl[1][0], false, false), s1 = __builtin_amdgcn_permlane16_swap(tl[0][1], tl[1][1], false, false);
            st16((u32x4){s0[0], s1[0], s0[1], s1[1]}, rC, (fq & 1) * 2048 + (fq >> 1) * 256 + fr * 16 + ((24 + (tn >> 2)) * 16384 + (tn & 3) * 512));
            dep_signal(c + 3 * 8192, (tm * 16 + lane) * 32 + tn, 16, lds, lane);
            STAMP(true, l * 5 + 4, 8 + w);
        }
        {
            PHASE_VARS();
            u32x4 xr[4];
            res_load(piece(wl + W_XA + tm * XP, false, w * 32, tn, lane), xr);
            f32x4 acc[2][4];
            compute_phase<CG5, 2, 4, 4, false, 1, 1, 2>(lds, w * 32, 0, false, lane, acc, [](int) {}, [](int) {} ST_PASS(l * 5 + 5));
            res_store(acc, xr, piece((l ? (char*)a->out : ws + W_XB) + tm * XP, l == 1, w * 32, tn, lane), sp + 131072 + (tm * 16 + tn) * 256 + w * 64, lane);
            dep_signal(c + 4 * 8192, (lane * 16 + tm) * 32 + tn, 16, lds, lane);
            STAMP(true, l * 5 + 5, 8 + w);
        }
    }
    asm volatile(".Lk_main_e1:");
}

extern "C" void kernel_launch(void* const* d_in, const int* in_sizes, int n_in, void* d_out, int out_size, void* d_ws, size_t ws_size, hipStream_t stream) {
    char* ws = (char*)d_ws;
    (void)hipFuncSetAttribute((const void*)k_main, hipFuncAttributeMaxDynamicSharedMemorySize, LDS_TOTAL);
    Args a;
    a.x0 = (const u16*)d_in[0]; a.wsre = (const u16*)d_in[1]; a.wsim = (const u16*)d_in[2]; a.wdre = (const u16*)d_in[3]; a.wdim = (const u16*)d_in[4];
    a.win = (const u16*)d_in[6]; a.wsp = (const u16*)d_in[7]; a.bsp = (const u16*)d_in[8]; a.wout = (const u16*)d_in[9]; a.ws = ws; a.out = (u16*)d_out;
    k_main<<<256, 512, LDS_TOTAL, stream>>>(a);
}
```

```cpp
#include <hip/hip_runtime.h>
#include <hip/hip_bf16.h>
#include <utility>

typedef unsigned short u16;
typedef unsigned long long u64;
typedef short bf16x8 __attribute__((ext_vector_type(8)));
typedef short s16x4 __attribute__((ext_vector_type(4)));
typedef float f32x2 __attribute__((ext_vector_type(2)));
typedef float f32x4 __attribute__((ext_vector_type(4)));
typedef unsigned u32x2 __attribute__((ext_vector_type(2)));
typedef unsigned u32x4 __attribute__((ext_vector_type(4)));
#define LAS __attribute__((address_space(3)))
#define GAS __attribute__((address_space(1)))
#define DEV __device__ __forceinline__

constexpr int waitimm(int vm, int lgkm) { return (vm & 15) | (7 << 4) | ((lgkm & 15) << 8) | ((vm >> 4) << 14); }
#define WAITCNT(vm, lgkm) __builtin_amdgcn_s_waitcnt(waitimm(vm, lgkm))
#define BAR() do { asm volatile("" ::: "memory"); __builtin_amdgcn_s_barrier(); asm volatile("" ::: "memory"); } while (0)
#define SB0() __builtin_amdgcn_sched_barrier(0)

constexpr int S_ = 2048, D_ = 1024, F_ = 3584, FH = 1792;
#define C2 (tm + tn + ((tm + 64) & first))
#define C4 (tm + tn + ((tm + 64) & first))
#define LIM1 (l ? LIM3 : 0)
#define LIM2 (tm * 2 + 2)
#define LIM3 ((tn & 7) * 2 + (tn >> 3) + 1)
#define LIM4 (tm * 2 + 2)
#define LIM5 (((tn & 7) * 3 + 3) >> 1)
#define OWNC(k) (8u * (l * 5 + k - 1))
constexpr int WP1 = 4, WP2 = 3, WP3 = 2, WP4 = 4, WP5 = 4;
constexpr int E2 = 0x000202;
constexpr int BT = 64, BP = 1;
constexpr int VN = 6, EXS = 3, DF = 3, Z1A = 1, Z1B = 3;
#define POLL_SLEEP 2
constexpr float LN_EPS = 1e-5f;
constexpr int SPIN_MAX = 1 << 17;

typedef __bf16 bf16x2 __attribute__((ext_vector_type(2)));
DEV unsigned pk_bf16(float lo, float hi) { return __builtin_bit_cast(unsigned, __builtin_convertvector((f32x2){lo, hi}, bf16x2)); }
DEV float bf_lo(unsigned v) { return __uint_as_float(v << 16); }
DEV float bf_hi(unsigned v) { return __uint_as_float(v & 0xffff0000u); }
DEV __amdgpu_buffer_rsrc_t rsrc(const void* p, unsigned bytes) { return __builtin_amdgcn_make_buffer_rsrc((void*)p, 0, bytes, 0x00020000); }
DEV void st16(u32x4 v, __amdgpu_buffer_rsrc_t r, int off) { __builtin_amdgcn_raw_buffer_store_b128(v, r, off, 0, 16); }
constexpr float GC0 = -2.3022081986f, GC1 = GC0 * 0.044715f;
template <class T> DEV f32x4 gelu_tanh(f32x4 t, T k3, T k1, T sd) {
    const f32x4 a = t * (t * t * k3 + k1);
    const f32x4 d = (f32x4){__builtin_amdgcn_exp2f(a[0]), __builtin_amdgcn_exp2f(a[1]), __builtin_amdgcn_exp2f(a[2]), __builtin_amdgcn_exp2f(a[3])} * sd + sd;
    return t * (f32x4){__builtin_amdgcn_rcpf(d[0]), __builtin_amdgcn_rcpf(d[1]), __builtin_amdgcn_rcpf(d[2]), __builtin_amdgcn_rcpf(d[3])};
}
DEV u32x4 pk8(f32x4 a, f32x4 b) { return (u32x4){pk_bf16(a[0], a[1]), pk_bf16(a[2], a[3]), pk_bf16(b[0], b[1]), pk_bf16(b[2], b[3])}; }
DEV f32x4 unpk4(unsigned a, unsigned b) { return (f32x4){bf_lo(a), bf_hi(a), bf_lo(b), bf_hi(b)}; }
__host__ __device__ constexpr int perm32(int slot) { return 8 * ((slot & 15) >> 2) + 4 * ((slot >> 4) & 1) + (slot & 3); }
__host__ __device__ constexpr int inv32(int row) { return 16 * ((row >> 2) & 1) + 4 * ((row >> 3) & 3) + (row & 3); }
DEV float fq_sum(float a) {
    const auto p = __builtin_amdgcn_permlane16_swap(__float_as_uint(a), __float_as_uint(a), false, false);
    a = __uint_as_float(p[0]) + __uint_as_float(p[1]);
    const auto q = __builtin_amdgcn_permlane32_swap(__float_as_uint(a), __float_as_uint(a), false, false);
    return __uint_as_float(q[0]) + __uint_as_float(q[1]);
}
template <int CTRL> DEV float dpp_mov(float a) { return __builtin_bit_cast(float, __builtin_amdgcn_update_dpp(0, __builtin_bit_cast(int, a), CTRL, 0xf, 0xf, true)); }
template <int CTRL> DEV float dpp_add(float a) { return a + __builtin_bit_cast(float, __builtin_amdgcn_update_dpp(0, __builtin_bit_cast(int, a), CTRL, 0xf, 0xf, true)); }
DEV float row16_sum(float a) { a = dpp_add<0x128>(a); a = dpp_add<0x124>(a); a = dpp_add<0x122>(a); return dpp_add<0x121>(a); }
constexpr unsigned XCH_EMPTY = 0xffffffffu;
DEV unsigned xch_word(float v) { const unsigned u = __float_as_uint(v); return u == XCH_EMPTY ? 0x7fc00000u : u; }

DEV int opaque_v(int v) { asm volatile("" : "+v"(v)); return v; }
DEV int opaque_s(int v) { asm volatile("" : "+s"(v)); return v; }
template <class T> DEV T* early(T* p) { asm volatile("" : "+s"(p)); return p; }
template <class T> DEV const LAS T* early_v(const LAS T* p) { asm volatile("" : "+v"(p)); return p; }
template <class F, int... I> DEV void static_for_impl(F&& f, std::integer_sequence<int, I...>) { (f(std::integral_constant<int, I>{}), ...); }
template <int N, class F> DEV void static_for(F&& f) { static_for_impl(f, std::make_integer_sequence<int, N>{}); }

template <int BM_, int BN_, int NS_, int NT_, int LDP_, int PBASE_, int QBASE_, int ROTP_, int ROTQ_, int OWN_> struct Cfg {
    static constexpr int OWN = OWN_;
    static constexpr int BM = BM_, BN = BN_, NS = NS_, NT = NT_, LDP = LDP_, PBASE = PBASE_, QBASE = QBASE_, ROTP = ROTP_, ROTQ = ROTQ_;
    static constexpr bool WISP = LDP_ != 0;
    static constexpr int PB = BM * 128, QB = BN * 128;
    static constexpr int LP = BM / 32, LQ = BN / 32;
    static constexpr int LW = WISP ? LP : LQ, LA = WISP ? LQ : LP;
};
constexpr int L_TIN = 131072, L_TOUT = 49152;
constexpr int L_R = 159744, L_M = L_R + 512, L_K3 = L_M + 512, L_K1 = L_K3 + 512, L_WD = L_K1 + 512, L_WS = L_WD + 512, L_JUNK = L_WS + 1024, L_SYNC = L_JUNK + 256, L_TAG = L_SYNC + 48, LDS_TOTAL = L_SYNC + 80;

template <class C, int WPRE, int DA, int DW, int N0, int N1, int NX> constexpr int ring_wait(int t) {
    int n = 0;
    auto ev = [&](int st, int cnt) { if (st == t + 1) n = 0; else n += cnt; };
    constexpr int O = C::OWN;
    ev(O, C::LA); if (WPRE < O + 2) ev(O + 1, C::LW); ev(O + 1, C::LA);
    if (t < O) return n;
    for (int s = O + 2; s < O + DW; ++s) { if (s >= WPRE) ev(s, C::LW); if (s < O + DA) ev(s, C::LA); }
    ev(-9, NX >> 24);
    for (int u = O; u < t; ++u) {
        if (u == C::NT - 1 - DW) ev(-9, N0 & 255);
        if (DA < DW && u + DA < C::NT) ev(u + DA, C::LA);
        if (u + DW < C::NT && u + DW >= WPRE) ev(u + DW, C::LW);
        if (DA == DW && u + DA < C::NT) ev(u + DA, C::LA);
        if (u < C::NT - 1 - DW) ev(-9, N0 >> 8 & 255);
        if (u < O + (N0 >> 16)) ev(-9, 4);
        if (u == C::NT - 1 - DA) ev(-9, N1);
        if (u >= C::NT - 4) ev(-9, (NX >> (u - C::NT + 4) * 8) & 255);
    }
    return n;
}
template <class C, int WPRE, int DA, int DW, int N0, int NX> constexpr int ring_head() {
    int h = C::OWN + 1;
    for (int t = C::OWN; t < C::NT / 2; ++t) if (ring_wait<C, WPRE, DA, DW, N0, 0, NX>(t) != ring_wait<C, WPRE, DA, DW, N0, 0, NX>(C::NT / 2) || t + DW < WPRE) h = t + 1;
    return h;
}
template <class C> DEV u64 k_order(int cls) {
    const bool far = cls >= 64;
    const int i0 = (cls + 3) & (far ? 7 : 3);
    u64 d = far ? 0x0016001600160016ull >> (i0 > 5 ? 16 : 0) : 0x0707070707070707ull >> (64 - 8 * ((C::NT + 2 - i0) >> 2));
    if (i0 < C::OWN || (C::LDP == D_ && (cls & 32))) d &= ~0xffull;
    return d << 2 * i0 ^ 0x5555555555555555ull;
}
template <class C> DEV int k_block(u64 ord, int lim, int st) {
    const int b = st - 1 + (int)((unsigned)(ord >> (2 * st)) & 3);
    return C::OWN && b < lim ? b - C::OWN : b;
}
template <int NT> DEV int row_offset(int lw, int lane) { return perm32(lw * 8 + (lane >> 3)) * NT * 128 + ((lane & 7) ^ (((lw & 1) * 4 + (lane >> 4)) & 7)) * 16; }
DEV void own_add(LAS unsigned char* lds, int off = 8) { asm volatile("s_mov_b64 exec, 1\n\ts_waitcnt lgkmcnt(0)\n\tds_add_u32 %0, %1\n\ts_mov_b64 exec, -1" :: "v"((unsigned)(size_t)(lds + L_SYNC + off)), "v"(1u) : "memory"); }
DEV void lds_until(LAS unsigned char* lds, int off, unsigned n) {
    while ((unsigned)__builtin_amdgcn_readfirstlane(*(volatile LAS unsigned*)(lds + L_SYNC + off)) < n) __builtin_amdgcn_s_sleep(0);
    asm volatile("" ::: "memory");
}
struct Ahead { const void* W; int v; unsigned kt; };
template <class C> DEV Ahead ahead(const void* W, int cls, int lim, int lw, int lane) {
    const u64 ord = k_order<C>(cls);
    unsigned kt = 0;
#pragma unroll
    for (int s = 0; s < 4; ++s) kt |= (s < C::OWN ? lim - C::OWN + s : k_block<C>(ord, lim, s)) << 8 * s;
    return Ahead{early(W), opaque_v(C::WISP ? row_offset<C::NT>(lw, lane) : lw * 1024 + lane * 16), (unsigned)opaque_s(kt)};
}
template <class C> DEV void weights(LAS unsigned char* lds, const Ahead& h, unsigned on, int lw, int s0, int s1) {
    lw = opaque_s(lw);
    const __amdgpu_buffer_rsrc_t r = rsrc(h.W, C::NT * (C::WISP ? C::PB : C::QB) & on);
#pragma unroll
    for (int s = s0; s < s1; ++s) {
        LAS unsigned char* dst = lds + (C::WISP ? C::PBASE + (s + C::ROTP) % C::NS * C::PB : C::QBASE + (s + C::ROTQ) % C::NS * C::QB) + lw * 1024;
        const int kt = h.kt >> 8 * s & 255;
#pragma unroll
        for (int i = 0; i < C::LW; ++i)
            __builtin_amdgcn_raw_ptr_buffer_load_lds(r, (LAS void*)(dst + i * 4096), 16, h.v, C::WISP ? i * C::NT * 4096 + kt * 128 : i * 4096 + kt * C::QB, 0, C::WISP ? 0 : 16);
    }
}
template <class C, int WPRE, int PRE, int DA, int DW, int N0, int N1, int NX, class Dep, class Tail>
DEV void loader_phase(LAS unsigned char* lds, const void* P, const void* Q, bool qrow, int cls, int lim, int lw, int lane, unsigned drained, int& level, u64 pace, int PN, Dep&& dep, Tail&& tail) {
    constexpr int O = C::OWN;
    static_assert(O <= 2 && PRE >= O && WPRE > O, "");
    constexpr int NS = C::NS, NT = C::NT, TH = ring_head<C, WPRE, DA, DW, N0 & 0xffff, NX>(), T0 = TH > O + (N0 >> 16) ? TH : O + (N0 >> 16), T1 = NT - 1 - DW;
    static_assert(C::PBASE + NS * C::PB <= L_R && C::QBASE + NS * C::QB <= L_R && (NS == 3 ? C::ROTQ == C::ROTP + 1 : C::ROTQ == C::ROTP && C::ROTP % 2 == 0) && DW >= 3 && NT >= 2 * NS && C::BM % 32 == 0 && DA >= 2 && DA <= DW && DW <= NS && WPRE <= NS && (NS == 4 || (DA == NS && DW == NS)), "");
    lane = opaque_v(lane); lw = opaque_s(lw);
    __amdgpu_buffer_rsrc_t rP = rsrc(early(P), NT * C::PB), rQ = rsrc(early(Q), NT * C::QB);
    const int vrow = row_offset<NT>(lw, lane), vfrag = lw * 1024 + lane * 16;
    const int vP = opaque_v(C::WISP ? vrow : vfrag), vQ = opaque_v(qrow ? vrow : vfrag), sq = qrow ? NT * 4096 : 4096, kq = qrow ? 128 : C::QB;
    auto issueP = [&](int slot, int kt) {
        LAS unsigned char* dst = lds + C::PBASE + slot * C::PB + lw * 1024;
#pragma unroll
        for (int i = 0; i < C::LP; ++i)
            __builtin_amdgcn_raw_ptr_buffer_load_lds(rP, (LAS void*)(dst + i * 4096), 16, vP, C::WISP ? i * NT * 4096 + kt * 128 : i * 4096 + kt * C::PB, 0, C::WISP ? 0 : 16);
    };
    auto issueQ = [&](int slot, int kt) {
        if (NS == 3) slot = slot == 2 ? 0 : slot + 1;
        LAS unsigned char* dst = lds + C::QBASE + slot * C::QB + lw * 1024;
#pragma unroll
        for (int j = 0; j < (C::BN + 31) / 32; ++j)
            if (j < C::LQ || j * 4 + lw < C::BN / 8) __builtin_amdgcn_raw_ptr_buffer_load_lds(rQ, (LAS void*)(dst + j * 4096), 16, vQ, j * sq + kt * kq, 0, 16);
    };
    u64 ord = k_order<C>(cls);
    asm volatile("" : "+s"(ord));
    if (C::OWN) lim = opaque_s(lim);
    auto order = [&](int st) { return k_block<C>(ord, lim, st); };
    auto at = [](int st) { return (st + C::ROTP) % NS; };
    auto issueW = [&](int slot, int st) { if (C::WISP) issueP(slot, order(st)); else issueQ(slot, order(st)); };
    auto issueA = [&](int slot, int st) { if (C::WISP) issueQ(slot, order(st)); else issueP(slot, order(st)); };
    if (drained) {
        auto drain = [&]() { while ((unsigned)__builtin_amdgcn_readfirstlane(*(volatile LAS unsigned*)(lds + L_SYNC + 16)) < drained) __builtin_amdgcn_s_sleep(1); };
        if constexpr (O != 0) { WAITCNT((PRE - O) * C::LW + (NT == 32 && C::BN == 128 ? (E2 >> 8 & 255) + (E2 >> 16) : 0), 15); own_add(lds); }
        drain();
#pragma unroll
        for (int s = PRE; s < WPRE; ++s) issueW(at(s), s);
        const int k0 = opaque_s(order(O)), k1 = opaque_s(order(O + 1));
        if (C::WISP) asm volatile("" : "+s"(rQ)); else asm volatile("" : "+s"(rP));
        const unsigned x = dep();
        if (C::WISP) issueQ(at(O), k0); else issueP(at(O), k0);
        if constexpr (WPRE < O + 2) { if (C::WISP) issueP(at(O + 1), k1); else issueQ(at(O + 1), k1); }
        if (C::WISP) issueQ(at(O + 1), k1); else issueP(at(O + 1), k1);
        if (lw == 0) {
            level = 0;
#pragma unroll
            for (int k = 1; k < 4; ++k) if ((x >> (6 + 6 * k) & 63) >= (NT == 14 ? 3 : 6)) level = k;
            if ((x >> 6 & 63) >= (NT == 14 ? 6 : 11)) level = min(level + 1, 3);
            asm volatile("ds_write_b32 %0, %1\n\ts_waitcnt lgkmcnt(0)" :: "v"((unsigned)(size_t)(lds + L_TAG + 8)), "v"(0x1041001u & ((4096u << 6 * level) - 1)) : "memory");
        }
        WAITCNT((ring_wait<C, WPRE, DA, DW, N0, N1, NX>(O - 1)), 15);
        BAR();
#pragma unroll
        for (int s = O + 2; s < O + DW; ++s) { if (s >= WPRE) issueW(at(s), s); if (s < O + DA) issueA(at(s), s); }
        tail(std::integral_constant<int, -1>{});
    } else {
        issueW(1, 1); issueA(1, 1);
        WAITCNT(8, 15);
        BAR();
        issueW(2, 2); issueA(2, 2); issueW(3, 3); issueA(3, 3);
        WAITCNT(16, 15);
        BAR();
        issueW(0, 4);
    }
    const int pt = (int)(pace >> (level * 16)) & 0xffff, pn = (lw == 0 && pt) ? PN : 0;
    unsigned due = 0;
    auto hold = [&](int t) {
        unsigned now = (unsigned)__builtin_amdgcn_s_memrealtime() * 10u;
        if (t == O) { due = now; return; }
        due += pt;
        if ((int)(now - due) > 100) due = now - 100;
        while ((int)(now - due) < 0) now = (unsigned)__builtin_amdgcn_s_memrealtime() * 10u;
    };
    auto fetch = [&](int t, int sl, bool w, bool a) {
        if (DA < DW && a) issueA((t + DA + C::ROTP) & 3, t + DA);
        if (w) issueW(NS == 4 ? (t + DW + C::ROTP) & 3 : sl, t + DW);
        if (DA == DW && a) issueA(NS == 4 ? (t + DA + C::ROTP) & 3 : sl, t + DA);
    };
    static_for<T0 - O>([&](auto ic) {
        constexpr int t = O + decltype(ic)::value;
        if (pn) hold(t);
        if (C::LDP == D_ && !drained) WAITCNT(12 + (N0 >> 16 ? (t - O) * 4 : 0), 15); else WAITCNT((ring_wait<C, WPRE, DA, DW, N0, N1, NX>(t)), 15);
        BAR();
        fetch(t, at(t), t + DW >= WPRE, true);
        tail(std::integral_constant<int, -2>{});
        static_assert((N0 >> 16) <= T0 - O, "");
        if constexpr (t - O < (N0 >> 16)) tail(std::integral_constant<int, -10 - (t - O)>{});
    });
    int t = T0, sl = at(T0);
    if constexpr (NS == 4 && DA == 3 && DW == 4) {
        auto raw = [&](bool p, int d, int so, auto&& then) {
#pragma unroll
            for (int i = 0; i < 4; ++i) {
                if (p) __builtin_amdgcn_raw_ptr_buffer_load_lds(rP, (LAS void*)(size_t)(d + i * 4096), 16, vP, so + i * (C::WISP ? NT * 4096 : 4096), 0, C::WISP ? 0 : 16);
                else if (i < C::LQ || lw < 2) __builtin_amdgcn_raw_ptr_buffer_load_lds(rQ, (LAS void*)(size_t)(d + i * 4096), 16, vQ, so + i * sq, 0, 16);
                SB0(); then(i); SB0();
            }
        };
        auto dstP = [&](int st) { return C::PBASE + ((st + C::ROTP) & 3) * C::PB + lw * 1024; };
        auto dstQ = [&](int st) { return C::QBASE + ((st + C::ROTQ) & 3) * C::QB + lw * 1024; };
        constexpr int kp = C::WISP ? 128 : C::PB;
        int da = opaque_s(C::WISP ? dstQ(T0 + 3) : dstP(T0 + 3)), sa = opaque_s(order(T0 + 3) * (C::WISP ? kq : kp));
#pragma unroll 1
        do {
            if (t < pn) hold(t);
            WAITCNT((ring_wait<C, WPRE, DA, DW, N0, N1, NX>(NT / 2)), 15);
            BAR(); SB0();
            int b, dw, sw;
            raw(!C::WISP, da, sa, [&](int i) {
                if (i == 0) b = opaque_s(t + 3 + (int)((unsigned)(ord >> (2 * t + 8)) & 3));
                if (i == 1) { b = opaque_s(C::OWN && b < lim ? b - C::OWN : b); sw = opaque_s(b * (C::WISP ? kp : kq)); }
                if (i == 2) dw = opaque_s(C::WISP ? dstP(t) : dstQ(t));
            });
            raw(C::WISP, dw, sw, [&](int i) {
                if (i == 0) da = opaque_s(C::WISP ? dstQ(t) : dstP(t));
                if (i == 1) { sa = opaque_s(b * (C::WISP ? kq : kp)); t = opaque_s(t + 1); }
            });
            tail(std::integral_constant<int, -2>{});
        } while (t < T1);
    } else
#pragma unroll 1
    for (; t < T1; ++t) {
        if (t < pn) hold(t);
        WAITCNT((ring_wait<C, WPRE, DA, DW, N0, N1, NX>(NT / 2)), 15);
        BAR();
        fetch(t, sl, true, true);
        tail(std::integral_constant<int, -2>{});
        sl = (sl + 1 == NS) ? 0 : sl + 1;
    }
    static_for<DW>([&](auto ic) {
        constexpr int u = T1 + decltype(ic)::value;
        WAITCNT((ring_wait<C, WPRE, DA, DW, N0, N1, NX>(u)), 15);
        BAR(); SB0();
        if constexpr (u == T1) tail(std::integral_constant<int, 0>{});
        fetch(u, at(u), u + DW < NT, u + DA < NT);
        if constexpr (u == NT - 1 - DA) tail(std::integral_constant<int, 1>{});
        if constexpr (u == T1) { SB0(); tail(std::integral_constant<int, -5>{}); }
        if constexpr (u == NT - 3) { SB0(); tail(std::integral_constant<int, 2>{}); WAITCNT(63, 0); }
        if constexpr (u >= NT - 4) { SB0(); tail(std::integral_constant<int, u - NT + 8>{}); }
    });
    tail(std::integral_constant<int, 3>{});
}

template <class C, int MT, int NTL, int NSWAP, bool ROLL, int RNUM, int RDEN, bool QROW, int HT, bool ZERO = true, bool COLS = false, class Hook, class Fin>
DEV void compute_phase(LAS unsigned char* lds, unsigned own, int prow0, int qrow0, bool qrow, int lane, f32x4 (&acc)[MT][NTL], Hook&& hook, Fin&& fin) {
    constexpr int NR = MT + NTL, NM = MT * NTL, NT = C::NT;
    const int fr = lane & 15, fq = lane >> 4;
    LAS unsigned char *pa[2], *pb[2];
#pragma unroll
    for (int k = 0; k < 2; ++k) {
        const int orow = fr * 128 + ((k * 4 + fq) ^ ((fr >> 1) & 7)) * 16, ofrag = k * 1024 + lane * 16;
        pa[k] = lds + C::PBASE + C::ROTP * C::PB + prow0 * 128 + (C::WISP ? orow : ofrag);
        pb[k] = lds + C::QBASE + C::ROTQ * C::QB + qrow0 * 128 + (qrow ? orow : ofrag);
        if (k == 0 || C::WISP) asm volatile("" : "+v"(pa[k]));
        if (k == 0 || QROW) asm volatile("" : "+v"(pb[k]));
    }
    auto rdp = [&](int k, int off) { return *(const LAS bf16x8*)(C::WISP ? pa[k] + off : pa[0] + (off + k * 1024)); };
    auto rdq = [&](int k, int off) { return *(const LAS bf16x8*)(QROW ? pb[k] + off : pb[0] + (off + k * 1024)); };
    constexpr int NADR = 2 + C::WISP + QROW;
    auto move = [&](int i, int dp, int dq) {
        if (i == 0) { pa[0] += dp; asm volatile("" : "+v"(pa[0])); }
        else if (i == 1) { pb[0] += dq; asm volatile("" : "+v"(pb[0])); }
        else if (i == 2 && C::WISP) { pa[1] += dp; asm volatile("" : "+v"(pa[1])); }
        else { pb[1] += dq; asm volatile("" : "+v"(pb[1])); }
    };
#pragma unroll
    for (int m = 0; m < MT; ++m)
#pragma unroll
        for (int n = 0; n < NTL; ++n) { if (ZERO) acc[m][n] = (f32x4){0.f, 0.f, 0.f, 0.f}; asm volatile("" : "+v"(acc[m][n])); }
    auto mfma = [&](int m, int n, const bf16x8& a, const bf16x8& b) {
        if (n >= NSWAP) asm volatile("v_mfma_f32_16x16x32_bf16 %0, %1, %2, %0" : "+v"(acc[m][n]) : "v"(a), "v"(b));
        else asm volatile("v_mfma_f32_16x16x32_bf16 %0, %1, %2, %0" : "+v"(acc[m][n]) : "v"(b), "v"(a));
    };
    auto settle = [&](int m0, int cnt) {
        asm volatile("s_nop 15" ::: "memory");
#pragma unroll
        for (int m = 0; m < MT; ++m)
#pragma unroll
            for (int n = 0; n < NTL; ++n) if (m >= m0 && m < m0 + cnt) asm volatile("" : "+v"(acc[m][n]));
    };
    if (C::OWN && own) lds_until(lds, 8, own); else BAR();
    if constexpr (!ROLL) {
        static_assert(C::NS == 4 && NT % 2 == 0 && HT % 2 == 0, "");
        bf16x8 fa[4][MT], fb[4][NTL];
        int dp = (2 - 2 * C::ROTP) * C::PB, dq = (2 - 2 * C::ROTP) * C::QB, t = 0;
        auto rd1 = [&](int set, int k, int i) {
            if (i == 0) fa[set][0] = rdp(k, (set >> 1) * C::PB);
            else if (i <= NTL) fb[set][i - 1] = rdq(k, (set >> 1) * C::QB + (i - 1) * 2048);
            else fa[set][i - NTL] = rdp(k, (set >> 1) * C::PB + (i - NTL) * 2048);
        };
        constexpr int RL = (2 * NR * RDEN + RNUM - 1) / RNUM;
        static_assert(RL + NADR <= 2 * NM, "");
        auto stg = [&](int set, bool rd) {
#pragma unroll
            for (int i = 0; i < 2 * NM; ++i) {
                const int j = i >> 1, st = set + ((i ^ j) & 1), m = j / NTL, n = (m & 1) ? NTL - 1 - j % NTL : j % NTL;
                mfma(m, n, fa[st][m], fb[st][n]); SB0();
                if (rd) {
#pragma unroll
                    for (int r = (i * RNUM + RDEN - 1) / RDEN; r < ((i + 1) * RNUM + RDEN - 1) / RDEN; ++r) if (r < 2 * NR) rd1((set ^ 2) + r / NR, r / NR, r % NR);
                    if (set == 0 && i >= RL && i < RL + NADR) move(i - RL, dp, dq);
                    if (set == 2 && i == RL) { dp = -dp; dq = -dq; t += 2; asm volatile("" : "+s"(dp), "+s"(dq), "+s"(t)); }
                    SB0();
                }
            }
        };
        static_assert((2 * NM * RNUM + RDEN - 1) / RDEN >= 2 * NR, "");
#pragma unroll
        for (int r = 0; r < 2 * NR; ++r) rd1(r / NR, r / NR, r % NR);
        hook(0); SB0();
#pragma unroll 1
        for (int seg = HT ? 0 : 1; seg < 2; ++seg) {
            const int e = seg ? NT - 2 : HT;
#pragma unroll 1
            while (t < e) {
                WAITCNT(63, 0);
                if (C::OWN == 2) asm volatile("s_cmp_eq_u32 %0, 0\n\ts_cbranch_scc1 1f\n\ts_barrier\n1:" :: "s"(t) : "scc", "memory"); else BAR();
                SB0();
                stg(0, true);
                WAITCNT(63, 0); BAR(); SB0();
                stg(2, true);
            }
            if (HT && seg == 0) hook(1);
            SB0();
        }
        hook(2); SB0();
        WAITCNT(63, 0); BAR(); SB0();
        auto rows = [&](int set, int m, int r0) {
#pragma unroll
            for (int i = 0; i < 2 * NTL; ++i) {
                const int st = set + ((i ^ (i >> 1)) & 1), r = r0 + i, j = r >> 1;
                mfma(m, i >> 1, fa[st][m], fb[st][i >> 1]); SB0();
                if (r < 2 * NR) { rd1(2 + (r & 1), r & 1, j == 0 ? NR - 1 : j <= NTL ? j : j == NR - 1 ? 0 : NR + NTL - 1 - j); SB0(); }
            }
        };
        if constexpr (COLS) {
            auto pair = [&](int set, int m, int n0, int r0) {
#pragma unroll
                for (int i = 0; i < 4; ++i) {
                    const int st = set + ((i ^ (i >> 1)) & 1), n = n0 + ((i >> 1) ^ (m & 1)), r = r0 + i, j = r >> 1;
                    mfma(m, n, fa[st][m], fb[st][n]); SB0();
                    if (r < 2 * NR) { rd1(2 + (r & 1), r & 1, j == 0 ? 7 : j < 3 ? 5 - j : j < 5 ? 9 - j : j - 5); SB0(); }
                }
            };
#pragma unroll
            for (int m = 3; m >= 0; --m) pair(0, m, 2, 12 - 4 * m);
            pair(2, 3, 2, 99);
            static_for<4>([&](auto ic) {
                constexpr int m = 3 - decltype(ic)::value;
                if constexpr (m > 0) pair(2, m - 1, 2, 99); else pair(2, 0, 0, 99);
                asm volatile("s_nop 3" : "+v"(acc[m][2]), "+v"(acc[m][3]));
                fin(std::integral_constant<int, m>{}); SB0();
            });
#pragma unroll
            for (int m = 0; m < 4; ++m) { if (m) pair(2, m, 0, 99); pair(0, m, 0, 99); }
            settle(0, MT);
            return;
        }
#pragma unroll
        for (int m = MT - 1; m >= MT / 2; --m) rows(0, m, (MT - 1 - m) * 2 * NTL);
        hook(3); SB0();
        static_for<MT>([&](auto ic) {
            constexpr int m = MT - 1 - decltype(ic)::value;
            if constexpr (m < MT / 2) rows(0, m, 2 * NR);
            rows(2, m, m == MT - 1 ? NM : 2 * NR);
            settle(m, 1);
            fin(std::integral_constant<int, m>{}); SB0();
        });
    } else {
        static_assert(MT == 2 && NADR == 2 && NTL == 2 * NSWAP && DF <= C::NS, "");
        constexpr int G = 8;
        bf16x8 fa[2][2][2], fb[NSWAP][2];
        int sl = C::ROTP, dp, dq, t = 0;
        auto unit = [&](int n0, int u, LAS unsigned char* const& q, int qo, LAS unsigned char* const* p, int po, bool bar, int job) {
#pragma unroll
            for (int j = 0; j < NM; ++j) {
                const int n = j >> 2, m = (j >> 1) & 1, k = (j ^ m) & 1;
                if (bar && j == G) { WAITCNT(63, 0); BAR(); SB0(); }
                mfma(m, n0 + n, fa[u][m][k], fb[n][k]); SB0();
                const int g0 = bar ? G : 0, np = p ? 4 : 0;
                if (p && j >= g0 && j < g0 + 4) fa[u ^ 1][(j - g0) >> 1][(j - g0) & 1] = *(const LAS bf16x8*)(*p + (po + (j - g0) * 1024));
#pragma unroll
                for (int r = 0; r < NTL; ++r) {
                    const int lo = (r >> 1) * 4 + 2 + (r & 1), at = g0 + np + r > lo ? g0 + np + r : lo;
                    if (qo >= 0 && at == j) fb[r >> 1][~r & 1] = *(const LAS bf16x8*)(q + (qo + (r >> 1) * 2048 + (~r & 1) * 1024));
                }
                if (job == 1 && j == 4) { sl = sl + 1 == C::NS ? 0 : sl + 1; asm volatile("" : "+s"(sl)); }
                if (job == 1 && j == 8) { dp = sl ? C::PB : (1 - C::NS) * C::PB; dq = sl != 2 ? C::QB : (1 - C::NS) * C::QB; asm volatile("" : "+s"(dp), "+s"(dq)); }
                if (job == 1 && j == 12) { ++t; asm volatile("" : "+s"(t)); }
                if (job == 2 && (j == 2 || j == 4)) move(j / 2 - 1, dp, dq);
                if (job == 3) hook(10 + j);
                SB0();
            }
        };
        auto stage = [&](int u) { unit(0, u, pb[0], NSWAP * 2048, nullptr, 0, false, 1); unit(NSWAP, u, pb[0], 0, &pa[0], 0, true, 2); };
#pragma unroll
        for (int r = 0; r < 4; ++r) fa[0][r >> 1][r & 1] = rdp(0, r * 1024);
#pragma unroll
        for (int r = 0; r < NTL; ++r) fb[r >> 1][r & 1] = rdq(0, r * 1024);
        SB0();
        constexpr int S = NT - DF;
        fin(-1); SB0();
        if (S & 1) stage(0);
#pragma unroll 1
        while (t < S) { fin(t); SB0(); stage(S & 1); stage(~S & 1); }
        constexpr int SS = (S + C::ROTQ) % C::NS;
        LAS unsigned char *qa = pa[0] - (S + C::ROTP) % C::NS * C::PB, *qb[2] = {pb[0] - SS * C::QB, pb[0] - (SS - 1) * C::QB};
        asm volatile("" : "+v"(qa), "+v"(qb[0]), "+v"(qb[1]));
        unit(0, S & 1, pb[0], NSWAP * 2048, nullptr, 0, false, 0);
        auto done = [&](int n0) {
            asm volatile("s_nop 15" ::: "memory");
#pragma unroll
            for (int j = 0; j < NTL; ++j) asm volatile("" : "+v"(acc[j & 1][n0 + (j >> 1)]));
        };
#pragma unroll
        for (int i = 0; i < 2 * DF - 1; ++i) {
            const int s = i < DF - 1 ? S + 1 + i : S + 2 + i - DF, n = i < DF - 1 ? NSWAP : 0, sn = (s + C::ROTQ) % C::NS;
            const bool more = i < 2 * DF - 2 && i != DF - 1;
            const int qo = (sn ? sn - 1 : 0) * C::QB + n * 2048, po = (s + C::ROTP) % C::NS * C::PB;
            unit(i < DF ? NSWAP : 0, (S + i) & 1, qb[sn ? 1 : 0], more ? qo : -1, more ? &qa : nullptr, po, i < DF - 1, i == DF - 1 ? 3 : 0);
            if (i == DF - 1) {
                hook(4); SB0();
#pragma unroll
                for (int r = 0; r < 4; ++r) fa[~(S + i) & 1][r >> 1][r & 1] = *(const LAS bf16x8*)(qa + (po + r * 1024));
#pragma unroll
                for (int r = 0; r < NTL; ++r) fb[r >> 1][r & 1] = *(const LAS bf16x8*)(qb[sn ? 1 : 0] + (qo + r * 1024));
                SB0();
            }
        }
        done(0);
    }
}

DEV u64 launch_tag(LAS unsigned char* lds) { return *(volatile LAS u64*)(lds + L_TAG); }
DEV const GAS unsigned* box_word(const unsigned* box) { const GAS unsigned* p = (const GAS unsigned*)box; asm volatile("" : "+v"(p)); return p; }
#define PS 2
#define STR_(x) #x
#define STR(x) STR_(x)
#define POLL_OP(r) "global_atomic_add " r ", %2, v236, off sc0\n\t"
#define POLL_LD(r) "s_sleep " STR(PS) "\n\t" POLL_OP(r)
#define POLL_CK(n, r) "s_waitcnt vmcnt(" n ")\n\tv_and_b32 v238, 63, " r "\n\tv_readfirstlane_b32 %0, " r "\n\tv_cmp_gt_u32 vcc, %3, v238\n\ts_cbranch_vccz 2f\n\t"
template <int NP> DEV unsigned box_poll(const GAS unsigned* p, unsigned need) {
    unsigned x; int n;
    asm volatile("s_mov_b64 exec, %4\n\tv_mov_b32 v236, 0\n\ts_mov_b32 %1, 0x20000\n1:\n\t" POLL_OP("v240") POLL_CK("0", "v240") "s_sleep " STR(POLL_SLEEP) "\n\t"
                 "s_sub_u32 %1, %1, 1\n\ts_cbranch_scc0 1b\n2:\n\ts_mov_b64 exec, -1"
                 : "=&s"(x), "=&s"(n) : "v"(p), "v"(need), "n"((1 << NP) - 1) : "v236", "v238", "v240", "vcc", "scc", "memory");
    return x;
}
template <int NP> DEV unsigned box_poll4(const GAS unsigned* p, unsigned need) {
    unsigned x; int n;
    asm volatile("s_mov_b64 exec, %4\n\tv_mov_b32 v236, 0\n\t" POLL_OP("v240") POLL_LD("v241") POLL_LD("v242") "s_mov_b32 %1, 0x10000\n1:\n\t"
                 POLL_LD("v243") POLL_CK("3", "v240") POLL_LD("v240") POLL_CK("3", "v241") POLL_LD("v241") POLL_CK("3", "v242") POLL_LD("v242") POLL_CK("3", "v243")
                 "s_sub_u32 %1, %1, 1\n\ts_cbranch_scc0 1b\n2:\n\ts_mov_b64 exec, -1"
                 : "=&s"(x), "=&s"(n) : "v"(p), "v"(need), "n"((1 << NP) - 1) : "v236", "v238", "v240", "v241", "v242", "v243", "vcc", "scc", "memory");
    return x;
}
template <int NP = 1> DEV unsigned dep_wait(const GAS unsigned* box, unsigned need, LAS unsigned char* lds, unsigned epoch, int lw) {
    volatile LAS unsigned* flag = (volatile LAS unsigned*)(lds + L_SYNC);
    unsigned x = 0;
    if (lw == 0) {
        x = box_poll4<NP>(box, need);
        *flag = epoch;
    } else {
        while ((unsigned)__builtin_amdgcn_readfirstlane(*flag) < epoch) __builtin_amdgcn_s_sleep(0);
    }
    asm volatile("" ::: "memory");
    return x;
}
template <int CNT = 16, int VM = 0> DEV unsigned dep_signal(unsigned* boxes, int tile, int ncons, int own, LAS unsigned char* lds, int lane, unsigned slow = ~0u, const unsigned* since = nullptr) {
    u64 t0, t1;
    unsigned inc = CNT == 16 ? *(volatile LAS unsigned*)(lds + L_TAG + 8) : 1u;
    GAS unsigned* p = (GAS unsigned*)boxes + tile * BT;
    unsigned old;
    asm volatile("s_memrealtime %0\n\ts_mov_b64 exec, 1\n\ts_waitcnt vmcnt(%7)\n\tds_add_rtn_u32 %2, %5, %6\n\ts_memrealtime %1\n\ts_mov_b64 exec, -1\n\ts_waitcnt lgkmcnt(0)"
                 : "=&s"(t0), "=&s"(t1), "=&v"(old), "+v"(p), "+v"(inc) : "v"((unsigned)(size_t)(lds + L_SYNC + CNT)), "v"(1u), "n"(VM) : "memory");
    if ((__builtin_amdgcn_readfirstlane(old) & 3) == 3 && lane < ncons && tile != own)
        (void)__hip_atomic_fetch_add(p, inc + ((unsigned)t1 - (since ? *since : (unsigned)t0) > slow ? 64u : 0u), __ATOMIC_RELAXED, __HIP_MEMORY_SCOPE_AGENT);
    return (__builtin_amdgcn_readfirstlane(old) & 3) == 3 ? (unsigned)t1 - (since ? *since : (unsigned)t0) : 0u;
}
DEV void touch(LAS unsigned char* lds, const u16* p, unsigned bytes, int off) {
    __builtin_amdgcn_raw_ptr_buffer_load_lds(rsrc(p, bytes), (LAS void*)(lds + L_JUNK), 4, off, 0, 0, 0);
}

template <int STRIDE> DEV u32x4 read_tr(LAS unsigned char* p) {
    const u32x2 a = __builtin_bit_cast(u32x2, __builtin_amdgcn_ds_read_tr16_b64_v4i16((LAS s16x4*)p)), b = __builtin_bit_cast(u32x2, __builtin_amdgcn_ds_read_tr16_b64_v4i16((LAS s16x4*)(p + 4 * STRIDE)));
    return (u32x4){a[0], a[1], b[0], b[1]};
}
DEV void block_sums_load(const void* p, unsigned bytes, int lw, int lane, f32x4 (&v)[4]) {
    const __amdgpu_buffer_rsrc_t r = rsrc(p, bytes);
#pragma unroll
    for (int i = 0; i < 4; ++i) v[i] = __builtin_bit_cast(f32x4, __builtin_amdgcn_raw_buffer_load_b128(r, (lane >> 4) * 1024 + (lane & 15) * 16, i * 4096 + lw * 256, 16));
}
DEV f32x4 block_sums(const f32x4 (&v)[4]) { const f32x4 s = (v[0] + v[1]) + (v[2] + v[3]); return (f32x4){fq_sum(s[0]), fq_sum(s[1]), fq_sum(s[2]), fq_sum(s[3])}; }
DEV void row_tables(LAS unsigned char* lds, int lw, int lane, const f32x4 (&v)[4], float sign) {
    const f32x4 t = block_sums(v);
    const float s = (lane & 16) ? t[2] : t[0], q = (lane & 16) ? t[3] : t[1];
    const float mean = s * (1.0f / D_), var = fmaxf(q * (1.0f / D_) - mean * mean, 0.f), r = __builtin_amdgcn_rsqf(var + LN_EPS);
    LAS float* p = (LAS float*)(lds + (lane < 32 ? L_R : L_M)) + lw * 32 + perm32(lane & 31);
    if (sign != 0.f) { p[0] = lane < 32 ? sign * r : -mean * sign * r; return; }
    p[0] = lane < 32 ? (var + LN_EPS) * r : -mean;
    p[(L_K3 - L_R) / 4] = lane < 32 ? GC1 * r * r * r : GC0 * r;
}

constexpr size_t MB = 1 << 20;
constexpr size_t LS = 30 * MB;
constexpr size_t W_T = 0, W_XA = 8 * MB, W_Z2T = 12 * MB, W_H = 19 * MB, W_EX = 80 * MB, W_XB = 60 * MB, W_WINT = 64 * MB, W_WOUTT = 71 * MB;
constexpr size_t W_SP = 75 * MB, W_GRAN = 77 * MB, W_WSPIN = 77 * MB + 65536, W_XCH = 77 * MB + 512 * 1024, W_CTR = 77 * MB + 384 * 1024, W_NONCE = 77 * MB + 448 * 1024;

typedef Cfg<128, 128, 4, D_ / 64, D_, 65536, 0, 0, 0, 1> CG1;
typedef Cfg<128, 128, 4, S_ / 64, S_, 65536, 0, 0, 0, 2> CG2;
typedef Cfg<128, 224, 3, D_ / 64, 0, 0, 73728, 1, 2, 1> CG3;
typedef Cfg<128, 112, 4, S_ / 64, S_, 32768, 98304, 0, 0, 2> CG4;
typedef Cfg<128, 128, 4, FH / 128, 0, 65536, 0, 2, 2, 1> CG5;
constexpr u64 pace4(int a, int b, int c, int d) { return (u64)a | (u64)b << 16 | (u64)c << 32 | (u64)d << 48; }
constexpr u64 PACE_T[7] = {pace4(630, 660, 760, 830), pace4(0, 355, 385, 420), pace4(0, 370, 402, 435), pace4(620, 640, 685, 740), pace4(0, 335, 362, 390), pace4(0, 355, 385, 420), pace4(385, 390, 408, 435)};
constexpr int PACE_N[6] = {8, 11, 11, 8, 11, 9};
constexpr unsigned SLOW_T[6] = {0, 72, 200, 60, 74, 200};
#define PACE(i) PACE_T[i], PACE_N[i]
#define PACE3 l ? PACE_T[3] : PACE_T[0], l ? PACE_N[3] : PACE_N[0]
#define PACE2 l ? PACE_T[2] : PACE_T[6], PACE_N[2]
#define SLOW(i) SLOW_T[i]
#define X1 0x040404
#define X2 0x040404
#define X3 0x000007
#define X5 0x040804
constexpr int xsum(int x, int n) { int s = 0; for (int i = 0; i < n; ++i) s += (x >> i * 8) & 255; return s; }
static_assert(xsum(X1, 3) <= WP1 * 4 && xsum(X2, 3) <= WP2 * 4 && xsum(X3, 3) <= WP3 * 7 && xsum(X5, 3) <= WP5 * 4 && xsum(X5, 1) <= 4 && xsum(X1, 2) <= 8 && xsum(X1, 3) <= 12, "");
static_assert(DF == 3 && xsum(X1, 1) <= 4 && xsum(X2, 1) <= 4 && xsum(X2, 2) <= 8 && xsum(X2, 3) <= 12 && xsum(X3, 2) <= 7, "");
constexpr int XP = 128 * D_ * 2;

struct Piece { __amdgpu_buffer_rsrc_t r; int v, sm, sh; };
DEV Piece piece2(const char* panel, bool row, int r0, int ch, int lane) {
    return Piece{rsrc(early(panel), XP), opaque_v((row ? perm32(lane & 15) * D_ * 2 + (lane >> 4) * 16 : lane * 16) + (row ? r0 * D_ * 2 + ch * 64 : (ch >> 1) * 16384 + (ch & 1) * 1024 + r0 * 128)),
                 opaque_s(row ? 64 * D_ : 4096), opaque_s(row ? 8 * D_ : 2048)};
}
DEV void res_load(const Piece& p, u32x4 (&xr)[4]) {
#pragma unroll
    for (int i = 0; i < 4; ++i) xr[i] = __builtin_amdgcn_raw_buffer_load_b128(p.r, p.v, (i >> 1) * p.sm + (i & 1) * p.sh, 16);
}
DEV void add8(f32x4& a, f32x4& b, u32x4 x) { a += unpk4(x[0], x[1]); b += unpk4(x[2], x[3]); }
typedef __bf16 bf8 __attribute__((ext_vector_type(8)));
DEV f32x4 row_sums(u32x4 o, f32x4 c) { return __builtin_amdgcn_mfma_f32_16x16x32_bf16(__builtin_bit_cast(bf8, (u32x4){0x3f803f80u, 0x3f803f80u, 0x3f803f80u, 0x3f803f80u}), __builtin_bit_cast(bf8, o), c, 0, 0, 0); }
DEV void res_sums(const f32x4 (&st)[4], __amdgpu_buffer_rsrc_t r, int lane, int off = 0) {
    auto diag = [&](const f32x4& q) { return (lane & 2) ? ((lane & 1) ? q[3] : q[2]) : ((lane & 1) ? q[1] : q[0]); };
    st16(__builtin_bit_cast(u32x4, (f32x4){st[0][0], diag(st[1]), st[2][0], diag(st[3])}), r, ((lane >> 4) == ((lane >> 2) & 3) ? (lane & 15) * 16 : 512) + off);
}

DEV void half_send(const f32x4 (&acc)[4], __amdgpu_buffer_rsrc_t r, int off) {
    st16(__builtin_elementwise_min(pk8(acc[2], acc[3]), (u32x4){XCH_EMPTY - 1, XCH_EMPTY - 1, XCH_EMPTY - 1, XCH_EMPTY - 1}), r, off);
}
DEV void half_start(f32x4 (&acc)[4][4], const u32x4 (&xr)[4]) {
#pragma unroll
    for (int i = 0; i < 4; ++i) { acc[i][0] = unpk4(xr[i][0], xr[i][1]); acc[i][1] = unpk4(xr[i][2], xr[i][3]); asm volatile("" : "+v"(acc[i][0]), "+v"(acc[i][1])); }
    asm volatile("s_nop 1");
}
DEV void half_take(f32x4 (&acc)[4][4], u32x4 (&xr)[4], __amdgpu_buffer_rsrc_t rI, const Piece& po, int lane) {
    u32x4 g[4];
    auto poll = [&]() {
#pragma unroll
        for (int i = 0; i < 4; ++i) g[i] = __builtin_amdgcn_raw_buffer_load_b128(rI, lane * 16 + i * 1024, 0, 16);
    };
    auto full = [&]() {
        unsigned mx = 0;
#pragma unroll
        for (int i = 0; i < 4; ++i) mx = max(max(mx, max(g[i][0], g[i][1])), max(g[i][2], g[i][3]));
        return __all(mx != XCH_EMPTY);
    };
#pragma unroll 1
    for (int spin = 0; spin < SPIN_MAX; ++spin) { poll(); if (full()) break; __builtin_amdgcn_s_sleep(1); asm volatile("" ::: "memory"); }
#pragma unroll
    for (int m = 0; m < 4; ++m) {
        add8(acc[m][0], acc[m][1], g[m]);
        xr[m] = pk8(acc[m][0], acc[m][1]);
        st16(xr[m], po.r, po.v + ((m >> 1) * po.sm + (m & 1) * po.sh));
        SB0();
    }
}
DEV void half_sums(const u32x4 (&xr)[4], __amdgpu_buffer_rsrc_t rS, LAS unsigned char* lds, int own, int lane) {
    f32x4 st[2][4];
#pragma unroll
    for (int m = 0; m < 4; ++m) {
        st[m >> 1][(m & 1) * 2] = row_sums(xr[m], (f32x4){0.f, 0.f, 0.f, 0.f});
        st[m >> 1][(m & 1) * 2 + 1] = __builtin_amdgcn_mfma_f32_16x16x32_bf16(__builtin_bit_cast(bf8, xr[m]), __builtin_bit_cast(bf8, xr[m]), (f32x4){0.f, 0.f, 0.f, 0.f}, 0, 0, 0);
    }
    res_sums(st[0], rS, lane); res_sums(st[1], rS, lane, 256);
    SB0();
#pragma unroll
    for (int m = 0; m < 4; ++m) *(LAS u32x4*)(lds + own + lane * 16 + m * 2048) = xr[m];
    own_add(lds);
}

DEV void win_block(LAS unsigned char* lds, const u16* win, int tm, int tn, int w, int lane) {
    const __amdgpu_buffer_rsrc_t r = rsrc(win + (size_t)tm * 64 * F_, 64 * F_ * 2);
#pragma unroll
    for (int j = 0; j < 7; ++j) {
        const int q = j * 4 + w, cb = q * 64 + lane, row = cb / 28, ch = cb - row * 28;
        __builtin_amdgcn_raw_ptr_buffer_load_lds(r, (LAS void*)(lds + L_TIN + q * 1024), 16, row * F_ * 2 + (ch < 14 ? ch * 16 : FH * 2 - 224 + ch * 16), tn * 224, 0, 2);
    }
}

struct Args { const u16 *x0, *wsre, *wsim, *wdre, *wdim, *win, *wsp, *bsp, *wout; char* ws; u16* out; };
typedef const __attribute__((address_space(4))) Args* ArgPtr;
DEV ArgPtr opaque_a(ArgPtr p) { asm volatile("" : "+s"(p)); return p; }

__global__ __launch_bounds__(512, 2) void k_main(Args) {
    extern __shared__ __attribute__((aligned(1024))) unsigned char shm[];
    LAS unsigned char* lds = (LAS unsigned char*)shm;
    const int tid = threadIdx.x, lane0 = tid & 63, w0 = __builtin_amdgcn_readfirstlane(tid >> 6);
    ArgPtr ka = (ArgPtr)__builtin_amdgcn_kernarg_segment_ptr();
    u64 px, pd; unsigned k1;
    {
        u64 pc; unsigned len, off;
        asm volatile("s_load_dwordx2 %2, %6, 0x0\n\ts_load_dword %4, %6, 0x54\n\ts_bitcmp1_b32 %8, 0\n\ts_cselect_b32 m0, 32, 24\n\ts_load_dwordx2 %3, %6, m0\n\t"
                     "s_getpc_b64 %0\n.Lk_main_pc:\n\ts_lshr_b32 %1, %8, 3\n\tv_lshlrev_b32 %5, 2, %9\n\tv_lshl_add_u32 %5, %1, 11, %5\n\t"
                     "s_max_i32 %1, .Lk_main_e0-.Lk_main_pc, 0\n\ts_max_i32 %1, .Lk_main_e1-.Lk_main_pc, %1\n\ts_mov_b32 m0, %7\n\tv_min_u32 %5, %1, %5\n\t"
                     "global_load_lds_dword %5, %0"
                     : "=&s"(pc), "=&s"(len), "=&s"(px), "=&s"(pd), "=&s"(k1), "=&v"(off) : "s"(ka), "n"(L_JUNK), "s"(blockIdx.x), "v"(tid) : "scc", "memory");
        SB0();
    }
    const int tm0 = ((blockIdx.x & 7) >> 1) * 4 + (blockIdx.x >> 6), tn0 = (blockIdx.x & 1) * 8 + ((blockIdx.x >> 3) & 7);
    {
        static_assert(CG1::PBASE == 65536 && CG1::QBASE == 0 && CG1::ROTP == 0, "");
        const int v = (((tid >> 5) & 3) * 8 + ((tid >> 7) & 1) * 4 + ((tid >> 3) & 3)) * 2048 + ((tid ^ (tid >> 4)) & 7) * 16;
        int m = w0 * 1024 - 4096, so = 0, t0, t1;
        asm volatile("s_cmp_lt_i32 %0, 0\n\ts_waitcnt lgkmcnt(0)\n\ts_cbranch_scc1 2f\n1:\n\t"
                     "s_add_u32 m0, %0, 0x10000\n\tv_add_u32 %2, %1, %5\n\tglobal_load_lds_dwordx4 %2, %7\n\t"
                     "s_mov_b32 m0, %0\n\tv_add_u32 %2, %1, %6\n\tglobal_load_lds_dwordx4 %2, %8\n\t"
                     "s_addk_i32 %0, 0x1000\n\ts_add_u32 %1, %1, 0x10000\n\ts_bitcmp1_b32 %1, 18\n\ts_cselect_b32 %3, 0xfffc0080, 0\n\ts_add_u32 %1, %1, %3\n\t"
                     "s_bitcmp0_b32 %0, 14\n\ts_cbranch_scc1 1b\n2:"
                     : "+s"(m), "+s"(so), "=&v"(t0), "=&s"(t1), "+s"(ka) : "v"(v + (tn0 & 7) * XP), "v"(v + tm0 * XP), "s"(pd), "s"(px), "s"(k1) : "scc", "memory");
        SB0();
    }
#define PHASE_VARS() const int lane = opaque_v(lane0), w = opaque_s(w0), tm = opaque_s(tm0), tn = opaque_s(tn0), fr = lane & 15, fq = lane >> 4; (void)fr; (void)fq; (void)w; \
    const ArgPtr a = opaque_a(ka); char* const ws = a->ws; char* const wl = ws + l * LS; unsigned* const ctr = (unsigned*)(ws + W_CTR); unsigned* const c = ctr + l * 5 * BP; const int me = tm * 16 + tn; (void)me; float* const sp = (float*)(ws + W_SP + l * 512 * 1024); \
    (void)wl; (void)c; (void)sp
    if (w0 >= 4) {
        const int lw = w0 - 4;
        int level = 0;
#pragma unroll 1
        for (int l = 0; l < 2; ++l) {
            const unsigned first = l == 0 ? ~0u : 0u;
            f32x4 v[4], u[4];
            { PHASE_VARS(); const int lt = (w - 4) * 64 + lane;
            const u16* pw = (tn < 8 ? a->wdre : a->wdim) + (size_t)(tn & 7) * 128 * D_;
            const __amdgpu_buffer_rsrc_t rg = rsrc(early(ws + W_GRAN), 65536 & first);
            const int goff = opaque_v((lt < 128 ? tm * 128 + lt : 1920 + tn * 128 + lt) * 16);
            const float* ps = early(sp + tm * 8192);
            u32x4 g;
            const GAS unsigned* box = box_word(ctr + 4 * BP + me * BT);
            const Ahead nx = ahead<CG2>((tn < 8 ? a->wsre : a->wsim) + (size_t)tm * 128 * S_, C2, LIM2, lw, lane);
            char* const pe = early(ws + W_EX + (me * 4 + lw) * 4096);
            loader_phase<CG1, WP1, xsum(X1, 3) / 4, 3, 4, 1 + (2 << 16), 0, X2 + E2>(lds, pw, (l == 0 ? (const char*)a->x0 : ws + W_XB) + tm * XP, l == 0, tm + tn + (32 & first), LIM1, lw, lane, l * 20, level, PACE(1), [&]() { return dep_wait(box, 15, lds, 5, lw); }, [&](auto ic) {
                constexpr int i = decltype(ic)::value;
                if constexpr (i == -10) {
                    block_sums_load(ps, 16384 & ~first, lw, lane, v);
                } else if constexpr (i == -11) {
                    block_sums_load(ps + 4096, 16384 & ~first, lw, lane, u);
                } else if constexpr (i == -5) {
                    if (l) {
#pragma unroll
                        for (int j = 0; j < 4; ++j) v[j] += u[j];
                        row_tables(lds, lw, lane, v, tn < 8 ? 1.0f : -1.0f);
                    }
                } else if constexpr (i == 0) {
                    g = __builtin_amdgcn_raw_buffer_load_b128(rg, goff, 0, 16);
                } else if constexpr (i >= 4) {
                    weights<CG2>(lds, nx, ~0u, lw, xsum(X2, i - 4) / 4, xsum(X2, i - 3) / 4);
#pragma unroll
                    for (int j = xsum(E2, i - 4); j < xsum(E2, i - 3); ++j) st16((u32x4){XCH_EMPTY, XCH_EMPTY, XCH_EMPTY, XCH_EMPTY}, rsrc(pe, 4096 & first), lane * 16 + j * 1024);
                } else if constexpr (i == 2) {
                    const float sign = tn < 8 ? 1.0f : -1.0f;
                    if (l) return;
                    const u64 tag = launch_tag(lds);
                    auto there = [&]() { return __all(g[1] == (unsigned)tag && g[3] == (unsigned)(tag >> 32)); };
                    if (!there()) {
                        asm volatile("" ::: "memory");
#pragma unroll 1
                        for (int spin = 0; spin < SPIN_MAX; ++spin) {
                            g = __builtin_amdgcn_raw_buffer_load_b128(rg, goff, 0, 16);
                            if (there()) break;
                            __builtin_amdgcn_s_sleep(2);
                        }
                    }
                    const float mean = __uint_as_float(g[0]) * (1.0f / D_), var = fmaxf(__uint_as_float(g[2]) * (1.0f / D_) - mean * mean, 0.f), rs = sign * __builtin_amdgcn_rsqf(var + LN_EPS);
                    if (lt < 128) { ((LAS float*)(lds + L_R))[lt] = rs; ((LAS float*)(lds + L_M))[lt] = -mean * rs; }
                    else ((LAS float*)(lds + L_WD))[lt - 128] = __uint_as_float(g[0]);
                }
            }); }
            { PHASE_VARS();
            unsigned tw;
            const unsigned* tbox = ctr + 10 * BP + me * BT;
            const GAS unsigned* box = box_word(c + me * BT);
            const Ahead nx = ahead<CG3>(ws + W_WINT + tn * 224 * D_ * 2, tm + tn, LIM3, lw, lane);
            loader_phase<CG2, WP2, xsum(X2, 3) / 4, 3, 4, 1, 0, X3>(lds, (tn < 8 ? a->wsre : a->wsim) + (size_t)tm * 128 * S_, wl + W_T + tn * 2 * XP, false, C2, LIM2, lw, lane, l * 20 + 4, level, PACE2,
                              [&]() { return dep_wait(box, 15, lds, l * 5 + 1, lw); },
                              [&](auto ic) {
                                  constexpr int i = decltype(ic)::value;
                                  if constexpr (i == 0) {
                                      tw = __hip_atomic_load((const GAS unsigned*)tbox, __ATOMIC_RELAXED, __HIP_MEMORY_SCOPE_AGENT);
                                  } else if constexpr (i >= 4 && xsum(X3, i - 4) < xsum(X3, i - 3)) {
                                      if constexpr (xsum(X3, i - 4) == 0) { if (!__all(tw == 17)) box_poll<1>(box_word(tbox), 17); }
                                      weights<CG3>(lds, nx, ~0u, lw, xsum(X3, i - 4) / 7, xsum(X3, i - 3) / 7);
                                  }
                              }); }
            { PHASE_VARS(); const int lt = (w - 4) * 64 + lane;
            const float* ps = early(sp + 262144 + tm * 8192);
            f32x4 v2[4];
            const char* pc = early(ws + W_WSPIN + (size_t)tn * 16384);
            const __amdgpu_buffer_rsrc_t rt = rsrc(early(a->wsp + (size_t)(tm * 128 + (tn & 7) * 16 + lw * 4) * S_), 16384), re = rsrc(early(ws + W_EX + (l * 8 + 4) * MB + (me * 4 + lw) * 4096), (4 * MB & first) + 4096);
            const int vt = opaque_v(65536), vs = opaque_v(fq * 4096 + (fr < 2 ? tm * 2 + fr : fr < LIM4 ? fr - 2 : fr) * 128);
            int so = 0;
            const GAS unsigned* box = box_word(c + BP + me * BT);
            loader_phase<CG3, WP3, xsum(X3, 3) / 7, 3, 3, (3 << 16) + 256, 5, 0>(lds, wl + W_XA + tm * XP, ws + W_WINT + tn * 224 * D_ * 2, false, tm + tn, LIM3, lw, lane, l * 20 + 8, level, PACE3,
                              [&]() { return dep_wait(box, 15, lds, l * 5 + 2, lw); },
                              [&](auto ic) {
                                  constexpr int i = decltype(ic)::value;
                                  if constexpr (i == -1) {
                                      if (blockIdx.x == 0 && l == 0 && lt == 0) __hip_atomic_store((GAS u64*)(ws + W_NONCE), launch_tag(lds), __ATOMIC_RELAXED, __HIP_MEMORY_SCOPE_AGENT);
                                  } else if constexpr (i == -2) {
                                      if (so < EXS) __builtin_amdgcn_raw_ptr_buffer_load_lds(rt, (LAS void*)(lds + L_JUNK), 4, vt, 0, 0, 0);
                                      else st16((u32x4){XCH_EMPTY, XCH_EMPTY, XCH_EMPTY, XCH_EMPTY}, re, lane * 16 + ((so - EXS) & 3) * 1024 + ((so - EXS) >> 2) * (int)(4 * MB));
                                      ++so;
                                  } else if constexpr (i <= -10) {
                                      if (i == -10) block_sums_load(ps, 16384, lw, lane, v);
                                      if (i == -11) block_sums_load(ps + 4096, 16384, lw, lane, v2);
                                      if (i == -12) block_sums_load(pc, 16384 & first, lw, lane, u);
                                  } else if constexpr (i == 1) {
                                      const __amdgpu_buffer_rsrc_t r = rsrc(a->wout + (size_t)tm * 112 * D_, (112 * D_ * 2) & first);
#pragma unroll
                                      for (int j = 0; j < 4; ++j)
                                          __builtin_amdgcn_raw_ptr_buffer_load_lds(r, (LAS void*)(lds + L_TOUT + lw * 1024 + j * 4096), 16, (lane >> 1) * D_ * 2 + (lane & 1) * 16, j * 32 * D_ * 2 + tn * 128 + lw * 32, 0, 0);
                                      __builtin_amdgcn_raw_ptr_buffer_load_lds(rt, (LAS void*)(lds + L_JUNK), 4, vs, 0, 0, 0);
                                  } else if constexpr (i == -5) {
#pragma unroll
                                      for (int j = 0; j < 4; ++j) v[j] += v2[j];
                                      row_tables(lds, lw, lane, v, 0.f);
                                      if (l == 0) {
                                          const f32x4 s = block_sums(u);
                                          if (lane < 16) *(LAS f32x4*)(lds + L_WS + lw * 256 + lane * 16) = s;
                                      }
                                  }
                              });
            const Ahead nx = ahead<CG4>(a->wsp + (size_t)tm * 128 * S_, C4, LIM4, lw, opaque_v(lane));
            const int to = (tm & 8) + (tn >> 1);
            weights<CG4>(lds, nx, ~0u, lw, 0, 1); weights<CG4>(lds, nx, ~0u, lw, 2, 4);
            if (l == 0) {
                WAITCNT(13, 15);
                asm volatile("" ::: "memory");
                const __amdgpu_buffer_rsrc_t ro = rsrc(ws + W_WOUTT + to * 64 * FH * 2, 64 * FH * 2);
                const int slot = ((lw >> 1) * 2 + ((tn & 1) ^ (tm >> 3))) * 32 + inv32((lw & 1) * 16 + fr), vo = (slot >> 4) * 2048 + fq * 256 + (slot & 15) * 16;
#pragma unroll
                for (int j = 0; j < 4; ++j) {
                    u32x2 x, y;
                    asm volatile("ds_read_b64_tr_b16 %0, %2\n\tds_read_b64_tr_b16 %1, %2 offset:128\n\ts_waitcnt lgkmcnt(0)"
                                 : "=&v"(x), "=&v"(y) : "v"((unsigned)(size_t)(lds + L_TOUT) + lw * 1024 + j * 4096 + fq * 256 + fr * 8) : "memory");
                    const int un = j < 3 ? (tm & 7) * 3 + j : 24 + ((tm & 7) >> 1);
                    if (j < 3 || fq < 2) st16((u32x4){x[0], x[1], y[0], y[1]}, ro, vo + ((un >> 1) * 16384 + (un & 1) * 1024 + (j < 3 ? 0 : (tm & 1) * 512)));
                }
            }
            SB0();
            weights<CG4>(lds, nx, ~0u, lw, 1, 2);
            SB0();
            if (l == 0) dep_signal<32, 4>(ctr + 11 * BP, lane * 16 + to, 16, -1, lds, lane);
            WAITCNT(0, 15); }
            { PHASE_VARS();
            const GAS unsigned* box = box_word((lane == 0 ? c + 2 * BP : ctr + 11 * BP) + me * BT);
            const Ahead nx = ahead<CG5>(ws + W_WOUTT + tn * 64 * FH * 2, tm + tn, LIM5, lw, lane);
            loader_phase<CG4, WP4, WP4, 3, 4, 0, 0, X5>(lds, a->wsp + (size_t)tm * 128 * S_, wl + W_Z2T + tn * 112 * S_ * 2, false, C4, LIM4, lw, lane, l * 20 + 12, level, PACE(4), [&]() { return dep_wait<2>(box, 15 + lane, lds, l * 5 + 3, lw); },
                              [&](auto ic) {
                                  constexpr int i = decltype(ic)::value;
                                  if constexpr (i >= 4) weights<CG5>(lds, nx, ~0u, lw, xsum(X5, i - 4) / 4, xsum(X5, i - 3) / 4);
                              }); }
            { PHASE_VARS();
            const GAS unsigned* box = box_word(c + 3 * BP + me * BT);
            const Ahead nx = ahead<CG1>((tn < 8 ? a->wdre : a->wdim) + (size_t)(tn & 7) * 128 * D_, tm + tn, LIM3, lw, lane);
            loader_phase<CG5, WP5, xsum(X5, 3) / 4, 3, 4, 0, 0, X1>(lds, wl + W_H + (tm * 2 + (tn >> 3)) * 128 * FH, ws + W_WOUTT + tn * 64 * FH * 2, false, tm + tn, LIM5, lw, lane, l * 20 + 16, level, PACE(5),
                              [&]() { return dep_wait(box, 7, lds, l * 5 + 4, lw); }, [&](auto ic) {
                                  constexpr int i = decltype(ic)::value;
                                  if constexpr (i >= 4) weights<CG1>(lds, nx, first, lw, xsum(X1, i - 4) / 4, xsum(X1, i - 3) / 4);
                              }); }
        }
        asm volatile(".Lk_main_e0:");
        return;
    }
    {
        const int l = 0;
        PHASE_VARS();
        u64 z = __hip_atomic_load((const GAS u64*)(ws + W_NONCE), __ATOMIC_RELAXED, __HIP_MEMORY_SCOPE_AGENT);
        st16((u32x4){0u, 0u, 0u, 0u}, rsrc(ctr + me * BT, w == 0 ? 128 : 0), lane * 16);
        SB0();
        const int xr = tm * 128 + tn * 8 + w * 2, wr = (tn & 7) * 128 + tm * 8 + w * 2;
        const __amdgpu_buffer_rsrc_t rx = rsrc(a->x0 + (size_t)xr * D_, 4096), rw = rsrc((tn < 8 ? a->wdre : a->wdim) + (size_t)wr * D_, 4096);
        u32x4 xa[4], b[4];
#pragma unroll
        for (int i = 0; i < 4; ++i) { xa[i] = __builtin_amdgcn_raw_buffer_load_b128(rx, i * 1024 + lane * 16, 0, 0); b[i] = __builtin_amdgcn_raw_buffer_load_b128(rw, i * 1024 + lane * 16, 0, 0); }
        win_block(lds, a->win, tm, tn, w, lane);
        const int tj = min(fr, VN - 1);
        touch(lds, (tn < 8 ? a->wsre : a->wsim) + (size_t)(tm * 128 + (tn & 7) * 16 + w * 4) * S_, 16384, fq * 4096 + (tj < 2 ? tm * 2 + tj : tj < LIM2 ? tj - 2 : tj) * 128);
        if (tid < 128) st16((u32x4){XCH_EMPTY, XCH_EMPTY, XCH_EMPTY, XCH_EMPTY}, rsrc(ws + W_XCH + blockIdx.x * 2048, 2048), tid * 16);
        if (tid < 12) *(LAS unsigned*)(lds + L_SYNC + tid * 4) = 0u;
        asm volatile("" : "+v"(z));
        z += 0x9E3779B97F4A7C15ull; z = (z ^ (z >> 30)) * 0xBF58476D1CE4E5B9ull; z = (z ^ (z >> 27)) * 0x94D049BB133111EBull; z ^= z >> 31;
        if (tid == 0) *(LAS u32x4*)(lds + L_TAG) = (u32x4){(unsigned)z, (unsigned)(z >> 32), 1u, 0u};
        float g0 = 0.f, g1 = 0.f;
#pragma unroll
        for (int r = 0; r < 2; ++r) {
            f32x4 sx = (f32x4){0.f, 0.f, 0.f, 0.f}, qx = sx, sw = sx;
#pragma unroll
            for (int i = 0; i < 2; ++i) {
                const f32x4 a0 = unpk4(xa[2 * r + i][0], xa[2 * r + i][1]), a1 = unpk4(xa[2 * r + i][2], xa[2 * r + i][3]);
                sx += a0 + a1; qx += a0 * a0 + a1 * a1;
                sw += unpk4(b[2 * r + i][0], b[2 * r + i][1]) + unpk4(b[2 * r + i][2], b[2 * r + i][3]);
            }
            const float s1 = fq_sum(row16_sum((sx[0] + sx[1]) + (sx[2] + sx[3]))), q1 = fq_sum(row16_sum((qx[0] + qx[1]) + (qx[2] + qx[3]))), s2 = fq_sum(row16_sum((sw[0] + sw[1]) + (sw[2] + sw[3])));
            if (lane == r) { g0 = s1; g1 = q1; }
            if (lane == 2 + r) g0 = s2;
        }
        if (lane < 4) st16((u32x4){__float_as_uint(g0), (unsigned)z, __float_as_uint(g1), (unsigned)(z >> 32)}, rsrc(ws + W_GRAN, 65536),
                                                             (lane < 2 ? xr + lane : 2046 + (tn >> 3) * 1024 + wr + lane) * 16);
    }
    WAITCNT(63, 0);
    const LAS float* R = (const LAS float*)(lds + L_R); const LAS float* WS = (const LAS float*)(lds + L_WS);
#pragma unroll 1
    for (int l = 0; l < 2; ++l) {
        {
            PHASE_VARS();
            const int wm = w >> 1, wn = w & 1;
            f32x4 acc[4][4];
            const __amdgpu_buffer_rsrc_t rC = rsrc(early(wl + W_T + tn * 2 * XP + (tm * 2 + wn) * 16384 + (wm ^ (tn >> 3)) * 4096), 12288);
            u32x4 tt[8];
            auto own = [&]() {
#pragma unroll
                for (int j = 0; j < 8; ++j) *(LAS u32x4*)(lds + CG2::QBASE + wn * CG2::QB + (wm ^ (tn >> 3)) * 4096 + lane * 16 + (j >> 2) * 8192 + (j & 3) * 1024) = tt[j];
                own_add(lds);
            };
            const LAS float *pr = early_v(R + wn * 64 + fq * 8), *pw = early_v((const LAS float*)(lds + L_WD) + wm * 64 + perm32(fr));
            f32x4 rr[4], mm[4];
            float wsum[4];
            compute_phase<CG1, 4, 4, 4, false, 2, 3, true, 0>(lds, OWNC(1), wm * 64, wn * 64, l == 0, lane, acc, [&](int t) {
                if (t == 2) WAITCNT(0, 15);
                if (t == 3) {
#pragma unroll
                    for (int i = 0; i < 4; ++i) {
                        const int o = (i >> 1) * 32 + (i & 1) * 4;
                        rr[i] = *(const LAS f32x4*)(pr + o); mm[i] = *(const LAS f32x4*)(pr + (L_M - L_R) / 4 + o); wsum[i] = pw[o];
                    }
                }
            }, [&](auto ic) {
                constexpr int m = decltype(ic)::value;
#pragma unroll
                for (int q = 0; q < 2; ++q) {
                    tt[m * 2 + q] = pk8(acc[m][2 * q] * rr[2 * q] + mm[2 * q] * wsum[m], acc[m][2 * q + 1] * rr[2 * q + 1] + mm[2 * q + 1] * wsum[m]);
                    st16(tt[m * 2 + q], rC, lane * 16 + (m >> 1) * 8192 + (m & 1) * 2048 + q * 1024);
                }
            });
            u32x4 tr[8];
            if (l == 0) {
#pragma unroll
                for (int j = 0; j < 4; ++j) {
                    const int g = min(j * 4 + w, 13), half = g >= 7, gg = g - half * 7;
                    LAS unsigned char* p = lds + L_TIN + (fq * 8 + (fr >> 2)) * 448 + (half * 224 + (gg < 6 ? (gg >> 1) * 64 + (gg & 1) * 8 : 192)) + ((fr & 3) << (gg < 6 ? 4 : 3));
                    tr[2 * j] = read_tr<448>(p); tr[2 * j + 1] = read_tr<448>(p + 32 * 448);
                }
#pragma unroll
                for (int j = 0; j < 4; ++j) ((LAS float*)(lds + L_WS))[(j * 4 + w) * 16 + fr] = row_sums(tr[2 * j + 1], row_sums(tr[2 * j], (f32x4){0.f, 0.f, 0.f, 0.f}))[0];
            } else {
#pragma unroll
                for (int j = 0; j < 8; ++j) asm volatile("" : "=v"(tr[j]));
            }
            dep_signal(c, lane * 16 + tn, 16, me, lds, lane, SLOW(1));
            own();
            if (l == 0) {
                const __amdgpu_buffer_rsrc_t ri = rsrc(ws + W_WINT + (tn * 16 + tm) * 224 * 128, 224 * 128);
#pragma unroll
                for (int j = 0; j < 8; ++j) st16(tr[j], ri, lane * 16 + w * 2048 + (j >> 1) * 8192 + (j & 1) * 1024);
            }
        }
        {
            PHASE_VARS();
            const int wm = w >> 1, wn = w & 1, ch = (tn & 7) * 4 + (tn >> 3) * 2 + wn;
            f32x4 acc[4][4];
            u32x4 xr[4];
            const char* ex = ws + W_EX + l * 8 * MB + w * 4096;
            const __amdgpu_buffer_rsrc_t rO = rsrc(early(ex + me * 16384), 4096), rI = rsrc(early(ex + (me ^ 8) * 16384), 4096);
            res_load(piece2((l ? ws + W_XB : (const char*)a->x0) + tm * XP, l == 0, wm * 64, ch, lane), xr);
            const Piece po{rsrc(early(wl + W_XA + tm * XP + (ch >> 1) * 16384 + wm * 8192 + wn * 1024), 7168), lane * 16, 4096, 2048};
            const __amdgpu_buffer_rsrc_t rS = rsrc(early(sp + 262144 + (tm * 32 + ch) * 256 + wm * 128), 512);
            compute_phase<CG2, 4, 4, 4, false, 2, 3, false, 12, true, true>(lds, OWNC(2), wm * 64, wn * 64, false, lane, acc, [&](int t) {
                if (t == 0) half_start(acc, xr);
                if (l) return;
                if (t == 0 && w == 3 && lane < 56) st16(*(const LAS u32x4*)(lds + L_WS + lane * 16), rsrc(ws + W_WSPIN + (size_t)(tn * 16 + tm) * 1024, 1024), lane * 16);
                if (t == 1) {
                    dep_signal<32>(ctr + 10 * BP, lane < 16 ? lane * 16 + tn : me ^ 8, 17, -1, lds, lane);
                }
            }, [&](auto ic) { half_send(acc[decltype(ic)::value], rO, lane * 16 + decltype(ic)::value * 1024); });
            const unsigned x0 = (unsigned)__builtin_amdgcn_s_memrealtime();
            half_take(acc, xr, rI, po, lane);
            half_sums(xr, rS, lds, CG3::PBASE + CG3::ROTP * CG3::PB + wm * 8192 + wn * 1024, lane);
            dep_signal(c + BP, tm * 16 + lane, 16, me, lds, lane, SLOW(2), &x0);
        }
        unsigned bb[2];
        u32x4 zz[7];
        {
            PHASE_VARS();
            f32x4 acc[2][14];
            const __amdgpu_buffer_rsrc_t rX = rsrc(early(ws + W_XCH + l * 262144 + (size_t)(tm * 4 + w) * 4096), 4096);
            const __amdgpu_buffer_rsrc_t rC = rsrc(early(wl + W_Z2T + (tn * 32 + tm * 2 + (w >> 1)) * 14336), 14336);
            const u16* pb = early(a->bsp + tm * 128 + w * 32);
            const LAS f32x4* pt; const LAS float* pc;
            f32x4 tb[8]; float cs[7];
            u32x4 g[4];
            auto poll = [&]() {
#pragma unroll
                for (int i = 0; i < 4; ++i) g[i] = __builtin_amdgcn_raw_buffer_load_b128(rX, lane * 16 + i * 1024, 0, 16);
            };
            compute_phase<CG3, 2, 14, 7, true, 1, 1, false, 0>(lds, OWNC(3), w * 32, 0, false, lane, acc, [&](int j) {
                if (j == 11) { pt = early_v((const LAS f32x4*)(R + w * 32 + fq * 8)); pc = early_v(WS + 112 + fr); }
                if (j >= 12 && j < 19) cs[j - 12] = pc[(j - 12) * 16];
                if (j >= 20 && j < 28) tb[j - 20] = pt[((j - 20) & 3) * 32 + ((j - 20) >> 2)];
                if (j != 4) return;
                f32x4 s[2], q[2];
#pragma unroll
                for (int m = 0; m < 2; ++m) {
#pragma unroll
                    for (int n = 7; n < 14; ++n) {
                        const f32x4 z = gelu_tanh(acc[m][n] + tb[4 * m + 1] * cs[n - 7], tb[4 * m + 2], tb[4 * m + 3], tb[4 * m]);
                        acc[m][n] = z;
                        if (n == 7) { s[m] = z; q[m] = z * z; } else { s[m] += z; q[m] += z * z; }
                        if (m == 0 && n == 8) asm volatile("" : "+v"(acc[0][7]), "+v"(acc[0][12]), "+v"(acc[1][12]), "+v"(acc[0][13]), "+v"(acc[1][13]));
                    }
                }
                float v8[8], v4[4], v2[2];
#pragma unroll
                for (int i = 0; i < 8; ++i) {
                    const float lo = (i & 1) ? q[0][i >> 1] : s[0][i >> 1], hi = (i & 1) ? q[1][i >> 1] : s[1][i >> 1];
                    v8[i] = ((fr & 8) ? hi : lo) + dpp_mov<0x128>((fr & 8) ? lo : hi);
                }
#pragma unroll
                for (int i = 0; i < 4; ++i) v4[i] = ((fr & 4) ? v8[i + 4] : v8[i]) + dpp_mov<0x141>(dpp_mov<0x1b>((fr & 4) ? v8[i] : v8[i + 4]));
#pragma unroll
                for (int i = 0; i < 2; ++i) v2[i] = ((fr & 2) ? v4[i + 2] : v4[i]) + dpp_mov<0x4e>((fr & 2) ? v4[i] : v4[i + 2]);
                const float pv = ((fr & 1) ? v2[1] : v2[0]) + dpp_mov<0xb1>((fr & 1) ? v2[0] : v2[1]);
                __builtin_amdgcn_raw_buffer_store_b32(xch_word(pv), rX, tn * 256 + fq * 64 + (fr & 6) * 8 + (fr & 8) + (fr & 1) * 4, 0, 16);
                SB0();
            }, [](int) {});
            own_add(lds, 12);
            auto z1 = [&](int n0, int n1) {
                const LAS float* p = R + w * 32 + perm32(fr);
                const float sd[2] = {p[0], p[4]}, nm[2] = {p[128], p[132]}, k3[2] = {p[256], p[260]}, k1[2] = {p[384], p[388]};
#pragma unroll
                for (int n = 0; n < 7; ++n) {
                    if (n < n0 || n >= n1) continue;
                    const f32x4 wv = *(const LAS f32x4*)(WS + n * 16 + fq * 4);
                    zz[n] = pk8(gelu_tanh(acc[0][n] + wv * nm[0], k3[0], k1[0], sd[0]), gelu_tanh(acc[1][n] + wv * nm[1], k3[1], k1[1], sd[1]));
                    asm volatile("" : "+v"(zz[n]));
                }
                SB0();
            };
            auto full = [&]() {
                unsigned mx = 0;
#pragma unroll
                for (int i = 0; i < 4; ++i) mx = max(max(mx, max(g[i][0], g[i][1])), max(g[i][2], g[i][3]));
                return __all(mx != XCH_EMPTY);
            };
            auto bias = [&]() {
#pragma unroll
                for (int m = 0; m < 2; ++m) bb[m] = pb[perm32(m * 16 + fr)];
                SB0();
            };
            SB0();
            poll(); SB0();
            z1(0, Z1A);
            if (!full()) {
                asm volatile("" ::: "memory");
#pragma unroll 1
                for (int spin = 0; spin < SPIN_MAX; ++spin) { poll(); if (full()) break; __builtin_amdgcn_s_sleep(2); }
            }
            const f32x4 t = (__builtin_bit_cast(f32x4, g[0]) + __builtin_bit_cast(f32x4, g[1])) + (__builtin_bit_cast(f32x4, g[2]) + __builtin_bit_cast(f32x4, g[3]));
            f32x4 e[4], ra, rb;
#pragma unroll
            for (int i = 0; i < 4; ++i) e[i] = __builtin_amdgcn_mfma_f32_16x16x4f32(t[i], 1.0f / FH, (f32x4){0.f, 0.f, 0.f, 0.f}, 0, 0, 0);
#pragma unroll
            for (int i = 0; i < 4; ++i) {
                ra[i] = __builtin_amdgcn_rsqf(fmaxf(e[1][i] - e[0][i] * e[0][i], 0.f) + LN_EPS);
                rb[i] = __builtin_amdgcn_rsqf(fmaxf(e[3][i] - e[2][i] * e[2][i], 0.f) + LN_EPS);
            }
            const f32x4 ma = -e[0] * ra, mb = -e[2] * rb;
            bias();
            u32x4 z2[7];
#pragma unroll
            for (int n = 0; n < 7; ++n) {
                z2[n] = pk8(acc[0][7 + n] * ra + ma, acc[1][7 + n] * rb + mb);
                st16(z2[n], rC, lane * 16 + (w & 1) * 1024 + n * 2048);
            }
            SB0();
            {
                lds_until(lds, 12, l * 4 + 4);
#pragma unroll
                for (int n = 0; n < 7; ++n) *(LAS u32x4*)(lds + CG4::QBASE + (w >> 1) * CG4::QB + (w & 1) * 1024 + lane * 16 + n * 2048) = z2[n];
                SB0();
            }
            z1(Z1A, Z1B);
            own_add(lds);
            dep_signal(c + 2 * BP, lane * 16 + tn, 16, me, lds, lane, SLOW(3));
            z1(Z1B, 7);
        }
        {
            PHASE_VARS();
            const float b[2] = {bf_lo(bb[0]), bf_lo(bb[1])};
            f32x4 acc[2][7];
#pragma unroll
            for (int i = 0; i < 14; ++i) acc[i & 1][i >> 1] = (f32x4){b[i & 1], b[i & 1], b[i & 1], b[i & 1]};
            const __amdgpu_buffer_rsrc_t rC = rsrc(early(wl + W_H + (tm * 2 + (tn >> 3)) * 128 * FH + w * 4096), opaque_s(128 * FH - w * 4096));
            int so[3];
#pragma unroll
            for (int q = 0; q < 3; ++q) so[q] = opaque_s((((tn & 7) * 3 + q) >> 1) * 16384 + ((tn + q) & 1) * 1024);
            const int s6 = opaque_s((12 + ((tn >> 2) & 1)) * 16384 + (tn & 3) * 512);
            u32x2 tl[2];
            u32x4 hh[2][3];
            compute_phase<CG4, 2, 7, 7, false, 4, 5, false, 0, false>(lds, OWNC(4), w * 32, 0, false, lane, acc, [](int) {}, [&](auto ic) {
                constexpr int m = decltype(ic)::value;
#pragma unroll
                for (int q = 0; q < 3; ++q) {
                    hh[m][q] = pk8(unpk4(zz[2 * q][m * 2], zz[2 * q][m * 2 + 1]) * acc[m][2 * q], unpk4(zz[2 * q + 1][m * 2], zz[2 * q + 1][m * 2 + 1]) * acc[m][2 * q + 1]);
                    st16(hh[m][q], rC, lane * 16 + so[q] + m * 2048);
                }
                const f32x4 v = unpk4(zz[6][m * 2], zz[6][m * 2 + 1]) * acc[m][6];
                tl[m] = (u32x2){pk_bf16(v[0], v[1]), pk_bf16(v[2], v[3])};
            });
            const auto s0 = __builtin_amdgcn_permlane16_swap(tl[0][0], tl[1][0], false, false), s1 = __builtin_amdgcn_permlane16_swap(tl[0][1], tl[1][1], false, false);
            st16((u32x4){s0[0], s1[0], s0[1], s1[1]}, rC, (fq & 1) * 2048 + (fq >> 1) * 256 + fr * 16 + s6);
            auto own = [&]() {
                SB0();
                LAS u32x4* o = (LAS u32x4*)(lds + CG5::PBASE + CG5::ROTP * CG5::PB + w * 4096 + lane * 16);
                if (tn & 1) { o[0] = hh[0][1]; o[64] = hh[0][2]; o[128] = hh[1][1]; o[192] = hh[1][2]; }
                else { o[0] = hh[0][0]; o[64] = hh[0][1]; o[128] = hh[1][0]; o[192] = hh[1][1]; }
                own_add(lds);
            };
            own();
            dep_signal(c + 3 * BP, (me & ~7) + lane, 8, me, lds, lane, SLOW(4));
        }
        {
            PHASE_VARS();
            const int wm = w >> 1, wn = w & 1, ch = (tn & 7) * 4 + (tn >> 3) * 2 + wn;
            f32x4 acc[4][4];
            u32x4 xr[4];
            const char* ex = ws + W_EX + (l * 8 + 4) * MB + w * 4096;
            const __amdgpu_buffer_rsrc_t rO = rsrc(early(ex + me * 16384), 4096), rI = rsrc(early(ex + (me ^ 8) * 16384), 4096);
            res_load(Piece{rsrc(wl + W_XA + tm * XP + (ch >> 1) * 16384 + wm * 8192 + wn * 1024, 7168), lane * 16, 4096, 2048}, xr);
            const Piece po = piece2((l ? (char*)a->out : ws + W_XB) + tm * XP, l == 1, wm * 64, ch, lane);
            const __amdgpu_buffer_rsrc_t rS = rsrc(early(sp + 131072 + (tm * 32 + ch) * 256 + wm * 128), 512);
            compute_phase<CG5, 4, 4, 4, false, 2, 3, false, 0, true, true>(lds, OWNC(5), wm * 64, wn * 64, false, lane, acc, [&](int t) { if (t == 0) half_start(acc, xr); },
                [&](auto ic) { half_send(acc[decltype(ic)::value], rO, lane * 16 + decltype(ic)::value * 1024); });
            const unsigned x0 = (unsigned)__builtin_amdgcn_s_memrealtime();
            half_take(acc, xr, rI, po, lane);
            if (l) break;
            half_sums(xr, rS, lds, CG1::QBASE + wm * 8192 + wn * 1024, lane);
            dep_signal(c + 4 * BP, tm * 16 + lane, 16, me, lds, lane, SLOW(5), &x0);
        }
    }
    asm volatile(".Lk_main_e1:");
}

extern "C" void kernel_launch(void* const* d_in, const int* in_sizes, int n_in, void* d_out, int out_size, void* d_ws, size_t ws_size, hipStream_t stream) {
    char* ws = (char*)d_ws;
    (void)hipFuncSetAttribute((const void*)k_main, hipFuncAttributeMaxDynamicSharedMemorySize, LDS_TOTAL + 96);
    Args a;
    a.x0 = (const u16*)d_in[0]; a.wsre = (const u16*)d_in[1]; a.wsim = (const u16*)d_in[2]; a.wdre = (const u16*)d_in[3]; a.wdim = (const u16*)d_in[4];
    a.win = (const u16*)d_in[6]; a.wsp = (const u16*)d_in[7]; a.bsp = (const u16*)d_in[8]; a.wout = (const u16*)d_in[9]; a.ws = ws; a.out = (u16*)d_out;
    k_main<<<256, 512, LDS_TOTAL + 96, stream>>>(a);
}
```
